```python
import torch

def _softmax(x, axis=-1):
    return torch.softmax(x, dim=axis)
def _gelu(x):
    return torch.nn.functional.gelu(x, approximate="tanh")
def _silu(x):
    return torch.nn.functional.silu(x)
def _sigmoid(x):
    return torch.sigmoid(x)
def _layernorm(x, axis=-1, eps=1e-5):
    m = x.mean(dim=axis, keepdim=True)
    v = x.var(dim=axis, keepdim=True, unbiased=False)
    return (x - m) / torch.sqrt(v + eps)
def _scatter_add(tgt, idx, src, axis=0):
    return tgt.clone().index_add_(axis, idx, src)
def _segment_sum(src, seg, n):
    out = torch.zeros((n,) + src.shape[1:], dtype=src.dtype)
    return out.index_add_(0, seg, src)
def _pad(x, pw):
    flat = tuple(v for pair in reversed(pw) for v in pair)
    return torch.nn.functional.pad(x, flat)

kernel_name = 'recurrent_scan__sliding_block_cached__glu_gated_bilinear__prompt'

def setup_inputs():

    _w = (lambda a: (lambda t: t.to(torch.bfloat16) if t.is_floating_point() else t)(torch.as_tensor(a)))
    def _A_setup():
        B, Sq, D = 1, 2048, 640
        Hq, Hkv, Sk, Dh = 32, 32, 2048, 192
        assert Hq % Hkv == 0, (Hq, Hkv)
        return {
            "x": _w((lambda *s: torch.randn(*s, dtype=torch.bfloat16) * 0.05)(B, Sq, D)),
            "k_cache": _w((lambda *s: torch.randn(*s, dtype=torch.bfloat16) * 1.6)(B, Hkv, Sk, Dh)),
            "v_cache": _w((lambda *s: torch.randn(*s, dtype=torch.bfloat16) * 1.6)(B, Hkv, Sk, Dh)),
            "wq": _w((lambda *s: torch.randn(*s, dtype=torch.bfloat16) * 1.6)(D, Hq * Dh) * (D ** -0.5)),
            "wo": _w((lambda *s: torch.randn(*s, dtype=torch.bfloat16) * 1.6)(Hq * Dh, D) * ((Hq * Dh) ** -0.5)),
        }
    _A_in = _A_setup()

    def _B_setup():
        B, S, D, F = 1, 2048, 640, 1536
        return {
            "x": _w((lambda *s: torch.randn(*s, dtype=torch.bfloat16) * 0.05)(B, S, D)),
            "w_gate": _w((lambda *s: torch.randn(*s, dtype=torch.bfloat16) * 0.2)(D, F) * (D ** -0.5)),
            "w_up": _w((lambda *s: torch.randn(*s, dtype=torch.bfloat16) * 0.2)(D, F) * (D ** -0.5)),
            "w_down": _w((lambda *s: torch.randn(*s, dtype=torch.bfloat16) * 0.2)(F, D) * (F ** -0.5)),
        }
    _B_in = _B_setup()

    return {
        "A_x": _A_in["x"],
        "A_k_cache": _A_in["k_cache"],
        "A_v_cache": _A_in["v_cache"],
        "A_wq": _A_in["wq"],
        "A_wo": _A_in["wo"],
        "B_x": _B_in["x"],
        "B_w_gate": _B_in["w_gate"],
        "B_w_up": _B_in["w_up"],
        "B_w_down": _B_in["w_down"],
    }

def reference(A_x, A_k_cache, A_v_cache, A_wq, A_wo, B_x, B_w_gate, B_w_up, B_w_down):
    def _A_ref(x, k_cache, v_cache, wq, wo):
        B, Sq, D = x.shape
        Hq, Dh = 32, 192
        Hkv, Sk = k_cache.shape[1], k_cache.shape[2]
        W = min(4096, max(256, Sk // 4))
        rep = Hq // Hkv
        q = (lambda x, *d: x.permute(*d))((x @ wq).reshape(B, Sq, Hq, Dh), 0, 2, 1, 3)
        k = (lambda x, n, axis: torch.repeat_interleave(x, n, dim=axis))(k_cache[:, :, Sk - W:, :], rep, axis=1)
        v = (lambda x, n, axis: torch.repeat_interleave(x, n, dim=axis))(v_cache[:, :, Sk - W:, :], rep, axis=1)
        scores = torch.einsum("bhqd,bhkd->bhqk", q, k) * (Dh ** -0.5)
        p = _softmax(scores, axis=-1)
        o = torch.einsum("bhqk,bhkd->bhqd", p, v)
        return (lambda x, *d: x.permute(*d))(o, 0, 2, 1, 3).reshape(B, Sq, Hq * Dh) @ wo

    def _B_ref(x, w_gate, w_up, w_down):
        g = torch.einsum("bsd,df->bsf", x, w_gate)
        u = torch.einsum("bsd,df->bsf", x, w_up)
        h = g * u
        return torch.einsum("bsf,fd->bsd", h, w_down)

    x = A_x
    xn = _layernorm(x, axis=-1)
    s = _A_ref(xn, A_k_cache, A_v_cache, A_wq, A_wo)
    g = _silu(_B_ref(xn, B_w_gate, B_w_up, B_w_down))
    return x + s * g
```

```cpp
#include <hip/hip_runtime.h>
#include <hip/hip_bf16.h>
typedef unsigned short u16;
__device__ __forceinline__ float bf2f(u16 x){ return __uint_as_float(((unsigned)x)<<16); }
__device__ __forceinline__ u16 f2bf(float f){ __hip_bfloat16 b = __float2bfloat16(f); return *(u16*)&b; }

constexpr int S=2048, D=640, H=32, DH=192, W=512, SK=2048, F=1536;

__global__ void k_ln(const u16* x, u16* xn){
  int row = blockIdx.x; int t = threadIdx.x;
  float v[10]; float s=0;
  for(int i=0;i<10;i++){ v[i]=bf2f(x[row*D+t+64*i]); s+=v[i]; }
  for(int o=32;o>0;o>>=1) s+=__shfl_xor(s,o);
  float m=s/D; float q=0;
  for(int i=0;i<10;i++){ float d=v[i]-m; q+=d*d; }
  for(int o=32;o>0;o>>=1) q+=__shfl_xor(q,o);
  float r=rsqrtf(q/D+1e-5f);
  for(int i=0;i<10;i++) xn[row*D+t+64*i]=f2bf((v[i]-m)*r);
}
template<bool TB, bool OUTF>
__global__ void k_gemm(const u16* A,int lda,long sA,const u16* B,int ldb,long sB,void* C,int ldc,long sC,int K,float alpha){
  __shared__ float As[16][65], Bs[16][65];
  int bx=blockIdx.x, by=blockIdx.y, bz=blockIdx.z;
  A+=bz*sA; B+=bz*sB;
  int t=threadIdx.x; int tx=t&15, ty=t>>4;
  float acc[4][4]={};
  for(int k0=0;k0<K;k0+=16){
    for(int i=t;i<1024;i+=256){ int r=i>>4,c=i&15; As[c][r]=bf2f(A[(long)(by*64+r)*lda+k0+c]); }
    if(TB){ for(int i=t;i<1024;i+=256){ int r=i>>4,c=i&15; Bs[c][r]=bf2f(B[(long)(bx*64+r)*ldb+k0+c]); } }
    else { for(int i=t;i<1024;i+=256){ int r=i>>6,c=i&63; Bs[r][c]=bf2f(B[(long)(k0+r)*ldb+bx*64+c]); } }
    __syncthreads();
    for(int k=0;k<16;k++){
      float a[4],b[4];
      for(int i=0;i<4;i++){a[i]=As[k][ty*4+i]; b[i]=Bs[k][tx*4+i];}
      for(int i=0;i<4;i++)for(int j=0;j<4;j++) acc[i][j]+=a[i]*b[j];
    }
    __syncthreads();
  }
  for(int i=0;i<4;i++)for(int j=0;j<4;j++){
    long idx=bz*sC+(long)(by*64+ty*4+i)*ldc+bx*64+tx*4+j;
    if(OUTF) ((float*)C)[idx]=acc[i][j]*alpha; else ((u16*)C)[idx]=f2bf(acc[i][j]*alpha);
  }
}
__global__ void k_softmax(const float* sc,u16* p){
  long row=blockIdx.x; int t=threadIdx.x;
  float v[8]; float m=-1e30f;
  for(int i=0;i<8;i++){v[i]=sc[row*W+t+64*i]; m=fmaxf(m,v[i]);}
  for(int o=32;o>0;o>>=1) m=fmaxf(m,__shfl_xor(m,o));
  float s=0; for(int i=0;i<8;i++){v[i]=__expf(v[i]-m); s+=v[i];}
  for(int o=32;o>0;o>>=1) s+=__shfl_xor(s,o);
  float r=1.f/s;
  for(int i=0;i<8;i++) p[row*W+t+64*i]=f2bf(v[i]*r);
}
__global__ void k_mul(const float* g,const float* u,u16* h,int n){ int i=blockIdx.x*256+threadIdx.x; if(i<n) h[i]=f2bf(g[i]*u[i]); }
__global__ void k_fin(const u16* x,const float* s,const float* g,u16* o,int n){ int i=blockIdx.x*256+threadIdx.x; if(i<n){ float gg=g[i]; gg=gg/(1.f+__expf(-gg)); o[i]=f2bf(bf2f(x[i])+s[i]*gg);} }

extern "C" void kernel_launch(void* const* d_in, const int* in_sizes, int n_in, void* d_out, int out_size, void* d_ws, size_t ws_size, hipStream_t stream){
  const u16* x=(const u16*)d_in[0]; const u16* kc=(const u16*)d_in[1]; const u16* vc=(const u16*)d_in[2];
  const u16* wq=(const u16*)d_in[3]; const u16* wo=(const u16*)d_in[4];
  const u16* wg=(const u16*)d_in[6]; const u16* wu=(const u16*)d_in[7]; const u16* wd=(const u16*)d_in[8];
  char* ws=(char*)d_ws; size_t off=0;
  u16* xn=(u16*)(ws+off); off+=(size_t)S*D*2;
  u16* q=(u16*)(ws+off); off+=(size_t)S*H*DH*2;
  u16* o=(u16*)(ws+off); off+=(size_t)S*H*DH*2;
  float* sc=(float*)(ws+off); off+=(size_t)H*S*W*4;
  u16* p=(u16*)(ws+off); off+=(size_t)H*S*W*2;
  float* g=(float*)sc; float* u=g+(size_t)S*F; u16* h=(u16*)(u+(size_t)S*F); float* sres=(float*)(h+(size_t)S*F); float* gres=sres+(size_t)S*D;
  k_ln<<<S,64,0,stream>>>(x,xn);
  k_gemm<false,false><<<dim3(H*DH/64,S/64,1),256,0,stream>>>(xn,D,0,wq,H*DH,0,q,H*DH,0,D,1.f);
  k_gemm<true,true><<<dim3(W/64,S/64,H),256,0,stream>>>(q,H*DH,DH,kc+(size_t)(SK-W)*DH,DH,(long)SK*DH,sc,W,(long)S*W,DH,0.07216878364870322f);
  k_softmax<<<H*S,64,0,stream>>>(sc,p);
  k_gemm<false,false><<<dim3(DH/64,S/64,H),256,0,stream>>>(p,W,(long)S*W,vc+(size_t)(SK-W)*DH,DH,(long)SK*DH,o,H*DH,DH,W,1.f);
  k_gemm<false,true><<<dim3(F/64,S/64,1),256,0,stream>>>(xn,D,0,wg,F,0,g,F,0,D,1.f);
  k_gemm<false,true><<<dim3(F/64,S/64,1),256,0,stream>>>(xn,D,0,wu,F,0,u,F,0,D,1.f);
  k_mul<<<S*F/256,256,0,stream>>>(g,u,h,S*F);
  k_gemm<false,true><<<dim3(D/64,S/64,1),256,0,stream>>>(o,H*DH,0,wo,D,0,sres,D,0,H*DH,1.f);
  k_gemm<false,true><<<dim3(D/64,S/64,1),256,0,stream>>>(h,F,0,wd,D,0,gres,D,0,F,1.f);
  k_fin<<<S*D/256,256,0,stream>>>(x,sres,gres,(u16*)d_out,S*D);
}
```

```cpp
#include <hip/hip_runtime.h>
typedef unsigned u32;
typedef unsigned long long u64;
typedef u32 u32x4 __attribute__((ext_vector_type(4)));
#define DI __device__ __forceinline__
#define U(x) ((u32)__builtin_amdgcn_readfirstlane(x))

constexpr u32 WS_FL = 4096, WS_FA = 16384, WS_FB = 32768, WS_XN = 1u << 20, WS_AB = 4u << 20, WS_PART = 36u << 20, WS_USED = 60u << 20;
constexpr u32 ARR = 2621440, OOB = 0x80000000u;

DI u32x4 srd(const void* p, u32 bytes) { u64 a = (u64)p; u32x4 r = {U((u32)a), U((u32)(a >> 32) & 0xffffu), U(bytes), 0x20000u}; return r; }

__global__ __launch_bounds__(256, 1) void fused_block(const char* x, const char* kc, const char* vc, const char* wq, const char* wo,
                                                      const char* wg, const char* wu, const char* wd, char* out, char* ws) {
  __shared__ __attribute__((aligned(16))) char smem[163840];
  u32 tid = threadIdx.x, b = blockIdx.x;
  u32x4 s_text;
  asm volatile("s_getpc_b64 s[96:97]\n.Lfused_text0:\n s_mov_b32 s98, .Lfused_text1-.Lfused_text0\n s_mov_b32 s99, 0x20000\n"
               "buffer_load_dword v235, %1, s[96:99], 0 offen" : "=&{s[96:99]}"(s_text) : "v"((tid & 0xf8) << 4) : "v235", "memory");
  u32x4 x0, x1, x2;
  {
    const u32 lx = (8 * b + (tid >> 5)) * 1280 + 16 * (tid & 31);
    asm volatile("buffer_load_dwordx4 %0, %5, %7, 0 offen\n buffer_load_dwordx4 %1, %5, %7, 0 offen offset:512\n"
                 "buffer_load_dwordx4 %2, %6, %7, 0 offen offset:1024"
                 : "=&{v[0:3]}"(x0), "=&{v[4:7]}"(x1), "=&{v[8:11]}"(x2), "+v"(tid), "+s"(b)
                 : "v"(lx), "v"(lx | ((tid & 16) << 27)), "s"(srd(x, 2621440)) : "memory");
  }
  u32 w = __builtin_amdgcn_readfirstlane(tid >> 6);
  u32x4 s_wq, s_gu;
  u32 twq, tgu;
  {
    const u32 l = tid & 63, l3 = l / 3, lr = l % 3, xcd = b & 7;
    const u32 wqo = (4 * xcd + ((b >> 3) & 3)) * 384 + w * 16 * 12288, guo = (w & 1) * 32 * 3072;
    s_wq = srd(wq + wqo, 7864320 - wqo);
    s_gu = srd((w < 2 ? wg : wu) + guo, 1966080 - guo);
    twq = ((64 * (l3 >> 1) + 2 * (b >> 5) + (l3 & 1)) * 12288 + 128 * lr) | ((11u - l) & OOB);
    tgu = ((64 * l3 + (b >> 3)) * 3072 + 384 * xcd + 128 * lr) | ((5u - l) & OOB);
    asm volatile("s_nop 4\n buffer_load_dword v235, %5, %3, 0 offen\n buffer_load_dword v235, %6, %4, 0 offen"
                 : "+v"(tid), "+s"(b), "+s"(w), "+{s[8:11]}"(s_wq), "+{s[12:15]}"(s_gu), "+{v50}"(twq), "+{v51}"(tgu)
                 :
                 : "v235", "memory");
  }
  u32x4 s_ws = srd(ws, WS_USED);
  u64 epoch, ev;
  {
    const u32 l = tid & 63, lc = l & 31, pc = 8 * w + (l >> 3), base = 73728 + (u32)(size_t)smem;
    asm volatile(
"s_nop 4\n"
"buffer_load_dwordx2 v[48:49], off, s[16:19], 0 sc1\n"
"s_waitcnt vmcnt(5)\n"
"v_lshlrev_b32 v12, 16, v0\n"
"v_and_b32 v13, 0xffff0000, v0\n"
"v_lshlrev_b32 v14, 16, v1\n"
"v_and_b32 v15, 0xffff0000, v1\n"
"v_lshlrev_b32 v16, 16, v2\n"
"v_and_b32 v17, 0xffff0000, v2\n"
"v_lshlrev_b32 v18, 16, v3\n"
"v_and_b32 v19, 0xffff0000, v3\n"
"s_waitcnt vmcnt(4)\n"
"v_lshlrev_b32 v20, 16, v4\n"
"v_and_b32 v21, 0xffff0000, v4\n"
"v_lshlrev_b32 v22, 16, v5\n"
"v_and_b32 v23, 0xffff0000, v5\n"
"v_lshlrev_b32 v24, 16, v6\n"
"v_and_b32 v25, 0xffff0000, v6\n"
"v_lshlrev_b32 v26, 16, v7\n"
"v_and_b32 v27, 0xffff0000, v7\n"
"s_waitcnt vmcnt(3)\n"
"v_lshlrev_b32 v28, 16, v8\n"
"v_and_b32 v29, 0xffff0000, v8\n"
"v_lshlrev_b32 v30, 16, v9\n"
"v_and_b32 v31, 0xffff0000, v9\n"
"v_lshlrev_b32 v32, 16, v10\n"
"v_and_b32 v33, 0xffff0000, v10\n"
"v_lshlrev_b32 v34, 16, v11\n"
"v_and_b32 v35, 0xffff0000, v11\n"
"v_pk_add_f32 v[36:37], v[12:13], v[14:15]\n"
"v_pk_add_f32 v[38:39], v[16:17], v[18:19]\n"
"v_pk_add_f32 v[36:37], v[36:37], v[20:21]\n"
"v_pk_add_f32 v[38:39], v[38:39], v[22:23]\n"
"v_pk_add_f32 v[36:37], v[36:37], v[24:25]\n"
"v_pk_add_f32 v[38:39], v[38:39], v[26:27]\n"
"v_pk_add_f32 v[36:37], v[36:37], v[28:29]\n"
"v_pk_add_f32 v[38:39], v[38:39], v[30:31]\n"
"v_pk_add_f32 v[36:37], v[36:37], v[32:33]\n"
"v_pk_add_f32 v[38:39], v[38:39], v[34:35]\n"
"v_pk_add_f32 v[36:37], v[36:37], v[38:39]\n"
"v_add_f32 v36, v36, v37\n"
"s_nop 1\n"
"v_add_f32_dpp v36, v36, v36 row_ror:1 row_mask:0xf bank_mask:0xf\n"
"s_nop 1\n"
"v_add_f32_dpp v36, v36, v36 row_ror:2 row_mask:0xf bank_mask:0xf\n"
"s_nop 1\n"
"v_add_f32_dpp v36, v36, v36 row_ror:4 row_mask:0xf bank_mask:0xf\n"
"s_nop 1\n"
"v_add_f32_dpp v36, v36, v36 row_ror:8 row_mask:0xf bank_mask:0xf\n"
"v_mov_b32 v46, v36\n"
"s_nop 1\n"
"v_permlane16_swap_b32 v36, v46\n"
"v_add_f32 v36, v36, v46\n"
"v_mul_f32 v37, 0xbacccccd, v36\n"
"v_mul_f32 v36, 0xbacccccd, v36\n"
"v_mul_f32 v38, v36, v44\n"
"v_mul_f32 v39, v36, v44\n"
"v_pk_add_f32 v[12:13], v[12:13], v[36:37]\n"
"v_pk_add_f32 v[14:15], v[14:15], v[36:37]\n"
"v_pk_add_f32 v[16:17], v[16:17], v[36:37]\n"
"v_pk_add_f32 v[18:19], v[18:19], v[36:37]\n"
"v_pk_add_f32 v[20:21], v[20:21], v[36:37]\n"
"v_pk_add_f32 v[22:23], v[22:23], v[36:37]\n"
"v_pk_add_f32 v[24:25], v[24:25], v[36:37]\n"
"v_pk_add_f32 v[26:27], v[26:27], v[36:37]\n"
"v_pk_add_f32 v[28:29], v[28:29], v[38:39]\n"
"v_pk_add_f32 v[30:31], v[30:31], v[38:39]\n"
"v_pk_add_f32 v[32:33], v[32:33], v[38:39]\n"
"v_pk_add_f32 v[34:35], v[34:35], v[38:39]\n"
"v_pk_mul_f32 v[36:37], v[12:13], v[12:13]\n"
"v_pk_mul_f32 v[38:39], v[14:15], v[14:15]\n"
"v_pk_fma_f32 v[36:37], v[16:17], v[16:17], v[36:37]\n"
"v_pk_fma_f32 v[38:39], v[18:19], v[18:19], v[38:39]\n"
"v_pk_fma_f32 v[36:37], v[20:21], v[20:21], v[36:37]\n"
"v_pk_fma_f32 v[38:39], v[22:23], v[22:23], v[38:39]\n"
"v_pk_fma_f32 v[36:37], v[24:25], v[24:25], v[36:37]\n"
"v_pk_fma_f32 v[38:39], v[26:27], v[26:27], v[38:39]\n"
"v_pk_fma_f32 v[36:37], v[28:29], v[28:29], v[36:37]\n"
"v_pk_fma_f32 v[38:39], v[30:31], v[30:31], v[38:39]\n"
"v_pk_fma_f32 v[36:37], v[32:33], v[32:33], v[36:37]\n"
"v_pk_fma_f32 v[38:39], v[34:35], v[34:35], v[38:39]\n"
"v_pk_add_f32 v[36:37], v[36:37], v[38:39]\n"
"v_add_f32 v36, v36, v37\n"
"s_nop 1\n"
"v_add_f32_dpp v36, v36, v36 row_ror:1 row_mask:0xf bank_mask:0xf\n"
"s_nop 1\n"
"v_add_f32_dpp v36, v36, v36 row_ror:2 row_mask:0xf bank_mask:0xf\n"
"s_nop 1\n"
"v_add_f32_dpp v36, v36, v36 row_ror:4 row_mask:0xf bank_mask:0xf\n"
"s_nop 1\n"
"v_add_f32_dpp v36, v36, v36 row_ror:8 row_mask:0xf bank_mask:0xf\n"
"v_mov_b32 v46, v36\n"
"s_nop 1\n"
"v_permlane16_swap_b32 v36, v46\n"
"v_add_f32 v36, v36, v46\n"
"v_mul_f32 v36, 0x3acccccd, v36\n"
"v_add_f32 v36, 0x3727c5ac, v36\n"
"v_rsq_f32 v36, v36\n"
"s_nop 0\n"
"v_mul_f32 v37, 0x3dd53b95, v36\n"
"v_mul_f32 v36, 0x3dd53b95, v36\n"
"v_pk_mul_f32 v[12:13], v[12:13], v[36:37]\n"
"v_pk_mul_f32 v[14:15], v[14:15], v[36:37]\n"
"v_pk_mul_f32 v[16:17], v[16:17], v[36:37]\n"
"v_pk_mul_f32 v[18:19], v[18:19], v[36:37]\n"
"v_cvt_pk_bf16_f32 v0, v12, v13\n"
"v_cvt_pk_bf16_f32 v1, v14, v15\n"
"v_cvt_pk_bf16_f32 v2, v16, v17\n"
"v_cvt_pk_bf16_f32 v3, v18, v19\n"
"ds_write_b128 v40, v[0:3] offset:0\n"
"v_pk_mul_f32 v[20:21], v[20:21], v[36:37]\n"
"v_pk_mul_f32 v[22:23], v[22:23], v[36:37]\n"
"v_pk_mul_f32 v[24:25], v[24:25], v[36:37]\n"
"v_pk_mul_f32 v[26:27], v[26:27], v[36:37]\n"
"v_cvt_pk_bf16_f32 v4, v20, v21\n"
"v_cvt_pk_bf16_f32 v5, v22, v23\n"
"v_cvt_pk_bf16_f32 v6, v24, v25\n"
"v_cvt_pk_bf16_f32 v7, v26, v27\n"
"ds_write_b128 v40, v[4:7] offset:4096\n"
"v_pk_mul_f32 v[28:29], v[28:29], v[36:37]\n"
"v_pk_mul_f32 v[30:31], v[30:31], v[36:37]\n"
"v_pk_mul_f32 v[32:33], v[32:33], v[36:37]\n"
"v_pk_mul_f32 v[34:35], v[34:35], v[36:37]\n"
"v_cvt_pk_bf16_f32 v8, v28, v29\n"
"v_cvt_pk_bf16_f32 v9, v30, v31\n"
"v_cvt_pk_bf16_f32 v10, v32, v33\n"
"v_cvt_pk_bf16_f32 v11, v34, v35\n"
"ds_write_b128 v40, v[8:11] offset:8192\n"
"s_waitcnt lgkmcnt(0)\n"
"s_barrier\n"
"ds_read_b128 v[12:15], v41 offset:0\n"
"ds_read_b128 v[16:19], v41 offset:4096\n"
"ds_read_b128 v[20:23], v41 offset:8192\n"
"s_mov_b32 s86, 32768\n"
"s_waitcnt lgkmcnt(2)\n"
"buffer_store_dwordx4 v[12:15], v42, s[16:19], 0 offen sc1\n"
"s_waitcnt lgkmcnt(1)\n"
"buffer_store_dwordx4 v[16:19], v42, s[16:19], s86 offen sc1\n"
"s_waitcnt lgkmcnt(0)\n"
"buffer_store_dwordx4 v[20:23], v42, s[16:19], s87 offen sc1\n"
"s_nop 1\n"
      : "=&{v[48:49]}"(ev), "+v"(tid), "+s"(b), "+s"(w), "+{s[16:19]}"(s_ws), "+{v[0:3]}"(x0), "+{v[4:7]}"(x1), "+{v[8:11]}"(x2)
      : "{v40}"(base + 128 * lc + 16 * ((2 * w + (l >> 5)) ^ (lc & 7))), "{v41}"(base + 128 * pc + 16 * ((l & 7) ^ (pc & 7))),
        "{v42}"(WS_XN + (b >> 3) * 81920 + (pc >> 2) * 4096 + ((b >> 1) & 3) * 1024 + (8 * (b & 1) + (l & 7) + 16 * (pc & 3)) * 16),
        "{v44}"(lc < 16 ? 1.f : 0.f), "{s87}"(U(65536u | ((1u - w) & OOB)))
      : "v12", "v13", "v14", "v15", "v16", "v17", "v18", "v19", "v20", "v21", "v22", "v23", "v24", "v25", "v26", "v27", "v28", "v29", "v30", "v31",
        "v32", "v33", "v34", "v35", "v36", "v37", "v38", "v39", "v46", "s86", "memory");
  }
  u32 wqd, gud0, gud1, gud2, l0, m0w, m0g;
  {
    const u32 l = tid & 63, rr = l >> 3, h = 4 * (b & 7) + ((b >> 3) & 3);
    u32 gud[3];
#pragma unroll
    for (int t = 0; t < 3; ++t) {
      const u32 P = 64 * t + l, rho = (P * 171) >> 10, cc = P - 6 * rho, kk = (rho & 0x13) | ((rho & 4) << 1) | ((rho & 8) >> 1);
      gud[t] = kk * 3072 + 96 * h + 16 * cc + (h & 1) * (cc < 4 ? 32 : -64) - 1024 * t;
    }
    wqd = ((rr & 3) + 8 * (rr >> 2)) * 12288 + 16 * ((l & 7) ^ (((l >> 4) & 3) << 1));
    gud0 = gud[0], gud1 = gud[1], gud2 = gud[2], l0 = l << 26, m0w = U(2048 * w), m0g = U(24576 + 6144 * (w >> 1) + 3072 * (w & 1));
    asm volatile(
"s_nop 4\n"
"s_waitcnt vmcnt(0)\n"
"v_readfirstlane_b32 s56, v48\n"
"v_readfirstlane_b32 s57, v49\n"
"v_add_u32 v48, 1, v48\n"
"s_barrier\n"
"buffer_store_dwordx2 v[48:49], v234, s[16:19], s58 offen sc1\n"
"s_add_u32 m0, s44, 0\n"
"s_mov_b32 s86, 0\n"
"buffer_load_dwordx4 v236, s[8:11], s86 offen lds\n"
"s_mov_b32 s87, 48128\n"
"buffer_load_dwordx4 v236, s[8:11], s87 offen offset:1024 lds\n"
"s_add_u32 m0, s44, 8192\n"
"s_mov_b32 s88, 128\n"
"buffer_load_dwordx4 v236, s[8:11], s88 offen lds\n"
"s_mov_b32 s89, 48256\n"
"buffer_load_dwordx4 v236, s[8:11], s89 offen offset:1024 lds\n"
"s_add_u32 m0, s44, 16384\n"
"s_mov_b32 s90, 256\n"
"buffer_load_dwordx4 v236, s[8:11], s90 offen lds\n"
"s_mov_b32 s91, 48384\n"
"buffer_load_dwordx4 v236, s[8:11], s91 offen offset:1024 lds\n"
"s_add_u32 m0, s45, 0\n"
"s_mov_b32 s86, 0\n"
"buffer_load_dwordx4 v237, s[12:15], s86 offen lds\n"
"buffer_load_dwordx4 v238, s[12:15], s86 offen offset:1024 lds\n"
"buffer_load_dwordx4 v239, s[12:15], s86 offen offset:2048 lds\n"
"s_mov_b32 s88, 1572864\n"
"s_mov_b32 s89, 393216\n"
"buffer_load_dword v235, v50, s[8:11], s88 offen\n"
"buffer_load_dword v235, v51, s[12:15], s89 offen\n"
"buffer_load_dword v235, v52, s[96:99], 0 offen\n"
      : "+v"(tid), "+s"(b), "+s"(w), "=&{s[56:57]}"(epoch), "+{v[48:49]}"(ev), "+{s[8:11]}"(s_wq), "+{s[12:15]}"(s_gu), "+{s[16:19]}"(s_ws),
        "+{v236}"(wqd), "+{v237}"(gud0), "+{v238}"(gud1), "+{v239}"(gud2), "+{v234}"(l0), "+{s44}"(m0w), "+{s45}"(m0g)
      :  "{s58}"(U((WS_FL + b * 8) | ((0u - w) & OOB))), "{v50}"(twq), "{v51}"(tgu), "{v52}"(128 * ((l & 3) + 4 * ((b >> 3) * 4 + w))), "{s[96:99]}"(s_text)
      : "v235", "s86", "s87", "s88", "s89", "s90", "s91", "s92", "s93", "scc", "memory");
  }
  const u32 xcd = b & 7, hh = (b >> 3) & 3, h = 4 * xcd + hh, qb = b >> 5, l = tid & 63;
  const u32 g = l >> 4, i = l & 15, q = (l >> 2) & 3, pp = l & 3, g0 = g & 1, g1 = g >> 1, rr = l >> 3, l3 = l / 3, lr = l % 3;
  const u32 swz = 16 * ((l & 7) ^ (((l >> 4) & 3) << 1));
  const u32 G = 4 * qb + w, abg = WS_AB + G * 983040;
  const u32 kvo = (h * 2048 + 1536) * 384;
  u32 vrd[4];
#pragma unroll
  for (int t = 0; t < 4; ++t) vrd[t] = 128 * (4 * g + q) + 16 * ((2 * t + (pp >> 1)) ^ (((q >> 1) + 2 * g0) << 1)) + 8 * (pp & 1);
  const u32 wrow = q + 4 * g0 + 16 * g1, wf = ((q >> 1) + 2 * g0) << 1;
  const u32 wqa0 = 128 * wrow + 16 * (pp ^ wf), wqa1 = 128 * wrow + 16 * ((4 + pp) ^ wf), gua = 24576 + 96 * wrow + 8 * pp;
  const u32 kf = ((i >> 1) & 3) << 1, kr0 = 128 * i + 16 * (g ^ kf), kr1 = 128 * i + 16 * ((4 + g) ^ kf);
  const u32 gul = 384 * xcd + 128 * lr;
  const u32 bi = b >> 3, act = bi < 25, bi5 = (bi * 52) >> 8, nt = bi - 5 * bi5, gr = (xcd & 1) & (bi5 >= 3), c = gr ? 4 : xcd >> 1, mt = (gr ? (xcd & 6) + bi5 - 3 : 5 * (xcd & 1) + bi5) & 7;
  const u32 tile = mt * 5 + nt, gb = 4 * mt + w, att = c < 4;
  const u32 rot = 0;
  u32 xo[3], pl[3];
#pragma unroll
  for (int k = 0; k < 3; ++k) {
    const u32 u = 10 * b + w + 4 * k, ut = u >> 6, um = (ut * 52) >> 8, bad = (9u - w - 4 * k) & OOB;
    xo[k] = ((256 * um + ((u >> 2) & 15) * 16) * 1280 + 256 * (ut - 5 * um) + 64 * (u & 3)) | bad;
    pl[k] = (WS_PART + u * 1024) | bad;
  }
  const u32 u0 = (10 * b) >> 4, u1 = (10 * b + 9) >> 4;
  asm volatile(
".macro MA d,dn,a,an,b,bn\n"
"v_mfma_f32_16x16x32_bf16 \\d[\\dn:\\dn+3], \\a[\\an:\\an+3], \\b[\\bn:\\bn+3], \\d[\\dn:\\dn+3]\n"
".endm\n"
".macro MZ d,dn,a,an,b,bn\n"
"v_mfma_f32_16x16x32_bf16 \\d[\\dn:\\dn+3], \\a[\\an:\\an+3], \\b[\\bn:\\bn+3], 0\n"
".endm\n"
".macro TR d,v,o\n"
"ds_read_b64_tr_b16 a[\\d:\\d+1], v\\v offset:\\o\n"
".endm\n"
"s_nop 4\n"
"s_mov_b32 s65, 1048576\n"
"v_lshrrev_b32 v45, 1, v249\n"
"s_cmp_eq_u32 s44, 2048\n"
"s_cbranch_scc0 L_NOPOLL%=\n"
"buffer_load_dwordx2 v[194:195], v45, s[16:19], s59 offen sc1\n"
"s_add_u32 s87, s56, 1\n"
"s_waitcnt vmcnt(0)\n"
"v_subrev_u32 v194, s87, v194\n"
"v_cmp_gt_u32 vcc, 8, v194\n"
"v_cmp_eq_u32 s[94:95], s57, v195\n"
"s_nop 3\n"
"s_and_b32 vcc_lo, vcc_lo, s94\n"
"s_andn2_b32 vcc_lo, -1, vcc_lo\n"
"s_cbranch_scc1 L_SLOW0_%=\n"
"L_BACK0_%=:\n"
"L_NOPOLL%=:\n"
"s_mov_b32 s51, 1\n"
"s_mov_b32 s55, 0x80000000\n"
"s_mov_b32 s0, 0\n"
"s_add_u32 s1, s44, 73728\n"
"s_add_u32 s2, s45, 73728\n"
"s_mov_b32 s3, 1572864\n"
"s_mov_b32 s4, 393216\n"
"s_add_u32 s5, s52, 16384\n"
"s_mov_b32 s6, 0\n"
"v_mov_b32 v228, 0xff800000\n"
"v_mov_b32 v229, 0xff800000\n"
"v_mov_b32 v230, 0xff800000\n"
"v_mov_b32 v231, 0xff800000\n"
"s_barrier\n"
"s_add_u32 s88, s52, 0\n"
"buffer_load_dwordx4 a[96:99], v249, s[16:19], s88 offen sc1\n"
"buffer_load_dwordx4 a[100:103], v249, s[16:19], s88 offen offset:1024 sc1\n"
"buffer_load_dwordx4 a[104:107], v249, s[16:19], s88 offen offset:2048 sc1\n"
"buffer_load_dwordx4 a[108:111], v249, s[16:19], s88 offen offset:3072 sc1\n"
"s_add_u32 m0, s44, 36864\n"
"s_mov_b32 s89, 786432\n"
"buffer_load_dwordx4 v236, s[8:11], s89 offen lds\n"
"s_mov_b32 s90, 834560\n"
"buffer_load_dwordx4 v236, s[8:11], s90 offen offset:1024 lds\n"
"s_add_u32 m0, s44, 45056\n"
"s_mov_b32 s91, 786560\n"
"buffer_load_dwordx4 v236, s[8:11], s91 offen lds\n"
"s_mov_b32 s86, 834688\n"
"buffer_load_dwordx4 v236, s[8:11], s86 offen offset:1024 lds\n"
"s_add_u32 m0, s44, 53248\n"
"s_mov_b32 s87, 786688\n"
"buffer_load_dwordx4 v236, s[8:11], s87 offen lds\n"
"s_mov_b32 s88, 834816\n"
"buffer_load_dwordx4 v236, s[8:11], s88 offen offset:1024 lds\n"
"s_add_u32 m0, s45, 36864\n"
"s_mov_b32 s89, 196608\n"
"buffer_load_dwordx4 v237, s[12:15], s89 offen lds\n"
"buffer_load_dwordx4 v238, s[12:15], s89 offen offset:1024 lds\n"
"buffer_load_dwordx4 v239, s[12:15], s89 offen offset:2048 lds\n"
"s_waitcnt vmcnt(16)\n"
"s_barrier\n"
"TR 192,252,0\n"
"TR 194,252,1024\n"
"TR 196,252,8\n"
"TR 198,252,1032\n"
"s_waitcnt vmcnt(12) lgkmcnt(2)\n"
"MZ v,0,a,192,a,96\n"
"s_waitcnt vmcnt(11)\n"
"s_add_u32 s86, s52, 4096\n"
"MZ v,4,a,192,a,100\n"
"s_waitcnt vmcnt(10)\n"
"TR 200,253,0\n"
"TR 202,253,1024\n"
"MZ v,8,a,192,a,104\n"
"s_waitcnt vmcnt(9)\n"
"TR 204,253,8\n"
"MZ v,12,a,192,a,108\n"
"s_waitcnt lgkmcnt(3)\n"
"buffer_load_dwordx4 a[112:115], v249, s[16:19], s86 offen sc1\n"
"MZ v,28,a,196,a,108\n"
"TR 206,253,1032\n"
"MZ v,24,a,196,a,104\n"
"TR 208,252,8192\n"
"TR 210,252,9216\n"
"MZ v,20,a,196,a,100\n"
"TR 212,252,8200\n"
"TR 214,252,9224\n"
"MZ v,16,a,196,a,96\n"
"s_waitcnt lgkmcnt(6)\n"
"buffer_load_dwordx4 a[116:119], v249, s[16:19], s86 offen offset:1024 sc1\n"
"MZ v,32,a,200,a,96\n"
"TR 216,253,8192\n"
"MZ v,36,a,200,a,100\n"
"TR 218,253,9216\n"
"TR 220,253,8200\n"
"MZ v,40,a,200,a,104\n"
"TR 222,253,9224\n"
"TR 224,252,16384\n"
"MZ v,44,a,200,a,108\n"
"s_waitcnt lgkmcnt(9)\n"
"TR 226,252,17408\n"
"v_cmp_gt_u32 vcc, 5120, v232\n"
"MZ v,60,a,204,a,108\n"
"TR 228,252,16392\n"
"TR 230,252,17416\n"
"MZ v,56,a,204,a,104\n"
"v_mov_b32 v198, s55\n"
"MZ v,52,a,204,a,100\n"
"buffer_load_dwordx4 a[120:123], v249, s[16:19], s86 offen offset:2048 sc1\n"
"MZ v,48,a,204,a,96\n"
"s_waitcnt lgkmcnt(8)\n"
"v_cndmask_b32 v198, v198, v232, vcc\n"
"MZ v,64,a,208,a,96\n"
"TR 232,253,16384\n"
"TR 234,253,17408\n"
"MZ v,68,a,208,a,100\n"
"s_add_u32 s87, s52, 8192\n"
"v_cmp_gt_u32 vcc, 2560, v232\n"
"MZ v,72,a,208,a,104\n"
"MZ v,76,a,208,a,108\n"
"MZ v,92,a,212,a,108\n"
"buffer_load_dwordx4 a[128:131], v249, s[16:19], s87 offen sc1\n"
"MZ v,88,a,212,a,104\n"
"TR 236,253,16392\n"
"MZ v,84,a,212,a,100\n"
"buffer_load_dwordx4 a[124:127], v249, s[16:19], s86 offen offset:3072 sc1\n"
"MZ v,80,a,212,a,96\n"
"s_waitcnt lgkmcnt(3)\n"
"TR 238,253,17416\n"
"MZ v,96,a,216,a,96\n"
"TR 192,255,0\n"
"TR 194,255,768\n"
"MZ v,100,a,216,a,100\n"
"MZ v,104,a,216,a,104\n"
"MZ v,108,a,216,a,108\n"
"buffer_load_dwordx4 a[132:135], v249, s[16:19], s87 offen offset:1024 sc1\n"
"MZ v,124,a,220,a,108\n"
"TR 196,255,6144\n"
"MZ v,120,a,220,a,104\n"
"TR 198,255,6912\n"
"MZ v,116,a,220,a,100\n"
"MZ v,112,a,220,a,96\n"
"MZ v,128,a,224,a,96\n"
"TR 200,255,32\n"
"TR 202,255,800\n"
"MZ v,132,a,224,a,100\n"
"MZ v,136,a,224,a,104\n"
"buffer_load_dwordx4 a[136:139], v249, s[16:19], s87 offen offset:2048 sc1\n"
"MZ v,140,a,224,a,108\n"
"MZ v,156,a,228,a,108\n"
"TR 204,255,6176\n"
"TR 206,255,6944\n"
"MZ v,152,a,228,a,104\n"
"s_add_u32 s88, s52, 12288\n"
"MZ v,148,a,228,a,100\n"
"MZ v,144,a,228,a,96\n"
"s_waitcnt lgkmcnt(4)\n"
"MZ v,160,a,232,a,96\n"
"buffer_load_dwordx4 a[144:147], v249, s[16:19], s88 offen sc1\n"
"MZ v,164,a,232,a,100\n"
"TR 208,255,64\n"
"MZ v,168,a,232,a,104\n"
"buffer_load_dwordx4 a[140:143], v249, s[16:19], s87 offen offset:3072 sc1\n"
"MZ v,172,a,232,a,108\n"
"TR 210,255,832\n"
"MZ v,188,a,236,a,108\n"
"TR 212,255,6208\n"
"TR 214,255,6976\n"
"MZ v,184,a,236,a,104\n"
"MZ v,180,a,236,a,100\n"
"MZ v,176,a,236,a,96\n"
"buffer_load_dwordx4 a[148:151], v249, s[16:19], s88 offen offset:1024 sc1\n"
"MZ a,0,a,192,a,96\n"
"TR 216,252,4096\n"
"MZ a,4,a,192,a,100\n"
"TR 218,252,5120\n"
"MZ a,8,a,192,a,104\n"
"MZ a,12,a,192,a,108\n"
"MZ a,28,a,196,a,108\n"
"TR 220,252,4104\n"
"TR 222,252,5128\n"
"MZ a,24,a,196,a,104\n"
"MZ a,20,a,196,a,100\n"
"buffer_load_dwordx4 a[152:155], v249, s[16:19], s88 offen offset:2048 sc1\n"
"MZ a,16,a,196,a,96\n"
"s_waitcnt lgkmcnt(4)\n"
"MZ a,32,a,200,a,96\n"
"TR 224,253,4096\n"
"TR 226,253,5120\n"
"MZ a,36,a,200,a,100\n"
"s_add_u32 m0, s1, 0\n"
"s_add_u32 s89, s3, 0\n"
"MZ a,40,a,200,a,104\n"
"MZ a,44,a,200,a,108\n"
"MZ a,60,a,204,a,108\n"
"buffer_load_dwordx4 v236, s[8:11], s89 offen lds\n"
"MZ a,56,a,204,a,104\n"
"TR 228,253,4104\n"
"MZ a,52,a,204,a,100\n"
"buffer_load_dwordx4 a[156:159], v249, s[16:19], s88 offen offset:3072 sc1\n"
"MZ a,48,a,204,a,96\n"
"TR 230,253,5128\n"
"MZ a,64,a,208,a,96\n"
"s_add_u32 s90, s3, 48128\n"
"TR 232,252,12288\n"
"TR 234,252,13312\n"
"MZ a,68,a,208,a,100\n"
"MZ a,72,a,208,a,104\n"
"MZ a,76,a,208,a,108\n"
"buffer_load_dwordx4 v236, s[8:11], s90 offen offset:1024 lds\n"
"MZ a,92,a,212,a,108\n"
"TR 236,252,12296\n"
"MZ a,88,a,212,a,104\n"
"TR 238,252,13320\n"
"MZ a,84,a,212,a,100\n"
"MZ a,80,a,212,a,96\n"
"s_waitcnt vmcnt(13) lgkmcnt(4)\n"
"s_add_u32 m0, s1, 8192\n"
"MA v,0,a,216,a,112\n"
"s_waitcnt vmcnt(12)\n"
"s_add_u32 s91, s3, 128\n"
"TR 192,253,12288\n"
"MA v,4,a,216,a,116\n"
"s_waitcnt vmcnt(11)\n"
"TR 194,253,13312\n"
"MA v,8,a,216,a,120\n"
"s_waitcnt vmcnt(9)\n"
"buffer_load_dwordx4 v236, s[8:11], s91 offen lds\n"
"MA v,12,a,216,a,124\n"
"MA v,28,a,220,a,124\n"
"TR 196,253,12296\n"
"TR 198,253,13320\n"
"MA v,24,a,220,a,120\n"
"MA v,20,a,220,a,116\n"
"s_add_u32 s86, s3, 48256\n"
"MA v,16,a,220,a,112\n"
"MA v,32,a,224,a,112\n"
"TR 200,252,20480\n"
"TR 202,252,21504\n"
"MA v,36,a,224,a,116\n"
"s_add_u32 s91, s3, 1572864\n"
"s_cmp_eq_u32 s100, s6\n"
"s_cselect_b32 s91, s91, s55\n"
"buffer_load_dwordx4 v236, s[8:11], s86 offen offset:1024 lds\n"
"MA v,40,a,224,a,120\n"
"MA v,44,a,224,a,124\n"
"MA v,60,a,228,a,124\n"
"s_add_u32 s86, s4, 393216\n"
"s_cmp_eq_u32 s100, s6\n"
"s_cselect_b32 s86, s86, s55\n"
"buffer_load_dword v235, v192, s[8:11], s91 offen\n"
"MA v,56,a,228,a,120\n"
"s_add_u32 m0, s1, 16384\n"
"s_add_u32 s87, s3, 256\n"
"MA v,52,a,228,a,116\n"
"TR 204,252,20488\n"
"TR 206,252,21512\n"
"MA v,48,a,228,a,112\n"
"s_waitcnt lgkmcnt(4)\n"
"buffer_load_dword v235, v193, s[12:15], s86 offen\n"
"MA v,64,a,232,a,112\n"
"TR 208,253,20480\n"
"MA v,68,a,232,a,116\n"
"buffer_load_dwordx4 v236, s[8:11], s87 offen lds\n"
"MA v,72,a,232,a,120\n"
"s_cmp_eq_u32 s101, s6\n"
"s_cselect_b32 s87, s68, s55\n"
"TR 210,253,21504\n"
"MA v,76,a,232,a,124\n"
"MA v,92,a,236,a,124\n"
"s_add_u32 s88, s3, 48384\n"
"TR 212,253,20488\n"
"TR 214,253,21512\n"
"MA v,88,a,236,a,120\n"
"buffer_load_dword v235, v198, s[36:39], s87 offen\n"
"MA v,84,a,236,a,116\n"
"MA v,80,a,236,a,112\n"
"buffer_load_dwordx4 v236, s[8:11], s88 offen offset:1024 lds\n"
"MA v,96,a,192,a,112\n"
"TR 216,255,3072\n"
"MA v,100,a,192,a,116\n"
"TR 218,255,3840\n"
"MA v,104,a,192,a,120\n"
"MA v,108,a,192,a,124\n"
"s_mov_b32 m0, s2\n"
"MA v,124,a,196,a,124\n"
"TR 220,255,9216\n"
"TR 222,255,9984\n"
"MA v,120,a,196,a,120\n"
"MA v,116,a,196,a,116\n"
"buffer_load_dwordx4 v237, s[12:15], s4 offen lds\n"
"MA v,112,a,196,a,112\n"
"s_waitcnt lgkmcnt(4)\n"
"MA v,128,a,200,a,112\n"
"s_add_u32 s1, s0, s44\n"
"TR 224,255,3104\n"
"TR 226,255,3872\n"
"MA v,132,a,200,a,116\n"
"s_add_u32 s2, s0, s45\n"
"s_add_u32 s0, s0, 36864\n"
"s_cmp_eq_u32 s0, 110592\n"
"s_cselect_b32 s0, 0, s0\n"
"MA v,136,a,200,a,120\n"
"v_add_u32 v252, s0, v250\n"
"v_add_u32 v253, s0, v251\n"
"MA v,140,a,200,a,124\n"
"MA v,156,a,204,a,124\n"
"TR 228,255,9248\n"
"TR 230,255,10016\n"
"MA v,152,a,204,a,120\n"
"buffer_load_dwordx4 v238, s[12:15], s4 offen offset:1024 lds\n"
"MA v,148,a,204,a,116\n"
"MA v,144,a,204,a,112\n"
"MA v,160,a,208,a,112\n"
"TR 232,255,3136\n"
"TR 234,255,3904\n"
"MA v,164,a,208,a,116\n"
"MA v,168,a,208,a,120\n"
"MA v,172,a,208,a,124\n"
"MA v,188,a,212,a,124\n"
"buffer_load_dwordx4 v239, s[12:15], s4 offen offset:2048 lds\n"
"MA v,184,a,212,a,120\n"
"TR 236,255,9280\n"
"MA v,180,a,212,a,116\n"
"TR 238,255,10048\n"
"v_add_u32 v255, s0, v254\n"
"MA v,176,a,212,a,112\n"
"s_waitcnt lgkmcnt(4)\n"
"MA a,0,a,216,a,112\n"
"s_add_u32 s89, s5, 0\n"
"s_add_u32 s3, s3, 786432\n"
"MA a,4,a,216,a,116\n"
"s_add_u32 s4, s4, 196608\n"
"s_add_u32 s6, s6, 1\n"
"MA a,8,a,216,a,120\n"
"MA a,12,a,216,a,124\n"
"buffer_load_dwordx4 a[160:163], v249, s[16:19], s89 offen sc1\n"
"MA a,28,a,220,a,124\n"
"MA a,24,a,220,a,120\n"
"s_waitcnt lgkmcnt(0)\n"
"s_barrier\n"
"TR 192,252,0\n"
"TR 194,252,1024\n"
"MA a,20,a,220,a,116\n"
"TR 196,252,4096\n"
"TR 198,252,5120\n"
"MA a,16,a,220,a,112\n"
"MA a,32,a,224,a,112\n"
"TR 200,252,8\n"
"TR 202,252,1032\n"
"MA a,36,a,224,a,116\n"
"TR 204,252,4104\n"
"TR 206,252,5128\n"
"MA a,40,a,224,a,120\n"
"buffer_load_dwordx4 a[164:167], v249, s[16:19], s89 offen offset:1024 sc1\n"
"MA a,44,a,224,a,124\n"
"MA a,60,a,228,a,124\n"
"MA a,56,a,228,a,120\n"
"MA a,52,a,228,a,116\n"
"MA a,48,a,228,a,112\n"
"MA a,64,a,232,a,112\n"
"TR 208,253,0\n"
"TR 210,253,1024\n"
"MA a,68,a,232,a,116\n"
"s_add_u32 m0, s1, 0\n"
"buffer_load_dwordx4 a[168:171], v249, s[16:19], s89 offen offset:2048 sc1\n"
"MA a,72,a,232,a,120\n"
"s_add_u32 s86, s3, 0\n"
"TR 212,253,4096\n"
"MA a,76,a,232,a,124\n"
"TR 214,253,5120\n"
"MA a,92,a,236,a,124\n"
"buffer_load_dwordx4 v236, s[8:11], s86 offen lds\n"
"MA a,88,a,236,a,120\n"
"MA a,84,a,236,a,116\n"
"MA a,80,a,236,a,112\n"
"s_waitcnt lgkmcnt(4)\n"
"MA v,0,a,192,a,128\n"
"s_waitcnt vmcnt(20)\n"
"buffer_load_dwordx4 a[172:175], v249, s[16:19], s89 offen offset:3072 sc1\n"
"MA v,0,a,196,a,144\n"
"s_waitcnt vmcnt(19)\n"
"s_add_u32 s87, s3, 48128\n"
"MA v,4,a,196,a,148\n"
"TR 216,253,8\n"
"TR 218,253,1032\n"
"MA v,4,a,192,a,132\n"
"buffer_load_dwordx4 v236, s[8:11], s87 offen offset:1024 lds\n"
"MA v,8,a,192,a,136\n"
"s_waitcnt vmcnt(19)\n"
"s_add_u32 s90, s5, 4096\n"
"MA v,8,a,196,a,152\n"
"s_waitcnt vmcnt(17)\n"
"TR 220,253,4104\n"
"TR 222,253,5128\n"
"MA v,12,a,196,a,156\n"
"s_add_u32 s5, s5, 8192\n"
"MA v,12,a,192,a,140\n"
"buffer_load_dwordx4 a[176:179], v249, s[16:19], s90 offen sc1\n"
"MA v,28,a,200,a,140\n"
"s_add_u32 m0, s1, 8192\n"
"MA v,28,a,204,a,156\n"
"s_add_u32 s88, s3, 128\n"
"TR 224,252,8192\n"
"TR 226,252,9216\n"
"MA v,24,a,204,a,152\n"
"buffer_load_dwordx4 v236, s[8:11], s88 offen lds\n"
"MA v,24,a,200,a,136\n"
"TR 228,252,12288\n"
"MA v,20,a,200,a,132\n"
"TR 230,252,13312\n"
"MA v,20,a,204,a,148\n"
"MA v,16,a,204,a,144\n"
"s_add_u32 s89, s3, 48256\n"
"buffer_load_dwordx4 a[180:183], v249, s[16:19], s90 offen offset:1024 sc1\n"
"MA v,16,a,200,a,128\n"
"s_waitcnt lgkmcnt(4)\n"
"MA v,32,a,208,a,128\n"
"TR 232,252,8200\n"
"TR 234,252,9224\n"
"MA v,32,a,212,a,144\n"
"buffer_load_dwordx4 v236, s[8:11], s89 offen offset:1024 lds\n"
"MA v,36,a,212,a,148\n"
"TR 236,252,12296\n"
"MA v,36,a,208,a,132\n"
"TR 238,252,13320\n"
"MA v,40,a,208,a,136\n"
"MA v,40,a,212,a,152\n"
"s_add_u32 m0, s1, 16384\n"
"buffer_load_dwordx4 a[184:187], v249, s[16:19], s90 offen offset:2048 sc1\n"
"MA v,44,a,212,a,156\n"
"MA v,44,a,208,a,140\n"
"MA v,60,a,216,a,140\n"
"TR 192,253,8192\n"
"TR 194,253,9216\n"
"TR 196,253,12288\n"
"MA v,60,a,220,a,156\n"
"buffer_load_dwordx4 a[188:191], v249, s[16:19], s90 offen offset:3072 sc1\n"
"MA v,56,a,220,a,152\n"
"s_add_u32 s90, s3, 256\n"
"TR 198,253,13312\n"
"MA v,56,a,216,a,136\n"
"buffer_load_dwordx4 v236, s[8:11], s90 offen lds\n"
"MA v,52,a,216,a,132\n"
"s_add_u32 s91, s3, 48384\n"
"MA v,52,a,220,a,148\n"
"MA v,48,a,220,a,144\n"
"MA v,48,a,216,a,128\n"
"s_waitcnt lgkmcnt(4)\n"
"buffer_load_dwordx4 v236, s[8:11], s91 offen offset:1024 lds\n"
"MA v,64,a,224,a,128\n"
"TR 200,253,8200\n"
"MA v,64,a,228,a,144\n"
"TR 202,253,9224\n"
"TR 204,253,12296\n"
"MA v,68,a,228,a,148\n"
"TR 206,253,13320\n"
"MA v,68,a,224,a,132\n"
"s_mov_b32 m0, s2\n"
"MA v,72,a,224,a,136\n"
"MA v,72,a,228,a,152\n"
"MA v,76,a,228,a,156\n"
"buffer_load_dwordx4 v237, s[12:15], s4 offen lds\n"
"MA v,76,a,224,a,140\n"
"MA v,92,a,232,a,140\n"
"TR 208,252,16384\n"
"TR 210,252,17408\n"
"MA v,92,a,236,a,156\n"
"TR 212,252,20480\n"
"TR 214,252,21504\n"
"MA v,88,a,236,a,152\n"
"MA v,88,a,232,a,136\n"
"MA v,84,a,232,a,132\n"
"MA v,84,a,236,a,148\n"
"buffer_load_dwordx4 v238, s[12:15], s4 offen offset:1024 lds\n"
"MA v,80,a,236,a,144\n"
"MA v,80,a,232,a,128\n"
"s_waitcnt lgkmcnt(4)\n"
"MA v,96,a,192,a,128\n"
"TR 216,252,16392\n"
"TR 218,252,17416\n"
"MA v,96,a,196,a,144\n"
"TR 220,252,20488\n"
"TR 222,252,21512\n"
"MA v,100,a,196,a,148\n"
"MA v,100,a,192,a,132\n"
"MA v,104,a,192,a,136\n"
"buffer_load_dwordx4 v239, s[12:15], s4 offen offset:2048 lds\n"
"MA v,104,a,196,a,152\n"
"MA v,108,a,196,a,156\n"
"MA v,108,a,192,a,140\n"
"MA v,124,a,200,a,140\n"
"s_add_u32 s86, s5, 0\n"
"TR 224,253,16384\n"
"MA v,124,a,204,a,156\n"
"TR 226,253,17408\n"
"TR 228,253,20480\n"
"MA v,120,a,204,a,152\n"
"TR 230,253,21504\n"
"MA v,120,a,200,a,136\n"
"buffer_load_dwordx4 a[96:99], v249, s[16:19], s86 offen sc1\n"
"MA v,116,a,200,a,132\n"
"MA v,116,a,204,a,148\n"
"MA v,112,a,204,a,144\n"
"MA v,112,a,200,a,128\n"
"s_waitcnt lgkmcnt(4)\n"
"MA v,128,a,208,a,128\n"
"TR 232,253,16392\n"
"TR 234,253,17416\n"
"MA v,128,a,212,a,144\n"
"TR 236,253,20488\n"
"TR 238,253,21512\n"
"MA v,132,a,212,a,148\n"
"buffer_load_dwordx4 a[100:103], v249, s[16:19], s86 offen offset:1024 sc1\n"
"MA v,132,a,208,a,132\n"
"MA v,136,a,208,a,136\n"
"MA v,136,a,212,a,152\n"
"MA v,140,a,212,a,156\n"
"MA v,140,a,208,a,140\n"
"MA v,156,a,216,a,140\n"
"TR 192,255,0\n"
"TR 194,255,768\n"
"MA v,156,a,220,a,156\n"
"buffer_load_dwordx4 a[104:107], v249, s[16:19], s86 offen offset:2048 sc1\n"
"MA v,152,a,220,a,152\n"
"TR 196,255,3072\n"
"MA v,152,a,216,a,136\n"
"TR 198,255,3840\n"
"MA v,148,a,216,a,132\n"
"MA v,148,a,220,a,148\n"
"MA v,144,a,220,a,144\n"
"MA v,144,a,216,a,128\n"
"s_waitcnt lgkmcnt(4)\n"
"MA v,160,a,224,a,128\n"
"buffer_load_dwordx4 a[108:111], v249, s[16:19], s86 offen offset:3072 sc1\n"
"MA v,160,a,228,a,144\n"
"s_add_u32 s88, s3, 1572864\n"
"s_cmp_eq_u32 s100, s6\n"
"s_cselect_b32 s88, s88, s55\n"
"TR 200,255,6144\n"
"MA v,164,a,228,a,148\n"
"TR 202,255,6912\n"
"TR 204,255,9216\n"
"MA v,164,a,224,a,132\n"
"TR 206,255,9984\n"
"MA v,168,a,224,a,136\n"
"s_add_u32 s87, s5, 4096\n"
"buffer_load_dword v235, v192, s[8:11], s88 offen\n"
"MA v,168,a,228,a,152\n"
"s_add_u32 s89, s4, 393216\n"
"s_cmp_eq_u32 s100, s6\n"
"s_cselect_b32 s89, s89, s55\n"
"MA v,172,a,228,a,156\n"
"MA v,172,a,224,a,140\n"
"buffer_load_dwordx4 a[112:115], v249, s[16:19], s87 offen sc1\n"
"MA v,188,a,232,a,140\n"
"TR 208,255,32\n"
"MA v,188,a,236,a,156\n"
"buffer_load_dword v235, v193, s[12:15], s89 offen\n"
"MA v,184,a,236,a,152\n"
"s_cmp_eq_u32 s101, s6\n"
"s_cselect_b32 s90, s68, s55\n"
"TR 210,255,800\n"
"MA v,184,a,232,a,136\n"
"TR 212,255,3104\n"
"TR 214,255,3872\n"
"MA v,180,a,232,a,132\n"
"MA v,180,a,236,a,148\n"
"buffer_load_dword v235, v198, s[36:39], s90 offen\n"
"MA v,176,a,236,a,144\n"
"MA v,176,a,232,a,128\n"
"s_waitcnt lgkmcnt(4)\n"
"buffer_load_dwordx4 a[116:119], v249, s[16:19], s87 offen offset:1024 sc1\n"
"s_mov_b32 s7, 2\n"
"L_LOOPP_%=:\n"
"MA a,0,a,192,a,128\n"
"TR 216,255,6176\n"
"MA a,0,a,196,a,144\n"
"TR 218,255,6944\n"
"TR 220,255,9248\n"
"MA a,4,a,196,a,148\n"
"TR 222,255,10016\n"
"MA a,4,a,192,a,132\n"
"MA a,8,a,192,a,136\n"
"MA a,8,a,196,a,152\n"
"buffer_load_dwordx4 a[120:123], v249, s[16:19], s87 offen offset:2048 sc1\n"
"MA a,12,a,196,a,156\n"
"MA a,12,a,192,a,140\n"
"MA a,28,a,200,a,140\n"
"TR 224,255,64\n"
"TR 226,255,832\n"
"MA a,28,a,204,a,156\n"
"s_add_u32 s1, s0, s44\n"
"TR 228,255,3136\n"
"TR 230,255,3904\n"
"MA a,24,a,204,a,152\n"
"s_add_u32 s2, s0, s45\n"
"s_add_u32 s0, s0, 36864\n"
"s_cmp_eq_u32 s0, 110592\n"
"s_cselect_b32 s0, 0, s0\n"
"MA a,24,a,200,a,136\n"
"v_add_u32 v252, s0, v250\n"
"v_add_u32 v253, s0, v251\n"
"MA a,20,a,200,a,132\n"
"buffer_load_dwordx4 a[124:127], v249, s[16:19], s87 offen offset:3072 sc1\n"
"MA a,20,a,204,a,148\n"
"MA a,16,a,204,a,144\n"
"MA a,16,a,200,a,128\n"
"s_waitcnt lgkmcnt(4)\n"
"MA a,32,a,208,a,128\n"
"TR 232,255,6208\n"
"TR 234,255,6976\n"
"MA a,32,a,212,a,144\n"
"TR 236,255,9280\n"
"TR 238,255,10048\n"
"MA a,36,a,212,a,148\n"
"v_add_u32 v255, s0, v254\n"
"MA a,36,a,208,a,132\n"
"MA a,40,a,208,a,136\n"
"MA a,40,a,212,a,152\n"
"MA a,44,a,212,a,156\n"
"MA a,44,a,208,a,140\n"
"MA a,60,a,216,a,140\n"
"s_waitcnt vmcnt(28) lgkmcnt(0)\n"
"s_barrier\n"
"TR 192,252,0\n"
"MA a,60,a,220,a,156\n"
"s_add_u32 s3, s3, 786432\n"
"TR 194,252,1024\n"
"TR 196,252,4096\n"
"MA a,56,a,220,a,152\n"
"s_add_u32 s4, s4, 196608\n"
"s_add_u32 s5, s5, 8192\n"
"TR 198,252,5120\n"
"MA a,56,a,216,a,136\n"
"s_add_u32 s6, s6, 1\n"
"MA a,52,a,216,a,132\n"
"MA a,52,a,220,a,148\n"
"MA a,48,a,220,a,144\n"
"MA a,48,a,216,a,128\n"
"MA a,64,a,224,a,128\n"
"TR 200,252,8\n"
"TR 202,252,1032\n"
"MA a,64,a,228,a,144\n"
"TR 204,252,4104\n"
"TR 206,252,5128\n"
"MA a,68,a,228,a,148\n"
"MA a,68,a,224,a,132\n"
"MA a,72,a,224,a,136\n"
"MA a,72,a,228,a,152\n"
"MA a,76,a,228,a,156\n"
"MA a,76,a,224,a,140\n"
"MA a,92,a,232,a,140\n"
"TR 208,253,0\n"
"TR 210,253,1024\n"
"MA a,92,a,236,a,156\n"
"s_add_u32 m0, s1, 0\n"
"s_add_u32 s86, s3, 0\n"
"TR 212,253,4096\n"
"MA a,88,a,236,a,152\n"
"TR 214,253,5120\n"
"MA a,88,a,232,a,136\n"
"MA a,84,a,232,a,132\n"
"buffer_load_dwordx4 v236, s[8:11], s86 offen lds\n"
"MA a,84,a,236,a,148\n"
"MA a,80,a,236,a,144\n"
"MA a,80,a,232,a,128\n"
"s_waitcnt vmcnt(28) lgkmcnt(4)\n"
"MA v,0,a,192,a,160\n"
"s_waitcnt vmcnt(22)\n"
"s_add_u32 s87, s3, 48128\n"
"TR 216,253,8\n"
"MA v,0,a,196,a,176\n"
"s_waitcnt vmcnt(20)\n"
"TR 218,253,1032\n"
"TR 220,253,4104\n"
"MA v,4,a,196,a,180\n"
"TR 222,253,5128\n"
"MA v,4,a,192,a,164\n"
"buffer_load_dwordx4 v236, s[8:11], s87 offen offset:1024 lds\n"
"MA v,8,a,192,a,168\n"
"s_waitcnt vmcnt(19)\n"
"MA v,8,a,196,a,184\n"
"s_waitcnt vmcnt(18)\n"
"MA v,12,a,196,a,188\n"
"MA v,12,a,192,a,172\n"
"s_add_u32 m0, s1, 8192\n"
"MA v,28,a,200,a,172\n"
"s_add_u32 s88, s3, 128\n"
"TR 224,252,8192\n"
"MA v,28,a,204,a,188\n"
"TR 226,252,9216\n"
"TR 228,252,12288\n"
"MA v,24,a,204,a,184\n"
"buffer_load_dwordx4 v236, s[8:11], s88 offen lds\n"
"MA v,24,a,200,a,168\n"
"TR 230,252,13312\n"
"MA v,20,a,200,a,164\n"
"MA v,20,a,204,a,180\n"
"MA v,16,a,204,a,176\n"
"s_add_u32 s89, s3, 48256\n"
"MA v,16,a,200,a,160\n"
"s_waitcnt lgkmcnt(4)\n"
"MA v,32,a,208,a,160\n"
"TR 232,252,8200\n"
"TR 234,252,9224\n"
"MA v,32,a,212,a,176\n"
"buffer_load_dwordx4 v236, s[8:11], s89 offen offset:1024 lds\n"
"MA v,36,a,212,a,180\n"
"TR 236,252,12296\n"
"MA v,36,a,208,a,164\n"
"TR 238,252,13320\n"
"MA v,40,a,208,a,168\n"
"MA v,40,a,212,a,184\n"
"s_add_u32 m0, s1, 16384\n"
"s_add_u32 s90, s3, 256\n"
"MA v,44,a,212,a,188\n"
"MA v,44,a,208,a,172\n"
"MA v,60,a,216,a,172\n"
"buffer_load_dwordx4 v236, s[8:11], s90 offen lds\n"
"MA v,60,a,220,a,188\n"
"TR 192,253,8192\n"
"MA v,56,a,220,a,184\n"
"TR 194,253,9216\n"
"TR 196,253,12288\n"
"MA v,56,a,216,a,168\n"
"TR 198,253,13312\n"
"MA v,52,a,216,a,164\n"
"s_add_u32 s91, s3, 48384\n"
"MA v,52,a,220,a,180\n"
"MA v,48,a,220,a,176\n"
"MA v,48,a,216,a,160\n"
"s_waitcnt lgkmcnt(4)\n"
"buffer_load_dwordx4 v236, s[8:11], s91 offen offset:1024 lds\n"
"MA v,64,a,224,a,160\n"
"TR 200,253,8200\n"
"MA v,64,a,228,a,176\n"
"TR 202,253,9224\n"
"TR 204,253,12296\n"
"MA v,68,a,228,a,180\n"
"TR 206,253,13320\n"
"MA v,68,a,224,a,164\n"
"s_mov_b32 m0, s2\n"
"MA v,72,a,224,a,168\n"
"MA v,72,a,228,a,184\n"
"MA v,76,a,228,a,188\n"
"buffer_load_dwordx4 v237, s[12:15], s4 offen lds\n"
"MA v,76,a,224,a,172\n"
"MA v,92,a,232,a,172\n"
"TR 208,252,16384\n"
"TR 210,252,17408\n"
"MA v,92,a,236,a,188\n"
"TR 212,252,20480\n"
"TR 214,252,21504\n"
"MA v,88,a,236,a,184\n"
"MA v,88,a,232,a,168\n"
"MA v,84,a,232,a,164\n"
"MA v,84,a,236,a,180\n"
"buffer_load_dwordx4 v238, s[12:15], s4 offen offset:1024 lds\n"
"MA v,80,a,236,a,176\n"
"MA v,80,a,232,a,160\n"
"s_waitcnt lgkmcnt(4)\n"
"MA v,96,a,192,a,160\n"
"TR 216,252,16392\n"
"TR 218,252,17416\n"
"MA v,96,a,196,a,176\n"
"TR 220,252,20488\n"
"TR 222,252,21512\n"
"MA v,100,a,196,a,180\n"
"MA v,100,a,192,a,164\n"
"MA v,104,a,192,a,168\n"
"buffer_load_dwordx4 v239, s[12:15], s4 offen offset:2048 lds\n"
"MA v,104,a,196,a,184\n"
"MA v,108,a,196,a,188\n"
"MA v,108,a,192,a,172\n"
"MA v,124,a,200,a,172\n"
"s_add_u32 s86, s5, 0\n"
"TR 224,253,16384\n"
"MA v,124,a,204,a,188\n"
"TR 226,253,17408\n"
"TR 228,253,20480\n"
"MA v,120,a,204,a,184\n"
"TR 230,253,21504\n"
"MA v,120,a,200,a,168\n"
"buffer_load_dwordx4 a[128:131], v249, s[16:19], s86 offen sc1\n"
"MA v,116,a,200,a,164\n"
"MA v,116,a,204,a,180\n"
"MA v,112,a,204,a,176\n"
"MA v,112,a,200,a,160\n"
"s_waitcnt lgkmcnt(4)\n"
"MA v,128,a,208,a,160\n"
"TR 232,253,16392\n"
"TR 234,253,17416\n"
"MA v,128,a,212,a,176\n"
"TR 236,253,20488\n"
"TR 238,253,21512\n"
"MA v,132,a,212,a,180\n"
"buffer_load_dwordx4 a[132:135], v249, s[16:19], s86 offen offset:1024 sc1\n"
"MA v,132,a,208,a,164\n"
"MA v,136,a,208,a,168\n"
"MA v,136,a,212,a,184\n"
"MA v,140,a,212,a,188\n"
"MA v,140,a,208,a,172\n"
"MA v,156,a,216,a,172\n"
"TR 192,255,0\n"
"TR 194,255,768\n"
"MA v,156,a,220,a,188\n"
"buffer_load_dwordx4 a[136:139], v249, s[16:19], s86 offen offset:2048 sc1\n"
"MA v,152,a,220,a,184\n"
"TR 196,255,3072\n"
"MA v,152,a,216,a,168\n"
"TR 198,255,3840\n"
"MA v,148,a,216,a,164\n"
"MA v,148,a,220,a,180\n"
"MA v,144,a,220,a,176\n"
"MA v,144,a,216,a,160\n"
"s_waitcnt lgkmcnt(4)\n"
"MA v,160,a,224,a,160\n"
"buffer_load_dwordx4 a[140:143], v249, s[16:19], s86 offen offset:3072 sc1\n"
"MA v,160,a,228,a,176\n"
"s_add_u32 s88, s3, 1572864\n"
"s_cmp_eq_u32 s100, s6\n"
"s_cselect_b32 s88, s88, s55\n"
"TR 200,255,6144\n"
"MA v,164,a,228,a,180\n"
"TR 202,255,6912\n"
"TR 204,255,9216\n"
"MA v,164,a,224,a,164\n"
"TR 206,255,9984\n"
"MA v,168,a,224,a,168\n"
"s_add_u32 s87, s5, 4096\n"
"buffer_load_dword v235, v192, s[8:11], s88 offen\n"
"MA v,168,a,228,a,184\n"
"s_add_u32 s89, s4, 393216\n"
"s_cmp_eq_u32 s100, s6\n"
"s_cselect_b32 s89, s89, s55\n"
"MA v,172,a,228,a,188\n"
"MA v,172,a,224,a,172\n"
"buffer_load_dwordx4 a[144:147], v249, s[16:19], s87 offen sc1\n"
"MA v,188,a,232,a,172\n"
"TR 208,255,32\n"
"MA v,188,a,236,a,188\n"
"buffer_load_dword v235, v193, s[12:15], s89 offen\n"
"MA v,184,a,236,a,184\n"
"s_cmp_eq_u32 s101, s6\n"
"s_cselect_b32 s90, s68, s55\n"
"TR 210,255,800\n"
"MA v,184,a,232,a,168\n"
"TR 212,255,3104\n"
"TR 214,255,3872\n"
"MA v,180,a,232,a,164\n"
"MA v,180,a,236,a,180\n"
"buffer_load_dword v235, v198, s[36:39], s90 offen\n"
"MA v,176,a,236,a,176\n"
"MA v,176,a,232,a,160\n"
"s_waitcnt lgkmcnt(4)\n"
"buffer_load_dwordx4 a[148:151], v249, s[16:19], s87 offen offset:1024 sc1\n"
"MA a,0,a,192,a,160\n"
"TR 216,255,6176\n"
"MA a,0,a,196,a,176\n"
"TR 218,255,6944\n"
"TR 220,255,9248\n"
"MA a,4,a,196,a,180\n"
"TR 222,255,10016\n"
"MA a,4,a,192,a,164\n"
"MA a,8,a,192,a,168\n"
"MA a,8,a,196,a,184\n"
"buffer_load_dwordx4 a[152:155], v249, s[16:19], s87 offen offset:2048 sc1\n"
"MA a,12,a,196,a,188\n"
"MA a,12,a,192,a,172\n"
"MA a,28,a,200,a,172\n"
"TR 224,255,64\n"
"TR 226,255,832\n"
"MA a,28,a,204,a,188\n"
"s_add_u32 s1, s0, s44\n"
"TR 228,255,3136\n"
"TR 230,255,3904\n"
"MA a,24,a,204,a,184\n"
"s_add_u32 s2, s0, s45\n"
"s_add_u32 s0, s0, 36864\n"
"s_cmp_eq_u32 s0, 110592\n"
"s_cselect_b32 s0, 0, s0\n"
"MA a,24,a,200,a,168\n"
"v_add_u32 v252, s0, v250\n"
"v_add_u32 v253, s0, v251\n"
"MA a,20,a,200,a,164\n"
"buffer_load_dwordx4 a[156:159], v249, s[16:19], s87 offen offset:3072 sc1\n"
"MA a,20,a,204,a,180\n"
"MA a,16,a,204,a,176\n"
"MA a,16,a,200,a,160\n"
"s_waitcnt lgkmcnt(4)\n"
"MA a,32,a,208,a,160\n"
"TR 232,255,6208\n"
"TR 234,255,6976\n"
"MA a,32,a,212,a,176\n"
"TR 236,255,9280\n"
"TR 238,255,10048\n"
"MA a,36,a,212,a,180\n"
"v_add_u32 v255, s0, v254\n"
"MA a,36,a,208,a,164\n"
"MA a,40,a,208,a,168\n"
"MA a,40,a,212,a,184\n"
"MA a,44,a,212,a,188\n"
"MA a,44,a,208,a,172\n"
"MA a,60,a,216,a,172\n"
"s_waitcnt vmcnt(31) lgkmcnt(0)\n"
"s_barrier\n"
"TR 192,252,0\n"
"MA a,60,a,220,a,188\n"
"s_add_u32 s3, s3, 786432\n"
"TR 194,252,1024\n"
"TR 196,252,4096\n"
"MA a,56,a,220,a,184\n"
"s_add_u32 s4, s4, 196608\n"
"s_add_u32 s5, s5, 8192\n"
"TR 198,252,5120\n"
"MA a,56,a,216,a,168\n"
"s_add_u32 s6, s6, 1\n"
"MA a,52,a,216,a,164\n"
"MA a,52,a,220,a,180\n"
"MA a,48,a,220,a,176\n"
"MA a,48,a,216,a,160\n"
"MA a,64,a,224,a,160\n"
"TR 200,252,8\n"
"TR 202,252,1032\n"
"MA a,64,a,228,a,176\n"
"TR 204,252,4104\n"
"TR 206,252,5128\n"
"MA a,68,a,228,a,180\n"
"MA a,68,a,224,a,164\n"
"MA a,72,a,224,a,168\n"
"MA a,72,a,228,a,184\n"
"MA a,76,a,228,a,188\n"
"MA a,76,a,224,a,172\n"
"MA a,92,a,232,a,172\n"
"TR 208,253,0\n"
"TR 210,253,1024\n"
"MA a,92,a,236,a,188\n"
"s_add_u32 m0, s1, 0\n"
"s_add_u32 s86, s3, 0\n"
"TR 212,253,4096\n"
"MA a,88,a,236,a,184\n"
"TR 214,253,5120\n"
"MA a,88,a,232,a,168\n"
"MA a,84,a,232,a,164\n"
"buffer_load_dwordx4 v236, s[8:11], s86 offen lds\n"
"MA a,84,a,236,a,180\n"
"MA a,80,a,236,a,176\n"
"MA a,80,a,232,a,160\n"
"s_waitcnt vmcnt(31) lgkmcnt(4)\n"
"MA v,0,a,192,a,96\n"
"s_waitcnt vmcnt(26)\n"
"s_add_u32 s87, s3, 48128\n"
"TR 216,253,8\n"
"MA v,0,a,196,a,112\n"
"s_waitcnt vmcnt(23)\n"
"TR 218,253,1032\n"
"TR 220,253,4104\n"
"MA v,4,a,196,a,116\n"
"TR 222,253,5128\n"
"MA v,4,a,192,a,100\n"
"buffer_load_dwordx4 v236, s[8:11], s87 offen offset:1024 lds\n"
"MA v,8,a,192,a,104\n"
"s_waitcnt vmcnt(23)\n"
"MA v,8,a,196,a,120\n"
"s_waitcnt vmcnt(22)\n"
"MA v,12,a,196,a,124\n"
"MA v,12,a,192,a,108\n"
"s_add_u32 m0, s1, 8192\n"
"MA v,28,a,200,a,108\n"
"s_add_u32 s88, s3, 128\n"
"TR 224,252,8192\n"
"MA v,28,a,204,a,124\n"
"TR 226,252,9216\n"
"TR 228,252,12288\n"
"MA v,24,a,204,a,120\n"
"buffer_load_dwordx4 v236, s[8:11], s88 offen lds\n"
"MA v,24,a,200,a,104\n"
"TR 230,252,13312\n"
"MA v,20,a,200,a,100\n"
"MA v,20,a,204,a,116\n"
"MA v,16,a,204,a,112\n"
"s_add_u32 s89, s3, 48256\n"
"MA v,16,a,200,a,96\n"
"s_waitcnt lgkmcnt(4)\n"
"MA v,32,a,208,a,96\n"
"TR 232,252,8200\n"
"TR 234,252,9224\n"
"MA v,32,a,212,a,112\n"
"buffer_load_dwordx4 v236, s[8:11], s89 offen offset:1024 lds\n"
"MA v,36,a,212,a,116\n"
"TR 236,252,12296\n"
"MA v,36,a,208,a,100\n"
"TR 238,252,13320\n"
"MA v,40,a,208,a,104\n"
"MA v,40,a,212,a,120\n"
"s_add_u32 m0, s1, 16384\n"
"s_add_u32 s90, s3, 256\n"
"MA v,44,a,212,a,124\n"
"MA v,44,a,208,a,108\n"
"MA v,60,a,216,a,108\n"
"buffer_load_dwordx4 v236, s[8:11], s90 offen lds\n"
"MA v,60,a,220,a,124\n"
"TR 192,253,8192\n"
"MA v,56,a,220,a,120\n"
"TR 194,253,9216\n"
"TR 196,253,12288\n"
"MA v,56,a,216,a,104\n"
"TR 198,253,13312\n"
"MA v,52,a,216,a,100\n"
"s_add_u32 s91, s3, 48384\n"
"MA v,52,a,220,a,116\n"
"MA v,48,a,220,a,112\n"
"MA v,48,a,216,a,96\n"
"s_waitcnt lgkmcnt(4)\n"
"buffer_load_dwordx4 v236, s[8:11], s91 offen offset:1024 lds\n"
"MA v,64,a,224,a,96\n"
"TR 200,253,8200\n"
"MA v,64,a,228,a,112\n"
"TR 202,253,9224\n"
"TR 204,253,12296\n"
"MA v,68,a,228,a,116\n"
"TR 206,253,13320\n"
"MA v,68,a,224,a,100\n"
"s_mov_b32 m0, s2\n"
"MA v,72,a,224,a,104\n"
"MA v,72,a,228,a,120\n"
"MA v,76,a,228,a,124\n"
"buffer_load_dwordx4 v237, s[12:15], s4 offen lds\n"
"MA v,76,a,224,a,108\n"
"MA v,92,a,232,a,108\n"
"TR 208,252,16384\n"
"TR 210,252,17408\n"
"MA v,92,a,236,a,124\n"
"TR 212,252,20480\n"
"TR 214,252,21504\n"
"MA v,88,a,236,a,120\n"
"MA v,88,a,232,a,104\n"
"MA v,84,a,232,a,100\n"
"MA v,84,a,236,a,116\n"
"buffer_load_dwordx4 v238, s[12:15], s4 offen offset:1024 lds\n"
"MA v,80,a,236,a,112\n"
"MA v,80,a,232,a,96\n"
"s_waitcnt lgkmcnt(4)\n"
"MA v,96,a,192,a,96\n"
"TR 216,252,16392\n"
"TR 218,252,17416\n"
"MA v,96,a,196,a,112\n"
"TR 220,252,20488\n"
"TR 222,252,21512\n"
"MA v,100,a,196,a,116\n"
"MA v,100,a,192,a,100\n"
"MA v,104,a,192,a,104\n"
"buffer_load_dwordx4 v239, s[12:15], s4 offen offset:2048 lds\n"
"MA v,104,a,196,a,120\n"
"MA v,108,a,196,a,124\n"
"MA v,108,a,192,a,108\n"
"MA v,124,a,200,a,108\n"
"s_add_u32 s86, s5, 0\n"
"TR 224,253,16384\n"
"MA v,124,a,204,a,124\n"
"TR 226,253,17408\n"
"TR 228,253,20480\n"
"MA v,120,a,204,a,120\n"
"TR 230,253,21504\n"
"MA v,120,a,200,a,104\n"
"buffer_load_dwordx4 a[160:163], v249, s[16:19], s86 offen sc1\n"
"MA v,116,a,200,a,100\n"
"MA v,116,a,204,a,116\n"
"MA v,112,a,204,a,112\n"
"MA v,112,a,200,a,96\n"
"s_waitcnt lgkmcnt(4)\n"
"MA v,128,a,208,a,96\n"
"TR 232,253,16392\n"
"TR 234,253,17416\n"
"MA v,128,a,212,a,112\n"
"TR 236,253,20488\n"
"TR 238,253,21512\n"
"MA v,132,a,212,a,116\n"
"buffer_load_dwordx4 a[164:167], v249, s[16:19], s86 offen offset:1024 sc1\n"
"MA v,132,a,208,a,100\n"
"MA v,136,a,208,a,104\n"
"MA v,136,a,212,a,120\n"
"MA v,140,a,212,a,124\n"
"MA v,140,a,208,a,108\n"
"MA v,156,a,216,a,108\n"
"TR 192,255,0\n"
"TR 194,255,768\n"
"MA v,156,a,220,a,124\n"
"buffer_load_dwordx4 a[168:171], v249, s[16:19], s86 offen offset:2048 sc1\n"
"MA v,152,a,220,a,120\n"
"TR 196,255,3072\n"
"MA v,152,a,216,a,104\n"
"TR 198,255,3840\n"
"MA v,148,a,216,a,100\n"
"MA v,148,a,220,a,116\n"
"MA v,144,a,220,a,112\n"
"MA v,144,a,216,a,96\n"
"s_waitcnt lgkmcnt(4)\n"
"MA v,160,a,224,a,96\n"
"buffer_load_dwordx4 a[172:175], v249, s[16:19], s86 offen offset:3072 sc1\n"
"MA v,160,a,228,a,112\n"
"s_add_u32 s88, s3, 1572864\n"
"s_cmp_eq_u32 s100, s6\n"
"s_cselect_b32 s88, s88, s55\n"
"TR 200,255,6144\n"
"MA v,164,a,228,a,116\n"
"TR 202,255,6912\n"
"TR 204,255,9216\n"
"MA v,164,a,224,a,100\n"
"TR 206,255,9984\n"
"MA v,168,a,224,a,104\n"
"s_add_u32 s87, s5, 4096\n"
"buffer_load_dword v235, v192, s[8:11], s88 offen\n"
"MA v,168,a,228,a,120\n"
"s_add_u32 s89, s4, 393216\n"
"s_cmp_eq_u32 s100, s6\n"
"s_cselect_b32 s89, s89, s55\n"
"MA v,172,a,228,a,124\n"
"MA v,172,a,224,a,108\n"
"buffer_load_dwordx4 a[176:179], v249, s[16:19], s87 offen sc1\n"
"MA v,188,a,232,a,108\n"
"TR 208,255,32\n"
"MA v,188,a,236,a,124\n"
"buffer_load_dword v235, v193, s[12:15], s89 offen\n"
"MA v,184,a,236,a,120\n"
"s_cmp_eq_u32 s101, s6\n"
"s_cselect_b32 s90, s68, s55\n"
"TR 210,255,800\n"
"MA v,184,a,232,a,104\n"
"TR 212,255,3104\n"
"TR 214,255,3872\n"
"MA v,180,a,232,a,100\n"
"MA v,180,a,236,a,116\n"
"buffer_load_dword v235, v198, s[36:39], s90 offen\n"
"MA v,176,a,236,a,112\n"
"MA v,176,a,232,a,96\n"
"s_waitcnt lgkmcnt(4)\n"
"buffer_load_dwordx4 a[180:183], v249, s[16:19], s87 offen offset:1024 sc1\n"
"MA a,0,a,192,a,96\n"
"TR 216,255,6176\n"
"MA a,0,a,196,a,112\n"
"TR 218,255,6944\n"
"TR 220,255,9248\n"
"MA a,4,a,196,a,116\n"
"TR 222,255,10016\n"
"MA a,4,a,192,a,100\n"
"MA a,8,a,192,a,104\n"
"MA a,8,a,196,a,120\n"
"buffer_load_dwordx4 a[184:187], v249, s[16:19], s87 offen offset:2048 sc1\n"
"MA a,12,a,196,a,124\n"
"MA a,12,a,192,a,108\n"
"MA a,28,a,200,a,108\n"
"TR 224,255,64\n"
"TR 226,255,832\n"
"MA a,28,a,204,a,124\n"
"s_add_u32 s1, s0, s44\n"
"TR 228,255,3136\n"
"TR 230,255,3904\n"
"MA a,24,a,204,a,120\n"
"s_add_u32 s2, s0, s45\n"
"s_add_u32 s0, s0, 36864\n"
"s_cmp_eq_u32 s0, 110592\n"
"s_cselect_b32 s0, 0, s0\n"
"MA a,24,a,200,a,104\n"
"v_add_u32 v252, s0, v250\n"
"v_add_u32 v253, s0, v251\n"
"MA a,20,a,200,a,100\n"
"buffer_load_dwordx4 a[188:191], v249, s[16:19], s87 offen offset:3072 sc1\n"
"MA a,20,a,204,a,116\n"
"MA a,16,a,204,a,112\n"
"MA a,16,a,200,a,96\n"
"s_waitcnt lgkmcnt(4)\n"
"MA a,32,a,208,a,96\n"
"TR 232,255,6208\n"
"TR 234,255,6976\n"
"MA a,32,a,212,a,112\n"
"TR 236,255,9280\n"
"TR 238,255,10048\n"
"MA a,36,a,212,a,116\n"
"v_add_u32 v255, s0, v254\n"
"MA a,36,a,208,a,100\n"
"MA a,40,a,208,a,104\n"
"MA a,40,a,212,a,120\n"
"MA a,44,a,212,a,124\n"
"MA a,44,a,208,a,108\n"
"MA a,60,a,216,a,108\n"
"s_waitcnt vmcnt(31) lgkmcnt(0)\n"
"s_barrier\n"
"TR 192,252,0\n"
"MA a,60,a,220,a,124\n"
"s_add_u32 s3, s3, 786432\n"
"TR 194,252,1024\n"
"TR 196,252,4096\n"
"MA a,56,a,220,a,120\n"
"s_add_u32 s4, s4, 196608\n"
"s_add_u32 s5, s5, 8192\n"
"TR 198,252,5120\n"
"MA a,56,a,216,a,104\n"
"s_add_u32 s6, s6, 1\n"
"MA a,52,a,216,a,100\n"
"MA a,52,a,220,a,116\n"
"MA a,48,a,220,a,112\n"
"MA a,48,a,216,a,96\n"
"MA a,64,a,224,a,96\n"
"TR 200,252,8\n"
"TR 202,252,1032\n"
"MA a,64,a,228,a,112\n"
"TR 204,252,4104\n"
"TR 206,252,5128\n"
"MA a,68,a,228,a,116\n"
"MA a,68,a,224,a,100\n"
"MA a,72,a,224,a,104\n"
"MA a,72,a,228,a,120\n"
"MA a,76,a,228,a,124\n"
"MA a,76,a,224,a,108\n"
"MA a,92,a,232,a,108\n"
"TR 208,253,0\n"
"TR 210,253,1024\n"
"MA a,92,a,236,a,124\n"
"s_add_u32 m0, s1, 0\n"
"s_add_u32 s86, s3, 0\n"
"TR 212,253,4096\n"
"MA a,88,a,236,a,120\n"
"TR 214,253,5120\n"
"MA a,88,a,232,a,104\n"
"MA a,84,a,232,a,100\n"
"buffer_load_dwordx4 v236, s[8:11], s86 offen lds\n"
"MA a,84,a,236,a,116\n"
"MA a,80,a,236,a,112\n"
"MA a,80,a,232,a,96\n"
"s_waitcnt vmcnt(31) lgkmcnt(4)\n"
"MA v,0,a,192,a,128\n"
"s_waitcnt vmcnt(26)\n"
"s_add_u32 s87, s3, 48128\n"
"TR 216,253,8\n"
"MA v,0,a,196,a,144\n"
"s_waitcnt vmcnt(23)\n"
"TR 218,253,1032\n"
"TR 220,253,4104\n"
"MA v,4,a,196,a,148\n"
"TR 222,253,5128\n"
"MA v,4,a,192,a,132\n"
"buffer_load_dwordx4 v236, s[8:11], s87 offen offset:1024 lds\n"
"MA v,8,a,192,a,136\n"
"s_waitcnt vmcnt(23)\n"
"MA v,8,a,196,a,152\n"
"s_waitcnt vmcnt(22)\n"
"MA v,12,a,196,a,156\n"
"MA v,12,a,192,a,140\n"
"s_add_u32 m0, s1, 8192\n"
"MA v,28,a,200,a,140\n"
"s_add_u32 s88, s3, 128\n"
"TR 224,252,8192\n"
"MA v,28,a,204,a,156\n"
"TR 226,252,9216\n"
"TR 228,252,12288\n"
"MA v,24,a,204,a,152\n"
"buffer_load_dwordx4 v236, s[8:11], s88 offen lds\n"
"MA v,24,a,200,a,136\n"
"TR 230,252,13312\n"
"MA v,20,a,200,a,132\n"
"MA v,20,a,204,a,148\n"
"MA v,16,a,204,a,144\n"
"s_add_u32 s89, s3, 48256\n"
"MA v,16,a,200,a,128\n"
"s_waitcnt lgkmcnt(4)\n"
"MA v,32,a,208,a,128\n"
"TR 232,252,8200\n"
"TR 234,252,9224\n"
"MA v,32,a,212,a,144\n"
"buffer_load_dwordx4 v236, s[8:11], s89 offen offset:1024 lds\n"
"MA v,36,a,212,a,148\n"
"TR 236,252,12296\n"
"MA v,36,a,208,a,132\n"
"TR 238,252,13320\n"
"MA v,40,a,208,a,136\n"
"MA v,40,a,212,a,152\n"
"s_add_u32 m0, s1, 16384\n"
"s_add_u32 s90, s3, 256\n"
"MA v,44,a,212,a,156\n"
"MA v,44,a,208,a,140\n"
"MA v,60,a,216,a,140\n"
"buffer_load_dwordx4 v236, s[8:11], s90 offen lds\n"
"MA v,60,a,220,a,156\n"
"TR 192,253,8192\n"
"MA v,56,a,220,a,152\n"
"TR 194,253,9216\n"
"TR 196,253,12288\n"
"MA v,56,a,216,a,136\n"
"TR 198,253,13312\n"
"MA v,52,a,216,a,132\n"
"s_add_u32 s91, s3, 48384\n"
"MA v,52,a,220,a,148\n"
"MA v,48,a,220,a,144\n"
"MA v,48,a,216,a,128\n"
"s_waitcnt lgkmcnt(4)\n"
"buffer_load_dwordx4 v236, s[8:11], s91 offen offset:1024 lds\n"
"MA v,64,a,224,a,128\n"
"TR 200,253,8200\n"
"MA v,64,a,228,a,144\n"
"TR 202,253,9224\n"
"TR 204,253,12296\n"
"MA v,68,a,228,a,148\n"
"TR 206,253,13320\n"
"MA v,68,a,224,a,132\n"
"s_mov_b32 m0, s2\n"
"MA v,72,a,224,a,136\n"
"MA v,72,a,228,a,152\n"
"MA v,76,a,228,a,156\n"
"buffer_load_dwordx4 v237, s[12:15], s4 offen lds\n"
"MA v,76,a,224,a,140\n"
"MA v,92,a,232,a,140\n"
"TR 208,252,16384\n"
"TR 210,252,17408\n"
"MA v,92,a,236,a,156\n"
"TR 212,252,20480\n"
"TR 214,252,21504\n"
"MA v,88,a,236,a,152\n"
"MA v,88,a,232,a,136\n"
"MA v,84,a,232,a,132\n"
"MA v,84,a,236,a,148\n"
"buffer_load_dwordx4 v238, s[12:15], s4 offen offset:1024 lds\n"
"MA v,80,a,236,a,144\n"
"MA v,80,a,232,a,128\n"
"s_waitcnt lgkmcnt(4)\n"
"MA v,96,a,192,a,128\n"
"TR 216,252,16392\n"
"TR 218,252,17416\n"
"MA v,96,a,196,a,144\n"
"TR 220,252,20488\n"
"TR 222,252,21512\n"
"MA v,100,a,196,a,148\n"
"MA v,100,a,192,a,132\n"
"MA v,104,a,192,a,136\n"
"buffer_load_dwordx4 v239, s[12:15], s4 offen offset:2048 lds\n"
"MA v,104,a,196,a,152\n"
"MA v,108,a,196,a,156\n"
"MA v,108,a,192,a,140\n"
"MA v,124,a,200,a,140\n"
"s_add_u32 s86, s5, 0\n"
"TR 224,253,16384\n"
"MA v,124,a,204,a,156\n"
"TR 226,253,17408\n"
"TR 228,253,20480\n"
"MA v,120,a,204,a,152\n"
"TR 230,253,21504\n"
"MA v,120,a,200,a,136\n"
"buffer_load_dwordx4 a[96:99], v249, s[16:19], s86 offen sc1\n"
"MA v,116,a,200,a,132\n"
"MA v,116,a,204,a,148\n"
"MA v,112,a,204,a,144\n"
"MA v,112,a,200,a,128\n"
"s_waitcnt lgkmcnt(4)\n"
"MA v,128,a,208,a,128\n"
"TR 232,253,16392\n"
"TR 234,253,17416\n"
"MA v,128,a,212,a,144\n"
"TR 236,253,20488\n"
"TR 238,253,21512\n"
"MA v,132,a,212,a,148\n"
"buffer_load_dwordx4 a[100:103], v249, s[16:19], s86 offen offset:1024 sc1\n"
"MA v,132,a,208,a,132\n"
"MA v,136,a,208,a,136\n"
"MA v,136,a,212,a,152\n"
"MA v,140,a,212,a,156\n"
"MA v,140,a,208,a,140\n"
"MA v,156,a,216,a,140\n"
"TR 192,255,0\n"
"TR 194,255,768\n"
"MA v,156,a,220,a,156\n"
"buffer_load_dwordx4 a[104:107], v249, s[16:19], s86 offen offset:2048 sc1\n"
"MA v,152,a,220,a,152\n"
"TR 196,255,3072\n"
"MA v,152,a,216,a,136\n"
"TR 198,255,3840\n"
"MA v,148,a,216,a,132\n"
"MA v,148,a,220,a,148\n"
"MA v,144,a,220,a,144\n"
"MA v,144,a,216,a,128\n"
"s_waitcnt lgkmcnt(4)\n"
"MA v,160,a,224,a,128\n"
"buffer_load_dwordx4 a[108:111], v249, s[16:19], s86 offen offset:3072 sc1\n"
"MA v,160,a,228,a,144\n"
"s_add_u32 s88, s3, 1572864\n"
"s_cmp_eq_u32 s100, s6\n"
"s_cselect_b32 s88, s88, s55\n"
"TR 200,255,6144\n"
"MA v,164,a,228,a,148\n"
"TR 202,255,6912\n"
"TR 204,255,9216\n"
"MA v,164,a,224,a,132\n"
"TR 206,255,9984\n"
"MA v,168,a,224,a,136\n"
"s_add_u32 s87, s5, 4096\n"
"buffer_load_dword v235, v192, s[8:11], s88 offen\n"
"MA v,168,a,228,a,152\n"
"s_add_u32 s89, s4, 393216\n"
"s_cmp_eq_u32 s100, s6\n"
"s_cselect_b32 s89, s89, s55\n"
"MA v,172,a,228,a,156\n"
"MA v,172,a,224,a,140\n"
"buffer_load_dwordx4 a[112:115], v249, s[16:19], s87 offen sc1\n"
"MA v,188,a,232,a,140\n"
"TR 208,255,32\n"
"MA v,188,a,236,a,156\n"
"buffer_load_dword v235, v193, s[12:15], s89 offen\n"
"MA v,184,a,236,a,152\n"
"s_cmp_eq_u32 s101, s6\n"
"s_cselect_b32 s90, s68, s55\n"
"TR 210,255,800\n"
"MA v,184,a,232,a,136\n"
"TR 212,255,3104\n"
"TR 214,255,3872\n"
"MA v,180,a,232,a,132\n"
"MA v,180,a,236,a,148\n"
"buffer_load_dword v235, v198, s[36:39], s90 offen\n"
"MA v,176,a,236,a,144\n"
"MA v,176,a,232,a,128\n"
"s_waitcnt lgkmcnt(4)\n"
"buffer_load_dwordx4 a[116:119], v249, s[16:19], s87 offen offset:1024 sc1\n"
"s_sub_u32 s7, s7, 1\n"
"s_cmp_lg_u32 s7, 0\n"
"s_cbranch_scc1 L_LOOPP_%=\n"
"MA a,0,a,192,a,128\n"
"TR 216,255,6176\n"
"MA a,0,a,196,a,144\n"
"TR 218,255,6944\n"
"TR 220,255,9248\n"
"MA a,4,a,196,a,148\n"
"TR 222,255,10016\n"
"MA a,4,a,192,a,132\n"
"MA a,8,a,192,a,136\n"
"MA a,8,a,196,a,152\n"
"buffer_load_dwordx4 a[120:123], v249, s[16:19], s87 offen offset:2048 sc1\n"
"MA a,12,a,196,a,156\n"
"MA a,12,a,192,a,140\n"
"MA a,28,a,200,a,140\n"
"TR 224,255,64\n"
"TR 226,255,832\n"
"MA a,28,a,204,a,156\n"
"s_add_u32 s1, s0, s44\n"
"TR 228,255,3136\n"
"TR 230,255,3904\n"
"MA a,24,a,204,a,152\n"
"s_add_u32 s2, s0, s45\n"
"s_add_u32 s0, s0, 36864\n"
"s_cmp_eq_u32 s0, 110592\n"
"s_cselect_b32 s0, 0, s0\n"
"MA a,24,a,200,a,136\n"
"v_add_u32 v252, s0, v250\n"
"v_add_u32 v253, s0, v251\n"
"MA a,20,a,200,a,132\n"
"buffer_load_dwordx4 a[124:127], v249, s[16:19], s87 offen offset:3072 sc1\n"
"MA a,20,a,204,a,148\n"
"MA a,16,a,204,a,144\n"
"MA a,16,a,200,a,128\n"
"s_waitcnt lgkmcnt(4)\n"
"MA a,32,a,208,a,128\n"
"TR 232,255,6208\n"
"TR 234,255,6976\n"
"MA a,32,a,212,a,144\n"
"TR 236,255,9280\n"
"TR 238,255,10048\n"
"MA a,36,a,212,a,148\n"
"v_add_u32 v255, s0, v254\n"
"MA a,36,a,208,a,132\n"
"MA a,40,a,208,a,136\n"
"MA a,40,a,212,a,152\n"
"MA a,44,a,212,a,156\n"
"MA a,44,a,208,a,140\n"
"s_waitcnt vmcnt(31)\n"
"s_barrier\n"
"MA a,60,a,216,a,140\n"
"TR 192,255,0\n"
"TR 194,255,768\n"
"MA a,60,a,220,a,156\n"
"TR 196,255,3072\n"
"TR 198,255,3840\n"
"MA a,56,a,220,a,152\n"
"MA a,56,a,216,a,136\n"
"MA a,52,a,216,a,132\n"
"MA a,52,a,220,a,148\n"
"MA a,48,a,220,a,144\n"
"MA a,48,a,216,a,128\n"
"s_waitcnt lgkmcnt(4)\n"
"MA a,64,a,224,a,128\n"
"TR 200,255,6144\n"
"TR 202,255,6912\n"
"MA a,64,a,228,a,144\n"
"TR 204,255,9216\n"
"TR 206,255,9984\n"
"MA a,68,a,228,a,148\n"
"MA a,68,a,224,a,132\n"
"MA a,72,a,224,a,136\n"
"MA a,72,a,228,a,152\n"
"MA a,76,a,228,a,156\n"
"MA a,76,a,224,a,140\n"
"MA a,92,a,232,a,140\n"
"TR 208,255,32\n"
"TR 210,255,800\n"
"MA a,92,a,236,a,156\n"
"TR 212,255,3104\n"
"TR 214,255,3872\n"
"MA a,88,a,236,a,152\n"
"MA a,88,a,232,a,136\n"
"MA a,84,a,232,a,132\n"
"MA a,84,a,236,a,148\n"
"MA a,80,a,236,a,144\n"
"MA a,80,a,232,a,128\n"
"s_waitcnt vmcnt(30) lgkmcnt(4)\n"
"MA a,0,a,192,a,160\n"
"s_waitcnt vmcnt(25)\n"
"TR 216,255,6176\n"
"TR 218,255,6944\n"
"MA a,0,a,196,a,176\n"
"s_waitcnt vmcnt(22)\n"
"TR 220,255,9248\n"
"TR 222,255,10016\n"
"MA a,4,a,196,a,180\n"
"MA a,4,a,192,a,164\n"
"MA a,8,a,192,a,168\n"
"s_waitcnt vmcnt(21)\n"
"MA a,8,a,196,a,184\n"
"s_waitcnt vmcnt(20)\n"
"MA a,12,a,196,a,188\n"
"MA a,12,a,192,a,172\n"
"MA a,28,a,200,a,172\n"
"TR 224,255,64\n"
"TR 226,255,832\n"
"MA a,28,a,204,a,188\n"
"s_lshl_b32 s91, s44, 2\n"
"s_add_u32 s91, s91, L_JOIN%=-.Lfused_text0\n"
"s_cmp_eq_u32 s53, 2\n"
"s_cselect_b32 s91, s91, s55\n"
"TR 228,255,3136\n"
"TR 230,255,3904\n"
"MA a,24,a,204,a,184\n"
"MA a,24,a,200,a,168\n"
"MA a,20,a,200,a,164\n"
"buffer_load_dword v235, v232, s[96:99], s91 offen\n"
"MA a,20,a,204,a,180\n"
"MA a,16,a,204,a,176\n"
"MA a,16,a,200,a,160\n"
"s_waitcnt lgkmcnt(4)\n"
"MA a,32,a,208,a,160\n"
"TR 232,255,6208\n"
"TR 234,255,6976\n"
"MA a,32,a,212,a,176\n"
"TR 236,255,9280\n"
"TR 238,255,10048\n"
"MA a,36,a,212,a,180\n"
"v_mov_b32 v255, s55\n"
"MA a,36,a,208,a,164\n"
"s_add_u32 m0, s44, 114688\n"
"s_mov_b32 s86, 0\n"
"v_cndmask_b32 v255, v255, v232, vcc\n"
"MA a,40,a,208,a,168\n"
"MA a,40,a,212,a,184\n"
"MA a,44,a,212,a,188\n"
"buffer_load_dwordx4 v248, s[20:23], s86 offen lds\n"
"MA a,44,a,208,a,172\n"
"s_waitcnt vmcnt(13)\n"
"s_barrier\n"
"MA a,60,a,216,a,172\n"
"s_mov_b32 s87, 2048\n"
"TR 192,254,0\n"
"MA a,60,a,220,a,188\n"
"TR 194,254,768\n"
"TR 196,254,3072\n"
"MA a,56,a,220,a,184\n"
"TR 198,254,3840\n"
"MA a,56,a,216,a,168\n"
"buffer_load_dwordx4 v248, s[20:23], s87 offen offset:1024 lds\n"
"MA a,52,a,216,a,164\n"
"MA a,52,a,220,a,180\n"
"s_add_u32 m0, s44, 122880\n"
"s_mov_b32 s88, 128\n"
"MA a,48,a,220,a,176\n"
"MA a,48,a,216,a,160\n"
"s_waitcnt lgkmcnt(4)\n"
"MA a,64,a,224,a,160\n"
"buffer_load_dwordx4 v248, s[20:23], s88 offen lds\n"
"MA a,64,a,228,a,176\n"
"TR 200,254,6144\n"
"MA a,68,a,228,a,180\n"
"s_mov_b32 s89, 2176\n"
"TR 202,254,6912\n"
"TR 204,254,9216\n"
"MA a,68,a,224,a,164\n"
"TR 206,254,9984\n"
"MA a,72,a,224,a,168\n"
"MA a,72,a,228,a,184\n"
"buffer_load_dwordx4 v248, s[20:23], s89 offen offset:1024 lds\n"
"MA a,76,a,228,a,188\n"
"MA a,76,a,224,a,172\n"
"s_add_u32 m0, s44, 131072\n"
"MA a,92,a,232,a,172\n"
"s_mov_b32 s90, 256\n"
"TR 208,254,32\n"
"MA a,92,a,236,a,188\n"
"TR 210,254,800\n"
"TR 212,254,3104\n"
"MA a,88,a,236,a,184\n"
"buffer_load_dwordx4 v248, s[20:23], s90 offen lds\n"
"MA a,88,a,232,a,168\n"
"TR 214,254,3872\n"
"MA a,84,a,232,a,164\n"
"s_mov_b32 s91, 2304\n"
"MA a,84,a,236,a,180\n"
"MA a,80,a,236,a,176\n"
"MA a,80,a,232,a,160\n"
"s_waitcnt vmcnt(16) lgkmcnt(4)\n"
"buffer_load_dwordx4 v248, s[20:23], s91 offen offset:1024 lds\n"
"MA a,0,a,192,a,96\n"
"s_waitcnt vmcnt(12)\n"
"TR 216,254,6176\n"
"MA a,0,a,196,a,112\n"
"s_waitcnt vmcnt(9)\n"
"s_add_u32 m0, s44, 139264\n"
"TR 218,254,6944\n"
"MA a,4,a,196,a,116\n"
"s_mov_b32 s86, 24576\n"
"TR 220,254,9248\n"
"MA a,4,a,192,a,100\n"
"TR 222,254,10016\n"
"MA a,8,a,192,a,104\n"
"s_waitcnt vmcnt(8)\n"
"buffer_load_dwordx4 v248, s[20:23], s86 offen lds\n"
"MA a,8,a,196,a,120\n"
"s_waitcnt vmcnt(8)\n"
"v_accvgpr_read_b32 v216, a0\n"
"MA a,12,a,196,a,124\n"
"s_mov_b32 s87, 26624\n"
"v_accvgpr_read_b32 v217, a1\n"
"MA a,12,a,192,a,108\n"
"v_accvgpr_read_b32 v218, a2\n"
"v_accvgpr_read_b32 v219, a3\n"
"MA a,28,a,200,a,108\n"
"TR 224,254,64\n"
"TR 226,254,832\n"
"MA a,28,a,204,a,124\n"
"buffer_load_dwordx4 v248, s[20:23], s87 offen offset:1024 lds\n"
"MA a,24,a,204,a,120\n"
"TR 228,254,3136\n"
"MA a,24,a,200,a,104\n"
"s_add_u32 m0, s44, 147456\n"
"s_mov_b32 s88, 24704\n"
"TR 230,254,3904\n"
"MA a,20,a,200,a,100\n"
"v_accvgpr_read_b32 v224, a4\n"
"v_accvgpr_read_b32 v225, a5\n"
"MA a,20,a,204,a,116\n"
"v_accvgpr_read_b32 v226, a6\n"
"v_accvgpr_read_b32 v227, a7\n"
"MA a,16,a,204,a,112\n"
"buffer_load_dwordx4 v248, s[20:23], s88 offen lds\n"
"MA a,16,a,200,a,96\n"
"s_waitcnt lgkmcnt(4)\n"
"MA a,32,a,208,a,96\n"
"s_mov_b32 s89, 26752\n"
"TR 232,254,6208\n"
"MA a,32,a,212,a,112\n"
"TR 234,254,6976\n"
"TR 236,254,9280\n"
"MA a,36,a,212,a,116\n"
"TR 238,254,10048\n"
"v_accvgpr_read_b32 v220, a16\n"
"MA a,36,a,208,a,100\n"
"buffer_load_dwordx4 v248, s[20:23], s89 offen offset:1024 lds\n"
"MA a,40,a,208,a,104\n"
"v_accvgpr_read_b32 v221, a17\n"
"MA a,40,a,212,a,120\n"
"s_add_u32 m0, s44, 155648\n"
"s_mov_b32 s90, 24832\n"
"v_accvgpr_read_b32 v222, a18\n"
"MA a,44,a,212,a,124\n"
"v_accvgpr_read_b32 v223, a19\n"
"v_mul_f32 v216, v216, v220\n"
"MA a,44,a,208,a,108\n"
"v_mul_f32 v217, v217, v221\n"
"v_mul_f32 v218, v218, v222\n"
"MA a,60,a,216,a,108\n"
"buffer_load_dwordx4 v248, s[20:23], s90 offen lds\n"
"MA a,60,a,220,a,124\n"
"TR 192,252,0\n"
"MA a,56,a,220,a,120\n"
"s_mov_b32 s91, 26880\n"
"TR 194,252,1024\n"
"TR 196,252,4096\n"
"MA a,56,a,216,a,104\n"
"TR 198,252,5120\n"
"v_mul_f32 v219, v219, v223\n"
"MA a,52,a,216,a,100\n"
"v_accvgpr_read_b32 v220, a20\n"
"v_accvgpr_read_b32 v221, a21\n"
"MA a,52,a,220,a,116\n"
"buffer_load_dwordx4 v248, s[20:23], s91 offen offset:1024 lds\n"
"MA a,48,a,220,a,112\n"
"v_accvgpr_read_b32 v222, a22\n"
"MA a,48,a,216,a,96\n"
"s_waitcnt lgkmcnt(4)\n"
"v_cvt_pk_bf16_f32 v192, v216, v217\n"
"v_accvgpr_read_b32 v223, a23\n"
"MA a,64,a,224,a,96\n"
"TR 200,252,8\n"
"TR 202,252,1032\n"
"MA a,64,a,228,a,112\n"
"TR 204,252,4104\n"
"TR 206,252,5128\n"
"MA a,68,a,228,a,116\n"
"v_cvt_pk_bf16_f32 v193, v218, v219\n"
"v_mul_f32 v224, v224, v220\n"
"MA a,68,a,224,a,100\n"
"v_mul_f32 v225, v225, v221\n"
"v_mul_f32 v226, v226, v222\n"
"MA a,72,a,224,a,104\n"
"v_mul_f32 v227, v227, v223\n"
"v_accvgpr_read_b32 v216, a8\n"
"MA a,72,a,228,a,120\n"
"v_accvgpr_read_b32 v217, a9\n"
"v_accvgpr_read_b32 v218, a10\n"
"MA a,76,a,228,a,124\n"
"v_accvgpr_read_b32 v219, a11\n"
"v_accvgpr_read_b32 v220, a24\n"
"MA a,76,a,224,a,108\n"
"v_accvgpr_read_b32 v221, a25\n"
"v_accvgpr_read_b32 v222, a26\n"
"MA a,92,a,232,a,108\n"
"TR 208,253,0\n"
"TR 210,253,1024\n"
"MA a,92,a,236,a,124\n"
"TR 212,253,4096\n"
"TR 214,253,5120\n"
"MA a,88,a,236,a,120\n"
"v_cvt_pk_bf16_f32 v196, v224, v225\n"
"v_accvgpr_read_b32 v223, a27\n"
"MA a,88,a,232,a,104\n"
"v_cvt_pk_bf16_f32 v197, v226, v227\n"
"v_mul_f32 v216, v216, v220\n"
"MA a,84,a,232,a,100\n"
"v_mul_f32 v217, v217, v221\n"
"v_mul_f32 v218, v218, v222\n"
"MA a,84,a,236,a,116\n"
"v_mul_f32 v219, v219, v223\n"
"v_accvgpr_read_b32 v224, a12\n"
"MA a,80,a,236,a,112\n"
"v_accvgpr_read_b32 v225, a13\n"
"v_accvgpr_read_b32 v226, a14\n"
"MA a,80,a,232,a,96\n"
"s_waitcnt lgkmcnt(4)\n"
"v_accvgpr_read_b32 v227, a15\n"
"v_accvgpr_read_b32 v220, a28\n"
"MA v,0,a,192,a,160\n"
"TR 216,253,8\n"
"TR 218,253,1032\n"
"MA v,0,a,196,a,176\n"
"TR 220,253,4104\n"
"TR 222,253,5128\n"
"MA v,4,a,196,a,180\n"
"v_accvgpr_read_b32 v221, a29\n"
"v_accvgpr_read_b32 v222, a30\n"
"MA v,4,a,192,a,164\n"
"v_cvt_pk_bf16_f32 v200, v216, v217\n"
"v_accvgpr_read_b32 v223, a31\n"
"MA v,8,a,192,a,168\n"
"v_cvt_pk_bf16_f32 v201, v218, v219\n"
"v_mul_f32 v224, v224, v220\n"
"MA v,8,a,196,a,184\n"
"v_mul_f32 v225, v225, v221\n"
"v_mul_f32 v226, v226, v222\n"
"MA v,12,a,196,a,188\n"
"v_mul_f32 v227, v227, v223\n"
"v_accvgpr_read_b32 v216, a32\n"
"MA v,12,a,192,a,172\n"
"v_accvgpr_read_b32 v217, a33\n"
"v_accvgpr_read_b32 v218, a34\n"
"MA v,28,a,200,a,172\n"
"TR 224,252,8192\n"
"TR 226,252,9216\n"
"MA v,28,a,204,a,188\n"
"TR 228,252,12288\n"
"TR 230,252,13312\n"
"MA v,24,a,204,a,184\n"
"v_accvgpr_read_b32 v219, a35\n"
"v_accvgpr_read_b32 v220, a48\n"
"MA v,24,a,200,a,168\n"
"v_accvgpr_read_b32 v221, a49\n"
"v_accvgpr_read_b32 v222, a50\n"
"MA v,20,a,200,a,164\n"
"v_cvt_pk_bf16_f32 v204, v224, v225\n"
"v_accvgpr_read_b32 v223, a51\n"
"MA v,20,a,204,a,180\n"
"v_cvt_pk_bf16_f32 v205, v226, v227\n"
"v_mul_f32 v216, v216, v220\n"
"MA v,16,a,204,a,176\n"
"v_mul_f32 v217, v217, v221\n"
"v_mul_f32 v218, v218, v222\n"
"MA v,16,a,200,a,160\n"
"s_waitcnt lgkmcnt(4)\n"
"v_mul_f32 v219, v219, v223\n"
"v_accvgpr_read_b32 v224, a36\n"
"MA v,32,a,208,a,160\n"
"TR 232,252,8200\n"
"TR 234,252,9224\n"
"MA v,32,a,212,a,176\n"
"TR 236,252,12296\n"
"TR 238,252,13320\n"
"MA v,36,a,212,a,180\n"
"v_accvgpr_read_b32 v225, a37\n"
"v_accvgpr_read_b32 v226, a38\n"
"MA v,36,a,208,a,164\n"
"v_accvgpr_read_b32 v227, a39\n"
"v_accvgpr_read_b32 v220, a52\n"
"MA v,40,a,208,a,168\n"
"v_accvgpr_read_b32 v221, a53\n"
"v_accvgpr_read_b32 v222, a54\n"
"MA v,40,a,212,a,184\n"
"v_cvt_pk_bf16_f32 v194, v216, v217\n"
"v_accvgpr_read_b32 v223, a55\n"
"MA v,44,a,212,a,188\n"
"v_cvt_pk_bf16_f32 v195, v218, v219\n"
"v_mul_f32 v224, v224, v220\n"
"MA v,44,a,208,a,172\n"
"v_mul_f32 v225, v225, v221\n"
"v_mul_f32 v226, v226, v222\n"
"MA v,60,a,216,a,172\n"
"TR 192,253,8192\n"
"TR 194,253,9216\n"
"MA v,60,a,220,a,188\n"
"TR 196,253,12288\n"
"TR 198,253,13312\n"
"MA v,56,a,220,a,184\n"
"v_mul_f32 v227, v227, v223\n"
"v_accvgpr_read_b32 v216, a40\n"
"MA v,56,a,216,a,168\n"
"v_accvgpr_read_b32 v217, a41\n"
"v_accvgpr_read_b32 v218, a42\n"
"MA v,52,a,216,a,164\n"
"v_accvgpr_read_b32 v219, a43\n"
"v_accvgpr_read_b32 v220, a56\n"
"MA v,52,a,220,a,180\n"
"v_accvgpr_read_b32 v221, a57\n"
"v_accvgpr_read_b32 v222, a58\n"
"MA v,48,a,220,a,176\n"
"v_cvt_pk_bf16_f32 v198, v224, v225\n"
"v_accvgpr_read_b32 v223, a59\n"
"MA v,48,a,216,a,160\n"
"s_waitcnt lgkmcnt(4)\n"
"v_cvt_pk_bf16_f32 v199, v226, v227\n"
"v_mul_f32 v216, v216, v220\n"
"MA v,64,a,224,a,160\n"
"TR 200,253,8200\n"
"TR 202,253,9224\n"
"MA v,64,a,228,a,176\n"
"TR 204,253,12296\n"
"TR 206,253,13320\n"
"MA v,68,a,228,a,180\n"
"v_mul_f32 v217, v217, v221\n"
"v_mul_f32 v218, v218, v222\n"
"MA v,68,a,224,a,164\n"
"v_mul_f32 v219, v219, v223\n"
"v_accvgpr_read_b32 v224, a44\n"
"MA v,72,a,224,a,168\n"
"v_accvgpr_read_b32 v225, a45\n"
"v_accvgpr_read_b32 v226, a46\n"
"MA v,72,a,228,a,184\n"
"v_accvgpr_read_b32 v227, a47\n"
"v_accvgpr_read_b32 v220, a60\n"
"MA v,76,a,228,a,188\n"
"v_accvgpr_read_b32 v221, a61\n"
"v_accvgpr_read_b32 v222, a62\n"
"MA v,76,a,224,a,172\n"
"v_accvgpr_read_b32 v223, a63\n"
"v_mul_f32 v224, v224, v220\n"
"MA v,92,a,232,a,172\n"
"TR 208,252,16384\n"
"TR 210,252,17408\n"
"MA v,92,a,236,a,188\n"
"TR 212,252,20480\n"
"TR 214,252,21504\n"
"MA v,88,a,236,a,184\n"
"v_mul_f32 v225, v225, v221\n"
"v_mul_f32 v226, v226, v222\n"
"MA v,88,a,232,a,168\n"
"v_cvt_pk_bf16_f32 v202, v216, v217\n"
"v_mul_f32 v227, v227, v223\n"
"MA v,84,a,232,a,164\n"
"v_cvt_pk_bf16_f32 v203, v218, v219\n"
"v_cvt_pk_bf16_f32 v206, v224, v225\n"
"MA v,84,a,236,a,180\n"
"v_cvt_pk_bf16_f32 v207, v226, v227\n"
"v_accvgpr_read_b32 v216, a64\n"
"MA v,80,a,236,a,176\n"
"buffer_store_dwordx4 v[192:195], v249, s[16:19], s48 offen sc1\n"
"MA v,80,a,232,a,160\n"
"s_waitcnt lgkmcnt(4)\n"
"v_accvgpr_read_b32 v217, a65\n"
"MA v,96,a,192,a,160\n"
"TR 216,252,16392\n"
"TR 218,252,17416\n"
"MA v,96,a,196,a,176\n"
"TR 220,252,20488\n"
"TR 222,252,21512\n"
"MA v,100,a,196,a,180\n"
"buffer_store_dwordx4 v[196:199], v249, s[16:19], s48 offen offset:1024 sc1\n"
"MA v,100,a,192,a,164\n"
"v_accvgpr_read_b32 v218, a66\n"
"MA v,104,a,192,a,168\n"
"buffer_store_dwordx4 v[200:203], v249, s[16:19], s48 offen offset:2048 sc1\n"
"MA v,104,a,196,a,184\n"
"v_accvgpr_read_b32 v219, a67\n"
"MA v,108,a,196,a,188\n"
"buffer_store_dwordx4 v[204:207], v249, s[16:19], s48 offen offset:3072 sc1\n"
"MA v,108,a,192,a,172\n"
"v_accvgpr_read_b32 v220, a80\n"
"MA v,124,a,200,a,172\n"
"TR 224,253,16384\n"
"TR 226,253,17408\n"
"MA v,124,a,204,a,188\n"
"TR 228,253,20480\n"
"TR 230,253,21504\n"
"MA v,120,a,204,a,184\n"
"v_accvgpr_read_b32 v221, a81\n"
"v_accvgpr_read_b32 v222, a82\n"
"MA v,120,a,200,a,168\n"
"v_accvgpr_read_b32 v223, a83\n"
"v_mul_f32 v216, v216, v220\n"
"MA v,116,a,200,a,164\n"
"v_mul_f32 v217, v217, v221\n"
"v_mul_f32 v218, v218, v222\n"
"MA v,116,a,204,a,180\n"
"v_mul_f32 v219, v219, v223\n"
"v_accvgpr_read_b32 v224, a68\n"
"MA v,112,a,204,a,176\n"
"v_accvgpr_read_b32 v225, a69\n"
"v_accvgpr_read_b32 v226, a70\n"
"MA v,112,a,200,a,160\n"
"s_waitcnt lgkmcnt(4)\n"
"v_accvgpr_read_b32 v227, a71\n"
"v_accvgpr_read_b32 v220, a84\n"
"MA v,128,a,208,a,160\n"
"TR 232,253,16392\n"
"TR 234,253,17416\n"
"MA v,128,a,212,a,176\n"
"TR 236,253,20488\n"
"TR 238,253,21512\n"
"MA v,132,a,212,a,180\n"
"v_accvgpr_read_b32 v221, a85\n"
"v_accvgpr_read_b32 v222, a86\n"
"MA v,132,a,208,a,164\n"
"v_cvt_pk_bf16_f32 v208, v216, v217\n"
"v_accvgpr_read_b32 v223, a87\n"
"MA v,136,a,208,a,168\n"
"v_cvt_pk_bf16_f32 v209, v218, v219\n"
"v_mul_f32 v224, v224, v220\n"
"MA v,136,a,212,a,184\n"
"v_mul_f32 v225, v225, v221\n"
"v_mul_f32 v226, v226, v222\n"
"MA v,140,a,212,a,188\n"
"v_mul_f32 v227, v227, v223\n"
"v_accvgpr_read_b32 v216, a72\n"
"MA v,140,a,208,a,172\n"
"v_accvgpr_read_b32 v217, a73\n"
"v_accvgpr_read_b32 v218, a74\n"
"MA v,156,a,216,a,172\n"
"TR 192,250,0\n"
"TR 194,250,1024\n"
"MA v,156,a,220,a,188\n"
"TR 196,250,4096\n"
"TR 198,250,5120\n"
"MA v,152,a,220,a,184\n"
"v_accvgpr_read_b32 v219, a75\n"
"v_accvgpr_read_b32 v220, a88\n"
"MA v,152,a,216,a,168\n"
"v_accvgpr_read_b32 v221, a89\n"
"v_accvgpr_read_b32 v222, a90\n"
"MA v,148,a,216,a,164\n"
"v_cvt_pk_bf16_f32 v210, v224, v225\n"
"v_accvgpr_read_b32 v223, a91\n"
"MA v,148,a,220,a,180\n"
"v_cvt_pk_bf16_f32 v211, v226, v227\n"
"v_mul_f32 v216, v216, v220\n"
"MA v,144,a,220,a,176\n"
"v_mul_f32 v217, v217, v221\n"
"v_mul_f32 v218, v218, v222\n"
"MA v,144,a,216,a,160\n"
"s_waitcnt lgkmcnt(4)\n"
"v_mul_f32 v219, v219, v223\n"
"v_accvgpr_read_b32 v224, a76\n"
"MA v,160,a,224,a,160\n"
"TR 200,250,8\n"
"TR 202,250,1032\n"
"MA v,160,a,228,a,176\n"
"TR 204,250,4104\n"
"TR 206,250,5128\n"
"MA v,164,a,228,a,180\n"
"v_accvgpr_read_b32 v225, a77\n"
"v_accvgpr_read_b32 v226, a78\n"
"MA v,164,a,224,a,164\n"
"v_accvgpr_read_b32 v227, a79\n"
"v_accvgpr_read_b32 v220, a92\n"
"MA v,168,a,224,a,168\n"
"v_accvgpr_read_b32 v221, a93\n"
"v_accvgpr_read_b32 v222, a94\n"
"MA v,168,a,228,a,184\n"
"v_accvgpr_read_b32 v223, a95\n"
"v_mul_f32 v224, v224, v220\n"
"MA v,172,a,228,a,188\n"
"v_mul_f32 v225, v225, v221\n"
"v_mul_f32 v226, v226, v222\n"
"MA v,172,a,224,a,172\n"
"v_cvt_pk_bf16_f32 v212, v216, v217\n"
"v_mul_f32 v227, v227, v223\n"
"MA v,188,a,232,a,172\n"
"TR 208,251,0\n"
"TR 210,251,1024\n"
"MA v,188,a,236,a,188\n"
"TR 212,251,4096\n"
"TR 214,251,5120\n"
"MA v,184,a,236,a,184\n"
"v_cvt_pk_bf16_f32 v213, v218, v219\n"
"v_cvt_pk_bf16_f32 v214, v224, v225\n"
"MA v,184,a,232,a,168\n"
"v_cvt_pk_bf16_f32 v215, v226, v227\n"
"v_mov_b32 v252, 0x3f803f80\n"
"MA v,180,a,232,a,164\n"
"buffer_store_dwordx2 v[208:209], v249, s[16:19], s49 offen sc1\n"
"MA v,180,a,236,a,180\n"
"v_mov_b32 v253, 0x3f803f80\n"
"MA v,176,a,236,a,176\n"
"buffer_store_dwordx2 v[210:211], v249, s[16:19], s49 offen offset:1024 sc1\n"
"MA v,176,a,232,a,160\n"
"s_waitcnt lgkmcnt(4)\n"
"MA v,0,a,192,a,96\n"
"TR 216,251,8\n"
"TR 218,251,1032\n"
"MA v,0,a,196,a,112\n"
"TR 220,251,4104\n"
"TR 222,251,5128\n"
"MA v,4,a,196,a,116\n"
"buffer_store_dwordx2 v[212:213], v249, s[16:19], s49 offen offset:2048 sc1\n"
"MA v,4,a,192,a,100\n"
"MA v,8,a,192,a,104\n"
"s_nop 1\n"
"v_cvt_pk_bf16_f32 v0, v0, v1\n"
"v_cvt_pk_bf16_f32 v1, v2, v3\n"
"MA v,8,a,196,a,120\n"
"v_accvgpr_write_b32 a0, v0\n"
"v_accvgpr_write_b32 a1, v1\n"
"MA v,12,a,196,a,124\n"
"v_cvt_pk_bf16_f32 v4, v4, v5\n"
"v_cvt_pk_bf16_f32 v5, v6, v7\n"
"MA v,12,a,192,a,108\n"
"v_accvgpr_write_b32 a4, v4\n"
"v_accvgpr_write_b32 a5, v5\n"
"MA v,28,a,200,a,108\n"
"TR 224,250,8192\n"
"TR 226,250,9216\n"
"MA v,28,a,204,a,124\n"
"TR 228,250,12288\n"
"TR 230,250,13312\n"
"MA v,24,a,204,a,120\n"
"v_cvt_pk_bf16_f32 v8, v8, v9\n"
"v_cvt_pk_bf16_f32 v9, v10, v11\n"
"MA v,24,a,200,a,104\n"
"v_accvgpr_write_b32 a8, v8\n"
"v_accvgpr_write_b32 a9, v9\n"
"MA v,20,a,200,a,100\n"
"v_cvt_pk_bf16_f32 v12, v12, v13\n"
"v_cvt_pk_bf16_f32 v13, v14, v15\n"
"MA v,20,a,204,a,116\n"
"v_cvt_pk_bf16_f32 v14, v28, v29\n"
"v_cvt_pk_bf16_f32 v15, v30, v31\n"
"MA v,16,a,204,a,112\n"
"v_cvt_pk_bf16_f32 v10, v24, v25\n"
"v_cvt_pk_bf16_f32 v11, v26, v27\n"
"MA v,16,a,200,a,96\n"
"s_waitcnt lgkmcnt(4)\n"
"v_accvgpr_write_b32 a10, v10\n"
"v_accvgpr_write_b32 a11, v11\n"
"MA v,32,a,208,a,96\n"
"TR 232,250,8200\n"
"TR 234,250,9224\n"
"MA v,32,a,212,a,112\n"
"TR 236,250,12296\n"
"TR 238,250,13320\n"
"MA v,36,a,212,a,116\n"
"v_cvt_pk_bf16_f32 v2, v16, v17\n"
"v_cvt_pk_bf16_f32 v3, v18, v19\n"
"MA v,36,a,208,a,100\n"
"v_accvgpr_write_b32 a2, v2\n"
"v_accvgpr_write_b32 a3, v3\n"
"MA v,40,a,208,a,104\n"
"v_cvt_pk_bf16_f32 v32, v32, v33\n"
"v_cvt_pk_bf16_f32 v33, v34, v35\n"
"MA v,40,a,212,a,120\n"
"v_cvt_pk_bf16_f32 v6, v20, v21\n"
"v_cvt_pk_bf16_f32 v7, v22, v23\n"
"MA v,44,a,212,a,124\n"
"v_accvgpr_write_b32 a16, v32\n"
"v_accvgpr_write_b32 a17, v33\n"
"MA v,44,a,208,a,108\n"
"v_cvt_pk_bf16_f32 v36, v36, v37\n"
"v_cvt_pk_bf16_f32 v37, v38, v39\n"
"MA v,60,a,216,a,108\n"
"TR 192,251,8192\n"
"TR 194,251,9216\n"
"MA v,60,a,220,a,124\n"
"TR 196,251,12288\n"
"TR 198,251,13312\n"
"MA v,56,a,220,a,120\n"
"v_accvgpr_write_b32 a6, v6\n"
"v_accvgpr_write_b32 a7, v7\n"
"MA v,56,a,216,a,104\n"
"v_accvgpr_write_b32 a20, v36\n"
"v_accvgpr_write_b32 a21, v37\n"
"MA v,52,a,216,a,100\n"
"v_cvt_pk_bf16_f32 v40, v40, v41\n"
"v_cvt_pk_bf16_f32 v41, v42, v43\n"
"MA v,52,a,220,a,116\n"
"v_accvgpr_write_b32 a24, v40\n"
"v_accvgpr_write_b32 a25, v41\n"
"MA v,48,a,220,a,112\n"
"v_cvt_pk_bf16_f32 v42, v56, v57\n"
"v_cvt_pk_bf16_f32 v43, v58, v59\n"
"MA v,48,a,216,a,96\n"
"s_waitcnt lgkmcnt(4)\n"
"v_accvgpr_write_b32 a26, v42\n"
"v_accvgpr_write_b32 a27, v43\n"
"MA v,64,a,224,a,96\n"
"TR 200,251,8200\n"
"TR 202,251,9224\n"
"MA v,64,a,228,a,112\n"
"TR 204,251,12296\n"
"TR 206,251,13320\n"
"MA v,68,a,228,a,116\n"
"v_cvt_pk_bf16_f32 v34, v48, v49\n"
"v_cvt_pk_bf16_f32 v35, v50, v51\n"
"MA v,68,a,224,a,100\n"
"v_accvgpr_write_b32 a18, v34\n"
"v_accvgpr_write_b32 a19, v35\n"
"MA v,72,a,224,a,104\n"
"v_cvt_pk_bf16_f32 v38, v52, v53\n"
"v_cvt_pk_bf16_f32 v39, v54, v55\n"
"MA v,72,a,228,a,120\n"
"v_accvgpr_write_b32 a22, v38\n"
"v_accvgpr_write_b32 a23, v39\n"
"MA v,76,a,228,a,124\n"
"v_cvt_pk_bf16_f32 v44, v44, v45\n"
"v_cvt_pk_bf16_f32 v45, v46, v47\n"
"MA v,76,a,224,a,108\n"
"v_cvt_pk_bf16_f32 v46, v60, v61\n"
"v_cvt_pk_bf16_f32 v47, v62, v63\n"
"MA v,92,a,232,a,108\n"
"TR 208,250,16384\n"
"TR 210,250,17408\n"
"MA v,92,a,236,a,124\n"
"TR 212,250,20480\n"
"TR 214,250,21504\n"
"MA v,88,a,236,a,120\n"
"v_accvgpr_write_b32 a12, v12\n"
"v_accvgpr_write_b32 a13, v13\n"
"MA v,88,a,232,a,104\n"
"v_accvgpr_write_b32 a14, v14\n"
"v_accvgpr_write_b32 a15, v15\n"
"MA v,84,a,232,a,100\n"
"v_accvgpr_write_b32 a28, v44\n"
"v_accvgpr_write_b32 a29, v45\n"
"MA v,84,a,236,a,116\n"
"v_accvgpr_write_b32 a30, v46\n"
"v_accvgpr_write_b32 a31, v47\n"
"MA v,80,a,236,a,112\n"
"buffer_store_dwordx2 v[214:215], v249, s[16:19], s49 offen offset:3072 sc1\n"
"MA v,80,a,232,a,96\n"
"s_waitcnt lgkmcnt(4)\n"
"v_cvt_pk_bf16_f32 v64, v64, v65\n"
"MA v,96,a,192,a,96\n"
"TR 216,250,16392\n"
"TR 218,250,17416\n"
"MA v,96,a,196,a,112\n"
"TR 220,250,20488\n"
"TR 222,250,21512\n"
"MA v,100,a,196,a,116\n"
"v_cvt_pk_bf16_f32 v65, v66, v67\n"
"v_cvt_pk_bf16_f32 v66, v80, v81\n"
"MA v,100,a,192,a,100\n"
"v_cvt_pk_bf16_f32 v67, v82, v83\n"
"v_accvgpr_write_b32 a32, v64\n"
"MA v,104,a,192,a,104\n"
"v_accvgpr_write_b32 a33, v65\n"
"v_accvgpr_write_b32 a34, v66\n"
"MA v,104,a,196,a,120\n"
"v_accvgpr_write_b32 a35, v67\n"
"v_cvt_pk_bf16_f32 v96, v96, v97\n"
"MA v,108,a,196,a,124\n"
"v_cvt_pk_bf16_f32 v97, v98, v99\n"
"v_accvgpr_write_b32 a48, v96\n"
"MA v,108,a,192,a,108\n"
"v_accvgpr_write_b32 a49, v97\n"
"v_cvt_pk_bf16_f32 v100, v100, v101\n"
"MA v,124,a,200,a,108\n"
"TR 224,251,16384\n"
"TR 226,251,17408\n"
"MA v,124,a,204,a,124\n"
"TR 228,251,20480\n"
"TR 230,251,21504\n"
"MA v,120,a,204,a,120\n"
"v_cvt_pk_bf16_f32 v68, v68, v69\n"
"v_cvt_pk_bf16_f32 v101, v102, v103\n"
"MA v,120,a,200,a,104\n"
"v_cvt_pk_bf16_f32 v69, v70, v71\n"
"v_cvt_pk_bf16_f32 v70, v84, v85\n"
"MA v,116,a,200,a,100\n"
"v_cvt_pk_bf16_f32 v71, v86, v87\n"
"v_accvgpr_write_b32 a52, v100\n"
"MA v,116,a,204,a,116\n"
"v_accvgpr_write_b32 a36, v68\n"
"v_accvgpr_write_b32 a53, v101\n"
"MA v,112,a,204,a,112\n"
"v_accvgpr_write_b32 a37, v69\n"
"v_accvgpr_write_b32 a38, v70\n"
"MA v,112,a,200,a,96\n"
"s_waitcnt lgkmcnt(4)\n"
"v_accvgpr_write_b32 a39, v71\n"
"v_cvt_pk_bf16_f32 v72, v72, v73\n"
"MA v,128,a,208,a,96\n"
"TR 232,251,16392\n"
"TR 234,251,17416\n"
"MA v,128,a,212,a,112\n"
"TR 236,251,20488\n"
"TR 238,251,21512\n"
"MA v,132,a,212,a,116\n"
"v_cvt_pk_bf16_f32 v98, v112, v113\n"
"v_cvt_pk_bf16_f32 v99, v114, v115\n"
"MA v,132,a,208,a,100\n"
"v_accvgpr_write_b32 a50, v98\n"
"v_accvgpr_write_b32 a51, v99\n"
"MA v,136,a,208,a,104\n"
"v_cvt_pk_bf16_f32 v102, v116, v117\n"
"v_cvt_pk_bf16_f32 v103, v118, v119\n"
"MA v,136,a,212,a,120\n"
"v_accvgpr_write_b32 a54, v102\n"
"v_accvgpr_write_b32 a55, v103\n"
"MA v,140,a,212,a,124\n"
"v_cvt_pk_bf16_f32 v73, v74, v75\n"
"v_cvt_pk_bf16_f32 v74, v88, v89\n"
"MA v,140,a,208,a,108\n"
"s_waitcnt vmcnt(8)\n"
"s_barrier\n"
"v_cvt_pk_bf16_f32 v75, v90, v91\n"
"MA v,156,a,216,a,108\n"
"s_waitcnt lgkmcnt(0)\n"
"s_barrier\n"
"ds_read_b128 a[192:195], v246 offset:0\n"
"MA v,156,a,220,a,124\n"
"ds_read_b128 a[196:199], v247 offset:0\n"
"v_accvgpr_write_b32 a40, v72\n"
"MA v,152,a,220,a,120\n"
"v_accvgpr_write_b32 a41, v73\n"
"v_accvgpr_write_b32 a42, v74\n"
"MA v,152,a,216,a,104\n"
"v_accvgpr_write_b32 a43, v75\n"
"v_cvt_pk_bf16_f32 v104, v104, v105\n"
"MA v,148,a,216,a,100\n"
"v_cvt_pk_bf16_f32 v105, v106, v107\n"
"v_cvt_pk_bf16_f32 v106, v120, v121\n"
"MA v,148,a,220,a,116\n"
"v_cvt_pk_bf16_f32 v107, v122, v123\n"
"v_accvgpr_write_b32 a56, v104\n"
"MA v,144,a,220,a,112\n"
"v_accvgpr_write_b32 a57, v105\n"
"v_accvgpr_write_b32 a58, v106\n"
"MA v,144,a,216,a,96\n"
"v_accvgpr_write_b32 a59, v107\n"
"v_cvt_pk_bf16_f32 v108, v108, v109\n"
"MA v,160,a,224,a,96\n"
"ds_read_b128 a[200:203], v246 offset:2048\n"
"v_cvt_pk_bf16_f32 v109, v110, v111\n"
"MA v,160,a,228,a,112\n"
"ds_read_b128 a[204:207], v247 offset:2048\n"
"v_cvt_pk_bf16_f32 v110, v124, v125\n"
"MA v,164,a,228,a,116\n"
"v_cvt_pk_bf16_f32 v76, v76, v77\n"
"v_cvt_pk_bf16_f32 v111, v126, v127\n"
"MA v,164,a,224,a,100\n"
"v_cvt_pk_bf16_f32 v77, v78, v79\n"
"v_cvt_pk_bf16_f32 v78, v92, v93\n"
"MA v,168,a,224,a,104\n"
"v_cvt_pk_bf16_f32 v79, v94, v95\n"
"v_accvgpr_write_b32 a60, v108\n"
"MA v,168,a,228,a,120\n"
"v_accvgpr_write_b32 a61, v109\n"
"v_accvgpr_write_b32 a62, v110\n"
"v_accvgpr_write_b32 a63, v111\n"
"MA v,172,a,228,a,124\n"
"v_accvgpr_write_b32 a44, v76\n"
"v_accvgpr_write_b32 a45, v77\n"
"MA v,172,a,224,a,108\n"
"v_accvgpr_write_b32 a46, v78\n"
"v_accvgpr_write_b32 a47, v79\n"
"MA v,188,a,232,a,108\n"
"ds_read_b128 a[208:211], v246 offset:8192\n"
"v_cvt_pk_bf16_f32 v128, v128, v129\n"
"MA v,188,a,236,a,124\n"
"ds_read_b128 a[212:215], v247 offset:8192\n"
"v_cvt_pk_bf16_f32 v129, v130, v131\n"
"MA v,184,a,236,a,120\n"
"v_cvt_pk_bf16_f32 v130, v144, v145\n"
"v_cvt_pk_bf16_f32 v131, v146, v147\n"
"MA v,184,a,232,a,104\n"
"v_accvgpr_write_b32 a64, v128\n"
"v_accvgpr_write_b32 a65, v129\n"
"MA v,180,a,232,a,100\n"
"v_accvgpr_write_b32 a66, v130\n"
"v_accvgpr_write_b32 a67, v131\n"
"MA v,180,a,236,a,116\n"
"v_cvt_pk_bf16_f32 v160, v160, v161\n"
"v_cvt_pk_bf16_f32 v161, v162, v163\n"
"MA v,176,a,236,a,112\n"
"v_accvgpr_write_b32 a80, v160\n"
"v_accvgpr_write_b32 a81, v161\n"
"MA v,176,a,232,a,96\n"
"s_waitcnt lgkmcnt(2)\n"
"v_cvt_pk_bf16_f32 v164, v164, v165\n"
"v_cvt_pk_bf16_f32 v165, v166, v167\n"
"MZ v,0,a,192,a,0\n"
"ds_read_b128 a[216:219], v246 offset:10240\n"
"v_cvt_pk_bf16_f32 v166, v180, v181\n"
"MA v,0,a,196,a,16\n"
"ds_read_b128 a[220:223], v247 offset:10240\n"
"v_cvt_pk_bf16_f32 v167, v182, v183\n"
"MZ v,4,a,196,a,20\n"
"v_cvt_pk_bf16_f32 v162, v176, v177\n"
"v_cvt_pk_bf16_f32 v163, v178, v179\n"
"MA v,4,a,192,a,4\n"
"s_add_u32 m0, s44, 65536\n"
"s_mov_b32 s86, 49152\n"
"v_accvgpr_write_b32 a82, v162\n"
"v_accvgpr_write_b32 a83, v163\n"
"MZ v,8,a,192,a,8\n"
"v_cvt_pk_bf16_f32 v132, v132, v133\n"
"v_cvt_pk_bf16_f32 v133, v134, v135\n"
"MA v,8,a,196,a,24\n"
"v_cvt_pk_bf16_f32 v134, v148, v149\n"
"v_cvt_pk_bf16_f32 v135, v150, v151\n"
"MZ v,12,a,196,a,28\n"
"buffer_load_dwordx4 v248, s[20:23], s86 offen lds\n"
"MA v,12,a,192,a,12\n"
"v_accvgpr_write_b32 a84, v164\n"
"MZ v,28,a,200,a,12\n"
"ds_read_b128 a[224:227], v246 offset:16384\n"
"v_accvgpr_write_b32 a85, v165\n"
"MA v,28,a,204,a,28\n"
"s_mov_b32 s87, 51200\n"
"ds_read_b128 a[228:231], v247 offset:16384\n"
"v_accvgpr_write_b32 a86, v166\n"
"MZ v,24,a,204,a,24\n"
"v_accvgpr_write_b32 a68, v132\n"
"v_accvgpr_write_b32 a87, v167\n"
"MA v,24,a,200,a,8\n"
"v_accvgpr_write_b32 a69, v133\n"
"v_accvgpr_write_b32 a70, v134\n"
"MZ v,20,a,200,a,4\n"
"buffer_load_dwordx4 v248, s[20:23], s87 offen offset:1024 lds\n"
"MA v,20,a,204,a,20\n"
"v_accvgpr_write_b32 a71, v135\n"
"MZ v,16,a,204,a,16\n"
"v_cvt_pk_bf16_f32 v136, v136, v137\n"
"v_cvt_pk_bf16_f32 v137, v138, v139\n"
"MA v,16,a,200,a,0\n"
"s_waitcnt lgkmcnt(2)\n"
"s_add_u32 m0, s44, 73728\n"
"v_cvt_pk_bf16_f32 v138, v152, v153\n"
"MA v,0,a,208,a,32\n"
"s_mov_b32 s88, 49280\n"
"ds_read_b128 a[232:235], v246 offset:18432\n"
"MA v,0,a,212,a,48\n"
"ds_read_b128 a[236:239], v247 offset:18432\n"
"v_cvt_pk_bf16_f32 v139, v154, v155\n"
"MA v,4,a,212,a,52\n"
"buffer_load_dwordx4 v248, s[20:23], s88 offen lds\n"
"MA v,4,a,208,a,36\n"
"v_accvgpr_write_b32 a72, v136\n"
"MA v,8,a,208,a,40\n"
"v_accvgpr_write_b32 a73, v137\n"
"v_accvgpr_write_b32 a74, v138\n"
"MA v,8,a,212,a,56\n"
"s_mov_b32 s89, 51328\n"
"v_accvgpr_write_b32 a75, v139\n"
"v_cvt_pk_bf16_f32 v168, v168, v169\n"
"MA v,12,a,212,a,60\n"
"v_cvt_pk_bf16_f32 v169, v170, v171\n"
"v_cvt_pk_bf16_f32 v170, v184, v185\n"
"MA v,12,a,208,a,44\n"
"v_cvt_pk_bf16_f32 v171, v186, v187\n"
"v_accvgpr_write_b32 a88, v168\n"
"MA v,28,a,216,a,44\n"
"buffer_load_dwordx4 v248, s[20:23], s89 offen offset:1024 lds\n"
"MA v,28,a,220,a,60\n"
"v_accvgpr_write_b32 a89, v169\n"
"MA v,24,a,220,a,56\n"
"v_accvgpr_write_b32 a90, v170\n"
"v_accvgpr_write_b32 a91, v171\n"
"MA v,24,a,216,a,40\n"
"s_add_u32 m0, s44, 81920\n"
"v_cvt_pk_bf16_f32 v172, v172, v173\n"
"v_cvt_pk_bf16_f32 v173, v174, v175\n"
"MA v,20,a,216,a,36\n"
"s_mov_b32 s90, 49408\n"
"v_cvt_pk_bf16_f32 v174, v188, v189\n"
"v_cvt_pk_bf16_f32 v175, v190, v191\n"
"MA v,20,a,220,a,52\n"
"v_cvt_pk_bf16_f32 v140, v140, v141\n"
"v_cvt_pk_bf16_f32 v141, v142, v143\n"
"MA v,16,a,220,a,48\n"
"v_cvt_pk_bf16_f32 v142, v156, v157\n"
"v_cvt_pk_bf16_f32 v143, v158, v159\n"
"MA v,16,a,216,a,32\n"
"s_waitcnt lgkmcnt(0)\n"
"v_accvgpr_write_b32 a92, v172\n"
"v_accvgpr_write_b32 a93, v173\n"
"MA v,0,a,224,a,64\n"
"v_accvgpr_write_b32 a94, v174\n"
"v_accvgpr_write_b32 a95, v175\n"
"MA v,0,a,228,a,80\n"
"s_mov_b32 s91, 51456\n"
"v_accvgpr_write_b32 a76, v140\n"
"v_accvgpr_write_b32 a77, v141\n"
"MA v,4,a,228,a,84\n"
"v_accvgpr_write_b32 a78, v142\n"
"v_accvgpr_write_b32 a79, v143\n"
"MA v,4,a,224,a,68\n"
"buffer_load_dwordx4 v248, s[20:23], s90 offen lds\n"
"MA v,8,a,224,a,72\n"
"MA v,8,a,228,a,88\n"
"ds_read_b128 a[192:195], v246 offset:4096\n"
"v_mov_b32 v250, 0x3f803f80\n"
"MA v,12,a,228,a,92\n"
"ds_read_b128 a[196:199], v247 offset:4096\n"
"v_mov_b32 v251, 0x3f803f80\n"
"MA v,12,a,224,a,76\n"
"buffer_load_dwordx4 v248, s[20:23], s91 offen offset:1024 lds\n"
"MA v,28,a,232,a,76\n"
"ds_read_b128 a[200:203], v246 offset:6144\n"
"MA v,28,a,236,a,92\n"
"ds_read_b128 a[204:207], v247 offset:6144\n"
"MA v,24,a,236,a,88\n"
"ds_read_b128 a[208:211], v246 offset:12288\n"
"MA v,24,a,232,a,72\n"
"ds_read_b128 a[212:215], v247 offset:12288\n"
"MA v,20,a,232,a,68\n"
"MA v,20,a,236,a,84\n"
"MA v,16,a,236,a,80\n"
"MA v,16,a,232,a,64\n"
"s_add_u32 m0, s44, 90112\n"
"L_JOIN%=:\n"
"s_waitcnt lgkmcnt(4)\n"
"MZ v,32,a,192,a,0\n"
"s_mov_b32 s86, 73728\n"
"ds_read_b128 a[216:219], v246 offset:14336\n"
"v_max3_f32 v224, v0, v1, v2\n"
"v_max3_f32 v225, v4, v5, v6\n"
"MA v,32,a,196,a,16\n"
"ds_read_b128 a[220:223], v247 offset:14336\n"
"v_max3_f32 v225, v225, v7, v20\n"
"v_max3_f32 v226, v8, v9, v10\n"
"v_max3_f32 v227, v12, v13, v14\n"
"MZ v,36,a,196,a,20\n"
"buffer_load_dwordx4 v248, s[20:23], s86 offen lds\n"
"MA v,36,a,192,a,4\n"
"v_max3_f32 v224, v224, v3, v16\n"
"v_max3_f32 v225, v225, v21, v22\n"
"MZ v,40,a,192,a,8\n"
"v_max3_f32 v224, v224, v17, v18\n"
"v_max3_f32 v225, v225, v23, v229\n"
"v_max3_f32 v226, v226, v11, v24\n"
"MA v,40,a,196,a,24\n"
"v_max3_f32 v224, v224, v19, v228\n"
"v_mov_b32 v229, v225\n"
"v_max3_f32 v226, v226, v25, v26\n"
"v_max3_f32 v227, v227, v15, v28\n"
"MZ v,44,a,196,a,28\n"
"v_mov_b32 v228, v224\n"
"v_permlane16_swap_b32 v225, v229\n"
"v_max3_f32 v226, v226, v27, v230\n"
"MA v,44,a,192,a,12\n"
"s_waitcnt lgkmcnt(2)\n"
"s_mov_b32 s87, 75776\n"
"v_permlane16_swap_b32 v224, v228\n"
"MZ v,60,a,200,a,12\n"
"ds_read_b128 a[224:227], v246 offset:20480\n"
"v_max_f32 v224, v224, v228\n"
"v_max_f32 v225, v225, v229\n"
"v_mov_b32 v230, v226\n"
"MA v,60,a,204,a,28\n"
"ds_read_b128 a[228:231], v247 offset:20480\n"
"v_mov_b32 v228, v224\n"
"v_mov_b32 v229, v225\n"
"v_max3_f32 v227, v227, v29, v30\n"
"MZ v,56,a,204,a,24\n"
"buffer_load_dwordx4 v248, s[20:23], s87 offen offset:1024 lds\n"
"MA v,56,a,200,a,8\n"
"v_permlane32_swap_b32 v224, v228\n"
"MZ v,52,a,200,a,4\n"
"v_max_f32 v224, v224, v228\n"
"v_permlane16_swap_b32 v226, v230\n"
"MA v,52,a,204,a,20\n"
"v_sub_f32 v208, 0, v224\n"
"v_sub_f32 v209, 0, v224\n"
"v_sub_f32 v210, 0, v224\n"
"v_max3_f32 v227, v227, v31, v231\n"
"MZ v,48,a,204,a,16\n"
"v_sub_f32 v211, 0, v224\n"
"v_permlane32_swap_b32 v225, v229\n"
"v_max_f32 v226, v226, v230\n"
"MA v,48,a,200,a,0\n"
"s_add_u32 m0, s44, 98304\n"
"v_max_f32 v225, v225, v229\n"
"v_mov_b32 v231, v227\n"
"MA v,32,a,208,a,32\n"
"s_mov_b32 s88, 73856\n"
"ds_read_b128 a[232:235], v246 offset:22528\n"
"v_mov_b32 v230, v226\n"
"v_add_f32 v0, v0, v208\n"
"MA v,32,a,212,a,48\n"
"ds_read_b128 a[236:239], v247 offset:22528\n"
"v_permlane16_swap_b32 v227, v231\n"
"v_add_f32 v1, v1, v209\n"
"MA v,36,a,212,a,52\n"
"buffer_load_dwordx4 v248, s[20:23], s88 offen lds\n"
"MA v,36,a,208,a,36\n"
"v_add_f32 v2, v2, v210\n"
"v_add_f32 v3, v3, v211\n"
"MA v,40,a,208,a,40\n"
"v_sub_f32 v212, 0, v225\n"
"v_sub_f32 v213, 0, v225\n"
"v_sub_f32 v214, 0, v225\n"
"v_sub_f32 v215, 0, v225\n"
"MA v,40,a,212,a,56\n"
"v_permlane32_swap_b32 v226, v230\n"
"v_max_f32 v227, v227, v231\n"
"MA v,44,a,212,a,60\n"
"v_exp_f32 v0, v0\n"
"v_exp_f32 v1, v1\n"
"MA v,44,a,208,a,44\n"
"s_waitcnt lgkmcnt(2)\n"
"s_mov_b32 s89, 75904\n"
"v_max_f32 v226, v226, v230\n"
"v_exp_f32 v2, v2\n"
"MA v,60,a,216,a,44\n"
"ds_read_b128 a[192:195], v246 offset:24576\n"
"v_exp_f32 v3, v3\n"
"MA v,60,a,220,a,60\n"
"ds_read_b128 a[196:199], v247 offset:24576\n"
"v_mov_b32 v231, v227\n"
"v_cvt_pk_bf16_f32 v64, v0, v1\n"
"v_cvt_pk_bf16_f32 v65, v2, v3\n"
"MA v,56,a,220,a,56\n"
"buffer_load_dwordx4 v248, s[20:23], s89 offen offset:1024 lds\n"
"MA v,56,a,216,a,40\n"
"v_add_f32 v4, v4, v212\n"
"v_add_f32 v5, v5, v213\n"
"MA v,52,a,216,a,36\n"
"v_sub_f32 v216, 0, v226\n"
"v_sub_f32 v217, 0, v226\n"
"v_add_f32 v6, v6, v214\n"
"v_add_f32 v7, v7, v215\n"
"MA v,52,a,220,a,52\n"
"v_sub_f32 v218, 0, v226\n"
"v_sub_f32 v219, 0, v226\n"
"v_permlane32_swap_b32 v227, v231\n"
"MA v,48,a,220,a,48\n"
"v_max_f32 v227, v227, v231\n"
"v_exp_f32 v4, v4\n"
"MA v,48,a,216,a,32\n"
"s_add_u32 m0, s44, 106496\n"
"v_exp_f32 v5, v5\n"
"v_add_f32 v8, v8, v216\n"
"MA v,32,a,224,a,64\n"
"s_mov_b32 s90, 73984\n"
"v_exp_f32 v6, v6\n"
"v_cvt_pk_bf16_f32 v68, v4, v5\n"
"MA v,32,a,228,a,80\n"
"v_exp_f32 v7, v7\n"
"v_add_f32 v9, v9, v217\n"
"v_add_f32 v10, v10, v218\n"
"MA v,36,a,228,a,84\n"
"buffer_load_dwordx4 v248, s[20:23], s90 offen lds\n"
"MA v,36,a,224,a,68\n"
"v_cvt_pk_bf16_f32 v69, v6, v7\n"
"v_add_f32 v11, v11, v219\n"
"MA v,40,a,224,a,72\n"
"ds_read_b128 a[200:203], v246 offset:26624\n"
"v_sub_f32 v220, 0, v227\n"
"v_sub_f32 v221, 0, v227\n"
"v_sub_f32 v222, 0, v227\n"
"MA v,40,a,228,a,88\n"
"ds_read_b128 a[204:207], v247 offset:26624\n"
"v_sub_f32 v223, 0, v227\n"
"v_exp_f32 v8, v8\n"
"MA v,44,a,228,a,92\n"
"v_exp_f32 v9, v9\n"
"v_add_f32 v12, v12, v220\n"
"MA v,44,a,224,a,76\n"
"s_waitcnt lgkmcnt(2)\n"
"v_exp_f32 v10, v10\n"
"v_exp_f32 v11, v11\n"
"MA v,60,a,232,a,76\n"
"v_cvt_pk_bf16_f32 v72, v8, v9\n"
"v_cvt_pk_bf16_f32 v73, v10, v11\n"
"v_add_f32 v13, v13, v221\n"
"v_add_f32 v14, v14, v222\n"
"MA v,60,a,236,a,92\n"
"s_mov_b32 s91, 76032\n"
"v_exp_f32 v12, v12\n"
"v_add_f32 v15, v15, v223\n"
"v_add_f32 v28, v28, v220\n"
"MA v,56,a,236,a,88\n"
"v_exp_f32 v13, v13\n"
"v_add_f32 v29, v29, v221\n"
"MA v,56,a,232,a,72\n"
"v_exp_f32 v14, v14\n"
"v_exp_f32 v15, v15\n"
"MA v,52,a,232,a,68\n"
"buffer_load_dwordx4 v248, s[20:23], s91 offen offset:1024 lds\n"
"MA v,52,a,236,a,84\n"
"v_add_f32 v30, v30, v222\n"
"v_add_f32 v31, v31, v223\n"
"v_cvt_pk_bf16_f32 v76, v12, v13\n"
"MA v,48,a,236,a,80\n"
"v_exp_f32 v28, v28\n"
"v_cvt_pk_bf16_f32 v77, v14, v15\n"
"MA v,48,a,232,a,64\n"
"v_exp_f32 v29, v29\n"
"v_exp_f32 v30, v30\n"
"v_mfma_f32_16x16x32_bf16 v[0:3], a[192:195], a[0:3], v[208:211]\n"
"v_add_f32 v24, v24, v216\n"
"v_add_f32 v25, v25, v217\n"
"v_exp_f32 v31, v31\n"
"MA v,0,a,196,a,16\n"
"v_add_f32 v26, v26, v218\n"
"v_add_f32 v27, v27, v219\n"
"v_cvt_pk_bf16_f32 v78, v28, v29\n"
"v_cvt_pk_bf16_f32 v79, v30, v31\n"
"v_mfma_f32_16x16x32_bf16 v[4:7], a[196:199], a[20:23], v[212:215]\n"
"v_add_f32 v20, v20, v212\n"
"v_exp_f32 v24, v24\n"
"MA v,4,a,192,a,4\n"
"v_exp_f32 v25, v25\n"
"v_exp_f32 v26, v26\n"
"v_mfma_f32_16x16x32_bf16 v[8:11], a[192:195], a[8:11], v[216:219]\n"
"s_cmp_eq_u32 s53, 0\n"
"s_cselect_b32 s86, s70, s55\n"
"v_add_f32 v21, v21, v213\n"
"v_add_f32 v22, v22, v214\n"
"v_exp_f32 v27, v27\n"
"MA v,8,a,196,a,24\n"
"ds_read_b128 a[208:211], v246 offset:32768\n"
"v_add_f32 v23, v23, v215\n"
"v_cvt_pk_bf16_f32 v74, v24, v25\n"
"v_cvt_pk_bf16_f32 v75, v26, v27\n"
"v_mfma_f32_16x16x32_bf16 v[12:15], a[196:199], a[28:31], v[220:223]\n"
"v_exp_f32 v20, v20\n"
"v_exp_f32 v21, v21\n"
"MA v,12,a,192,a,12\n"
"s_waitcnt lgkmcnt(1)\n"
"ds_read_b128 a[212:215], v247 offset:32768\n"
"v_exp_f32 v22, v22\n"
"v_mfma_f32_16x16x32_bf16 v[28:31], a[200:203], a[12:15], v[220:223]\n"
"v_add_f32 v16, v16, v208\n"
"v_add_f32 v17, v17, v209\n"
"v_exp_f32 v23, v23\n"
"MA v,28,a,204,a,28\n"
"v_add_f32 v18, v18, v210\n"
"v_add_f32 v19, v19, v211\n"
"v_cvt_pk_bf16_f32 v70, v20, v21\n"
"v_cvt_pk_bf16_f32 v71, v22, v23\n"
"v_mfma_f32_16x16x32_bf16 v[24:27], a[204:207], a[24:27], v[216:219]\n"
"v_exp_f32 v16, v16\n"
"v_exp_f32 v17, v17\n"
"MA v,24,a,200,a,8\n"
"v_exp_f32 v18, v18\n"
"v_cvt_pk_bf16_f32 v66, v16, v17\n"
"v_mfma_f32_16x16x32_bf16 v[20:23], a[200:203], a[4:7], v[212:215]\n"
"s_add_u32 s8, s56, 1\n"
"ds_read_b128 a[216:219], v246 offset:34816\n"
"v_exp_f32 v19, v19\n"
"v_mov_b32 v193, s57\n"
"MA v,20,a,204,a,20\n"
"buffer_load_dword v235, v233, s[24:27], s86 offen\n"
"v_cvt_pk_bf16_f32 v67, v18, v19\n"
"v_mfma_f32_16x16x32_bf16 v[16:19], a[204:207], a[16:19], v[208:211]\n"
"ds_read_b128 a[220:223], v247 offset:34816\n"
"v_add_f32 v32, v32, v208\n"
"v_add_f32 v33, v33, v209\n"
"MA v,16,a,200,a,0\n"
"s_waitcnt lgkmcnt(2)\n"
"ds_read_b128 a[224:227], v246 offset:40960\n"
"v_add_f32 v34, v34, v210\n"
"v_add_f32 v35, v35, v211\n"
"MA v,0,a,208,a,32\n"
"ds_read_b128 a[228:231], v247 offset:40960\n"
"v_exp_f32 v32, v32\n"
"MA v,0,a,212,a,48\n"
"ds_read_b128 a[232:235], v246 offset:43008\n"
"v_exp_f32 v33, v33\n"
"MA v,4,a,212,a,52\n"
"ds_read_b128 a[236:239], v247 offset:43008\n"
"v_cvt_pk_bf16_f32 v80, v32, v33\n"
"MA v,4,a,208,a,36\n"
"v_exp_f32 v34, v34\n"
"v_add_f32 v36, v36, v212\n"
"MA v,8,a,208,a,40\n"
"v_exp_f32 v35, v35\n"
"v_add_f32 v37, v37, v213\n"
"MA v,8,a,212,a,56\n"
"v_cvt_pk_bf16_f32 v81, v34, v35\n"
"v_add_f32 v38, v38, v214\n"
"v_add_f32 v39, v39, v215\n"
"MA v,12,a,212,a,60\n"
"v_exp_f32 v36, v36\n"
"MA v,12,a,208,a,44\n"
"s_waitcnt lgkmcnt(3)\n"
"v_exp_f32 v37, v37\n"
"v_add_f32 v40, v40, v216\n"
"MA v,28,a,216,a,44\n"
"ds_read_b128 a[192:195], v246 offset:28672\n"
"v_exp_f32 v38, v38\n"
"MA v,28,a,220,a,60\n"
"ds_read_b128 a[196:199], v247 offset:28672\n"
"v_exp_f32 v39, v39\n"
"MA v,24,a,220,a,56\n"
"v_cvt_pk_bf16_f32 v84, v36, v37\n"
"v_cvt_pk_bf16_f32 v85, v38, v39\n"
"v_add_f32 v41, v41, v217\n"
"MA v,24,a,216,a,40\n"
"v_add_f32 v42, v42, v218\n"
"v_add_f32 v43, v43, v219\n"
"MA v,20,a,216,a,36\n"
"v_exp_f32 v40, v40\n"
"v_mov_b32 v192, s8\n"
"MA v,20,a,220,a,52\n"
"v_exp_f32 v41, v41\n"
"v_add_f32 v44, v44, v220\n"
"MA v,16,a,220,a,48\n"
"v_exp_f32 v42, v42\n"
"v_cvt_pk_bf16_f32 v88, v40, v41\n"
"MA v,16,a,216,a,32\n"
"v_exp_f32 v43, v43\n"
"MA v,0,a,224,a,64\n"
"s_waitcnt lgkmcnt(1)\n"
"ds_read_b128 a[200:203], v246 offset:30720\n"
"v_cvt_pk_bf16_f32 v89, v42, v43\n"
"v_add_f32 v45, v45, v221\n"
"MA v,0,a,228,a,80\n"
"s_waitcnt vmcnt(13)\n"
"buffer_store_dwordx2 v[192:193], v234, s[16:19], s60 offen sc1\n"
"MA v,4,a,228,a,84\n"
"ds_read_b128 a[204:207], v247 offset:30720\n"
"MA v,4,a,224,a,68\n"
"v_add_f32 v46, v46, v222\n"
"v_add_f32 v47, v47, v223\n"
"v_add_f32 v60, v60, v220\n"
"MA v,8,a,224,a,72\n"
"v_exp_f32 v44, v44\n"
"MA v,8,a,228,a,88\n"
"v_exp_f32 v45, v45\n"
"v_add_f32 v61, v61, v221\n"
"MA v,12,a,228,a,92\n"
"v_exp_f32 v46, v46\n"
"v_add_f32 v62, v62, v222\n"
"MA v,12,a,224,a,76\n"
"v_exp_f32 v47, v47\n"
"v_add_f32 v63, v63, v223\n"
"MA v,28,a,232,a,76\n"
"v_cvt_pk_bf16_f32 v92, v44, v45\n"
"v_cvt_pk_bf16_f32 v93, v46, v47\n"
"MA v,28,a,236,a,92\n"
"v_add_f32 v56, v56, v216\n"
"v_exp_f32 v60, v60\n"
"MA v,24,a,236,a,88\n"
"v_add_f32 v57, v57, v217\n"
"v_exp_f32 v61, v61\n"
"MA v,24,a,232,a,72\n"
"v_add_f32 v58, v58, v218\n"
"v_exp_f32 v62, v62\n"
"MA v,20,a,232,a,68\n"
"v_add_f32 v59, v59, v219\n"
"v_exp_f32 v63, v63\n"
"MA v,20,a,236,a,84\n"
"v_cvt_pk_bf16_f32 v94, v60, v61\n"
"v_cvt_pk_bf16_f32 v95, v62, v63\n"
"MA v,16,a,236,a,80\n"
"v_add_f32 v52, v52, v212\n"
"v_exp_f32 v56, v56\n"
"MA v,16,a,232,a,64\n"
"v_add_f32 v53, v53, v213\n"
"v_exp_f32 v57, v57\n"
"v_mfma_f32_16x16x32_bf16 v[32:35], a[192:195], a[0:3], v[208:211]\n"
"s_waitcnt lgkmcnt(0)\n"
"v_add_f32 v54, v54, v214\n"
"v_exp_f32 v58, v58\n"
"MA v,32,a,196,a,16\n"
"v_add_f32 v55, v55, v215\n"
"v_exp_f32 v59, v59\n"
"v_mfma_f32_16x16x32_bf16 v[36:39], a[196:199], a[20:23], v[212:215]\n"
"v_cvt_pk_bf16_f32 v90, v56, v57\n"
"v_cvt_pk_bf16_f32 v91, v58, v59\n"
"MA v,36,a,192,a,4\n"
"ds_read_b128 a[208:211], v246 offset:36864\n"
"v_exp_f32 v52, v52\n"
"v_mfma_f32_16x16x32_bf16 v[40:43], a[192:195], a[8:11], v[216:219]\n"
"s_cmp_eq_u32 s53, 1\n"
"s_cselect_b32 s87, s70, s55\n"
"ds_read_b128 a[212:215], v247 offset:36864\n"
"v_exp_f32 v53, v53\n"
"MA v,40,a,196,a,24\n"
"v_add_f32 v48, v48, v208\n"
"v_exp_f32 v54, v54\n"
"v_mfma_f32_16x16x32_bf16 v[44:47], a[196:199], a[28:31], v[220:223]\n"
"v_exp_f32 v55, v55\n"
"MA v,44,a,192,a,12\n"
"v_add_f32 v49, v49, v209\n"
"v_add_f32 v50, v50, v210\n"
"v_add_f32 v51, v51, v211\n"
"v_mfma_f32_16x16x32_bf16 v[60:63], a[200:203], a[12:15], v[220:223]\n"
"ds_read_b128 a[216:219], v246 offset:38912\n"
"v_cvt_pk_bf16_f32 v86, v52, v53\n"
"v_cvt_pk_bf16_f32 v87, v54, v55\n"
"MA v,60,a,204,a,28\n"
"ds_read_b128 a[220:223], v247 offset:38912\n"
"v_exp_f32 v48, v48\n"
"v_mfma_f32_16x16x32_bf16 v[56:59], a[204:207], a[24:27], v[216:219]\n"
"ds_read_b128 a[224:227], v246 offset:45056\n"
"v_exp_f32 v49, v49\n"
"MA v,56,a,200,a,8\n"
"v_exp_f32 v50, v50\n"
"v_exp_f32 v51, v51\n"
"v_mfma_f32_16x16x32_bf16 v[52:55], a[200:203], a[4:7], v[212:215]\n"
"ds_read_b128 a[228:231], v247 offset:45056\n"
"v_cvt_pk_bf16_f32 v82, v48, v49\n"
"v_cvt_pk_bf16_f32 v83, v50, v51\n"
"v_mov_b32 v224, 0\n"
"MA v,52,a,204,a,20\n"
"buffer_load_dword v235, v233, s[24:27], s87 offen\n"
"v_mfma_f32_16x16x32_bf16 v[48:51], a[204:207], a[16:19], v[208:211]\n"
"v_exp_f32 v0, v0\n"
"MA v,48,a,200,a,0\n"
"s_waitcnt lgkmcnt(4)\n"
"v_exp_f32 v1, v1\n"
"v_mov_b32 v225, s51\n"
"MA v,32,a,208,a,32\n"
"ds_read_b128 a[232:235], v246 offset:47104\n"
"v_cvt_pk_bf16_f32 v0, v0, v1\n"
"MA v,32,a,212,a,48\n"
"ds_read_b128 a[236:239], v247 offset:47104\n"
"v_exp_f32 v2, v2\n"
"MA v,36,a,212,a,52\n"
"v_exp_f32 v3, v3\n"
"MA v,36,a,208,a,36\n"
"v_exp_f32 v16, v16\n"
"v_cvt_pk_bf16_f32 v1, v2, v3\n"
"MA v,40,a,208,a,40\n"
"v_exp_f32 v17, v17\n"
"v_accvgpr_write_b32 a96, v0\n"
"MA v,40,a,212,a,56\n"
"v_exp_f32 v18, v18\n"
"MA v,44,a,212,a,60\n"
"v_exp_f32 v19, v19\n"
"v_cvt_pk_bf16_f32 v2, v16, v17\n"
"MA v,44,a,208,a,44\n"
"s_waitcnt vmcnt(9)\n"
"s_barrier\n"
"v_cvt_pk_bf16_f32 v3, v18, v19\n"
"s_waitcnt lgkmcnt(2)\n"
"MA v,60,a,216,a,44\n"
"ds_read_b128 a[192:195], v244 offset:0\n"
"v_accvgpr_write_b32 a97, v1\n"
"v_accvgpr_write_b32 a98, v2\n"
"MA v,60,a,220,a,60\n"
"ds_read_b128 a[196:199], v245 offset:0\n"
"v_accvgpr_write_b32 a99, v3\n"
"v_add_u32 v226, 131072, v249\n"
"MA v,56,a,220,a,56\n"
"v_exp_f32 v4, v4\n"
"MA v,56,a,216,a,40\n"
"v_exp_f32 v5, v5\n"
"MA v,52,a,216,a,36\n"
"v_exp_f32 v6, v6\n"
"v_cvt_pk_bf16_f32 v4, v4, v5\n"
"MA v,52,a,220,a,52\n"
"v_exp_f32 v7, v7\n"
"v_accvgpr_write_b32 a100, v4\n"
"MA v,48,a,220,a,48\n"
"v_exp_f32 v20, v20\n"
"v_cvt_pk_bf16_f32 v5, v6, v7\n"
"MA v,48,a,216,a,32\n"
"v_exp_f32 v21, v21\n"
"MA v,32,a,224,a,64\n"
"v_exp_f32 v22, v22\n"
"v_cvt_pk_bf16_f32 v6, v20, v21\n"
"MA v,32,a,228,a,80\n"
"v_exp_f32 v23, v23\n"
"v_accvgpr_write_b32 a101, v5\n"
"MA v,36,a,228,a,84\n"
"v_cvt_pk_bf16_f32 v7, v22, v23\n"
"v_accvgpr_write_b32 a102, v6\n"
"MA v,36,a,224,a,68\n"
"v_accvgpr_write_b32 a103, v7\n"
"v_exp_f32 v8, v8\n"
"MA v,40,a,224,a,72\n"
"ds_read_b128 a[200:203], v244 offset:2048\n"
"v_exp_f32 v9, v9\n"
"MA v,40,a,228,a,88\n"
"v_exp_f32 v10, v10\n"
"MA v,44,a,228,a,92\n"
"v_exp_f32 v11, v11\n"
"v_cvt_pk_bf16_f32 v8, v8, v9\n"
"MA v,44,a,224,a,76\n"
"s_waitcnt lgkmcnt(1)\n"
"v_exp_f32 v24, v24\n"
"v_cvt_pk_bf16_f32 v9, v10, v11\n"
"MA v,60,a,232,a,76\n"
"v_exp_f32 v25, v25\n"
"MA v,60,a,236,a,92\n"
"v_exp_f32 v26, v26\n"
"v_cvt_pk_bf16_f32 v10, v24, v25\n"
"MA v,56,a,236,a,88\n"
"ds_read_b128 a[204:207], v245 offset:2048\n"
"v_exp_f32 v27, v27\n"
"MA v,56,a,232,a,72\n"
"v_cvt_pk_bf16_f32 v11, v26, v27\n"
"v_accvgpr_write_b32 a104, v8\n"
"MA v,52,a,232,a,68\n"
"v_accvgpr_write_b32 a105, v9\n"
"v_accvgpr_write_b32 a106, v10\n"
"v_accvgpr_write_b32 a107, v11\n"
"MA v,52,a,236,a,84\n"
"s_add_u32 m0, s44, 114688\n"
"ds_read_b128 a[208:211], v244 offset:8192\n"
"v_exp_f32 v12, v12\n"
"MA v,48,a,236,a,80\n"
"s_mov_b32 s88, 98304\n"
"v_exp_f32 v13, v13\n"
"MA v,48,a,232,a,64\n"
"v_exp_f32 v14, v14\n"
"v_cvt_pk_bf16_f32 v12, v12, v13\n"
"v_mfma_f32_16x16x32_bf16 v[0:3], a[192:195], a[0:3], v[208:211]\n"
"v_exp_f32 v15, v15\n"
"v_accvgpr_write_b32 a108, v12\n"
"MA v,0,a,196,a,16\n"
"v_exp_f32 v28, v28\n"
"v_cvt_pk_bf16_f32 v13, v14, v15\n"
"v_mfma_f32_16x16x32_bf16 v[4:7], a[196:199], a[20:23], v[212:215]\n"
"v_exp_f32 v29, v29\n"
"v_accvgpr_write_b32 a109, v13\n"
"MA v,4,a,192,a,4\n"
"v_exp_f32 v30, v30\n"
"v_exp_f32 v31, v31\n"
"v_mfma_f32_16x16x32_bf16 v[8:11], a[192:195], a[8:11], v[216:219]\n"
"v_cvt_pk_bf16_f32 v14, v28, v29\n"
"v_cvt_pk_bf16_f32 v15, v30, v31\n"
"MA v,8,a,196,a,24\n"
"ds_read_b128 a[212:215], v245 offset:8192\n"
"v_accvgpr_write_b32 a110, v14\n"
"v_accvgpr_write_b32 a111, v15\n"
"v_mfma_f32_16x16x32_bf16 v[12:15], a[196:199], a[28:31], v[220:223]\n"
"buffer_load_dwordx4 v248, s[20:23], s88 offen lds\n"
"MA v,12,a,192,a,12\n"
"s_waitcnt lgkmcnt(1)\n"
"v_mfma_f32_16x16x32_bf16 v[28:31], a[200:203], a[12:15], v[220:223]\n"
"s_cmp_eq_u32 s53, 2\n"
"s_cselect_b32 s88, s70, s55\n"
"ds_read_b128 a[216:219], v244 offset:10240\n"
"MA v,28,a,204,a,28\n"
"buffer_load_dword v235, v233, s[24:27], s88 offen\n"
"v_mfma_f32_16x16x32_bf16 v[24:27], a[204:207], a[24:27], v[216:219]\n"
"ds_read_b128 a[220:223], v245 offset:10240\n"
"MA v,24,a,200,a,8\n"
"s_mov_b32 s89, 100352\n"
"ds_read_b128 a[224:227], v244 offset:16384\n"
"v_mfma_f32_16x16x32_bf16 v[20:23], a[200:203], a[4:7], v[212:215]\n"
"ds_read_b128 a[228:231], v245 offset:16384\n"
"v_exp_f32 v32, v32\n"
"MA v,20,a,204,a,20\n"
"v_exp_f32 v33, v33\n"
"v_mfma_f32_16x16x32_bf16 v[16:19], a[204:207], a[16:19], v[208:211]\n"
"buffer_load_dwordx4 v248, s[20:23], s89 offen offset:1024 lds\n"
"MA v,16,a,200,a,0\n"
"v_cvt_pk_bf16_f32 v32, v32, v33\n"
"MA v,0,a,208,a,32\n"
"s_waitcnt lgkmcnt(3)\n"
"ds_read_b128 a[232:235], v244 offset:18432\n"
"v_exp_f32 v34, v34\n"
"MA v,0,a,212,a,48\n"
"ds_read_b128 a[236:239], v245 offset:18432\n"
"v_accvgpr_write_b32 a112, v32\n"
"MA v,4,a,212,a,52\n"
"v_exp_f32 v35, v35\n"
"MA v,4,a,208,a,36\n"
"v_exp_f32 v48, v48\n"
"v_cvt_pk_bf16_f32 v33, v34, v35\n"
"MA v,8,a,208,a,40\n"
"v_exp_f32 v49, v49\n"
"v_accvgpr_write_b32 a113, v33\n"
"MA v,8,a,212,a,56\n"
"v_exp_f32 v50, v50\n"
"v_cvt_pk_bf16_f32 v34, v48, v49\n"
"MA v,12,a,212,a,60\n"
"v_exp_f32 v51, v51\n"
"MA v,12,a,208,a,44\n"
"v_cvt_pk_bf16_f32 v35, v50, v51\n"
"v_accvgpr_write_b32 a114, v34\n"
"MA v,28,a,216,a,44\n"
"s_waitcnt lgkmcnt(1)\n"
"ds_read_b128 a[192:195], v244 offset:4096\n"
"v_accvgpr_write_b32 a115, v35\n"
"MA v,28,a,220,a,60\n"
"s_add_u32 m0, s44, 122880\n"
"s_mov_b32 s90, 98432\n"
"ds_read_b128 a[196:199], v245 offset:4096\n"
"MA v,24,a,220,a,56\n"
"v_exp_f32 v36, v36\n"
"MA v,24,a,216,a,40\n"
"v_exp_f32 v37, v37\n"
"MA v,20,a,216,a,36\n"
"buffer_load_dwordx4 v248, s[20:23], s90 offen lds\n"
"MA v,20,a,220,a,52\n"
"v_cvt_pk_bf16_f32 v36, v36, v37\n"
"MA v,16,a,220,a,48\n"
"v_exp_f32 v38, v38\n"
"v_accvgpr_write_b32 a116, v36\n"
"MA v,16,a,216,a,32\n"
"v_exp_f32 v39, v39\n"
"MA v,0,a,224,a,64\n"
"v_exp_f32 v52, v52\n"
"v_cvt_pk_bf16_f32 v37, v38, v39\n"
"MA v,0,a,228,a,80\n"
"v_exp_f32 v53, v53\n"
"v_accvgpr_write_b32 a117, v37\n"
"MA v,4,a,228,a,84\n"
"v_exp_f32 v54, v54\n"
"v_cvt_pk_bf16_f32 v38, v52, v53\n"
"MA v,4,a,224,a,68\n"
"v_exp_f32 v55, v55\n"
"v_accvgpr_write_b32 a118, v38\n"
"MA v,8,a,224,a,72\n"
"v_cvt_pk_bf16_f32 v39, v54, v55\n"
"v_exp_f32 v40, v40\n"
"MA v,8,a,228,a,88\n"
"v_accvgpr_write_b32 a119, v39\n"
"v_exp_f32 v41, v41\n"
"MA v,12,a,228,a,92\n"
"ds_read_b128 a[200:203], v244 offset:6144\n"
"v_exp_f32 v42, v42\n"
"MA v,12,a,224,a,76\n"
"v_exp_f32 v43, v43\n"
"v_cvt_pk_bf16_f32 v40, v40, v41\n"
"MA v,28,a,232,a,76\n"
"s_waitcnt lgkmcnt(1)\n"
"v_exp_f32 v56, v56\n"
"MA v,28,a,236,a,92\n"
"s_mov_b32 s91, 100480\n"
"v_exp_f32 v57, v57\n"
"v_cvt_pk_bf16_f32 v41, v42, v43\n"
"MA v,24,a,236,a,88\n"
"v_exp_f32 v58, v58\n"
"v_cvt_pk_bf16_f32 v42, v56, v57\n"
"MA v,24,a,232,a,72\n"
"ds_read_b128 a[204:207], v245 offset:6144\n"
"v_exp_f32 v59, v59\n"
"MA v,20,a,232,a,68\n"
"v_cvt_pk_bf16_f32 v43, v58, v59\n"
"v_accvgpr_write_b32 a120, v40\n"
"v_accvgpr_write_b32 a121, v41\n"
"MA v,20,a,236,a,84\n"
"ds_read_b128 a[208:211], v244 offset:12288\n"
"v_accvgpr_write_b32 a122, v42\n"
"v_accvgpr_write_b32 a123, v43\n"
"MA v,16,a,236,a,80\n"
"ds_read_b128 a[212:215], v245 offset:12288\n"
"v_exp_f32 v44, v44\n"
"MA v,16,a,232,a,64\n"
"v_exp_f32 v45, v45\n"
"v_mfma_f32_16x16x32_bf16 v[32:35], a[192:195], a[0:3], v[208:211]\n"
"v_exp_f32 v46, v46\n"
"v_exp_f32 v47, v47\n"
"MA v,32,a,196,a,16\n"
"v_exp_f32 v60, v60\n"
"v_cvt_pk_bf16_f32 v44, v44, v45\n"
"v_mfma_f32_16x16x32_bf16 v[36:39], a[196:199], a[20:23], v[212:215]\n"
"v_exp_f32 v61, v61\n"
"v_cvt_pk_bf16_f32 v45, v46, v47\n"
"MA v,36,a,192,a,4\n"
"v_exp_f32 v62, v62\n"
"v_exp_f32 v63, v63\n"
"v_mfma_f32_16x16x32_bf16 v[40:43], a[192:195], a[8:11], v[216:219]\n"
"s_cmp_eq_u32 s53, 3\n"
"s_cselect_b32 s89, s70, s55\n"
"v_cvt_pk_bf16_f32 v46, v60, v61\n"
"v_cvt_pk_bf16_f32 v47, v62, v63\n"
"v_accvgpr_write_b32 a124, v44\n"
"MA v,40,a,196,a,24\n"
"s_mov_b32 s86, 98560\n"
"v_accvgpr_write_b32 a125, v45\n"
"v_accvgpr_write_b32 a126, v46\n"
"v_accvgpr_write_b32 a127, v47\n"
"v_mfma_f32_16x16x32_bf16 v[44:47], a[196:199], a[28:31], v[220:223]\n"
"buffer_load_dwordx4 v248, s[20:23], s91 offen offset:1024 lds\n"
"MA v,44,a,192,a,12\n"
"s_waitcnt lgkmcnt(0)\n"
"ds_read_b128 a[216:219], v244 offset:14336\n"
"v_mfma_f32_16x16x32_bf16 v[60:63], a[200:203], a[12:15], v[220:223]\n"
"buffer_load_dword v235, v233, s[24:27], s89 offen\n"
"MA v,60,a,204,a,28\n"
"s_add_u32 m0, s44, 131072\n"
"v_mfma_f32_16x16x32_bf16 v[56:59], a[204:207], a[24:27], v[216:219]\n"
"ds_read_b128 a[220:223], v245 offset:14336\n"
"MA v,56,a,200,a,8\n"
"buffer_load_dwordx4 v248, s[20:23], s86 offen lds\n"
"v_mfma_f32_16x16x32_bf16 v[52:55], a[200:203], a[4:7], v[212:215]\n"
"ds_read_b128 a[224:227], v244 offset:20480\n"
"MA v,52,a,204,a,20\n"
"ds_read_b128 a[228:231], v245 offset:20480\n"
"v_mfma_f32_16x16x32_bf16 v[48:51], a[204:207], a[16:19], v[208:211]\n"
"v_exp_f32 v0, v0\n"
"MA v,48,a,200,a,0\n"
"v_exp_f32 v1, v1\n"
"MA v,32,a,208,a,32\n"
"ds_read_b128 a[232:235], v244 offset:22528\n"
"v_exp_f32 v2, v2\n"
"MA v,32,a,212,a,48\n"
"ds_read_b128 a[236:239], v245 offset:22528\n"
"v_exp_f32 v3, v3\n"
"MA v,36,a,212,a,52\n"
"v_exp_f32 v16, v16\n"
"v_cvt_pk_bf16_f32 v0, v0, v1\n"
"MA v,36,a,208,a,36\n"
"s_mov_b32 s87, 100608\n"
"v_exp_f32 v17, v17\n"
"MA v,40,a,208,a,40\n"
"v_exp_f32 v18, v18\n"
"v_cvt_pk_bf16_f32 v1, v2, v3\n"
"MA v,40,a,212,a,56\n"
"v_exp_f32 v19, v19\n"
"v_cvt_pk_bf16_f32 v2, v16, v17\n"
"MA v,44,a,212,a,60\n"
"buffer_load_dwordx4 v248, s[20:23], s87 offen offset:1024 lds\n"
"MA v,44,a,208,a,44\n"
"s_waitcnt lgkmcnt(2)\n"
"MA v,60,a,216,a,44\n"
"s_waitcnt vmcnt(11)\n"
"s_barrier\n"
"ds_read_b128 a[192:195], v244 offset:24576\n"
"v_cvt_pk_bf16_f32 v3, v18, v19\n"
"MA v,60,a,220,a,60\n"
"ds_read_b128 a[196:199], v245 offset:24576\n"
"v_accvgpr_write_b32 a128, v0\n"
"v_accvgpr_write_b32 a129, v1\n"
"MA v,56,a,220,a,56\n"
"v_accvgpr_write_b32 a130, v2\n"
"v_accvgpr_write_b32 a131, v3\n"
"MA v,56,a,216,a,40\n"
"v_exp_f32 v4, v4\n"
"MA v,52,a,216,a,36\n"
"v_exp_f32 v5, v5\n"
"MA v,52,a,220,a,52\n"
"v_exp_f32 v6, v6\n"
"v_cvt_pk_bf16_f32 v4, v4, v5\n"
"MA v,48,a,220,a,48\n"
"v_exp_f32 v7, v7\n"
"v_accvgpr_write_b32 a132, v4\n"
"MA v,48,a,216,a,32\n"
"v_exp_f32 v20, v20\n"
"v_cvt_pk_bf16_f32 v5, v6, v7\n"
"MA v,32,a,224,a,64\n"
"v_exp_f32 v21, v21\n"
"v_accvgpr_write_b32 a133, v5\n"
"MA v,32,a,228,a,80\n"
"v_exp_f32 v22, v22\n"
"MA v,36,a,228,a,84\n"
"v_exp_f32 v23, v23\n"
"v_cvt_pk_bf16_f32 v6, v20, v21\n"
"MA v,36,a,224,a,68\n"
"ds_read_b128 a[200:203], v244 offset:26624\n"
"v_cvt_pk_bf16_f32 v7, v22, v23\n"
"v_accvgpr_write_b32 a134, v6\n"
"MA v,40,a,224,a,72\n"
"v_accvgpr_write_b32 a135, v7\n"
"v_exp_f32 v8, v8\n"
"MA v,40,a,228,a,88\n"
"ds_read_b128 a[204:207], v245 offset:26624\n"
"v_exp_f32 v9, v9\n"
"MA v,44,a,228,a,92\n"
"v_exp_f32 v10, v10\n"
"v_cvt_pk_bf16_f32 v8, v8, v9\n"
"MA v,44,a,224,a,76\n"
"s_waitcnt lgkmcnt(2)\n"
"v_exp_f32 v11, v11\n"
"MA v,60,a,232,a,76\n"
"v_exp_f32 v24, v24\n"
"v_cvt_pk_bf16_f32 v9, v10, v11\n"
"MA v,60,a,236,a,92\n"
"v_exp_f32 v25, v25\n"
"v_accvgpr_write_b32 a136, v8\n"
"MA v,56,a,236,a,88\n"
"v_exp_f32 v26, v26\n"
"v_cvt_pk_bf16_f32 v10, v24, v25\n"
"MA v,56,a,232,a,72\n"
"v_exp_f32 v27, v27\n"
"v_accvgpr_write_b32 a137, v9\n"
"MA v,52,a,232,a,68\n"
"ds_read_b128 a[208:211], v244 offset:32768\n"
"v_cvt_pk_bf16_f32 v11, v26, v27\n"
"v_accvgpr_write_b32 a138, v10\n"
"MA v,52,a,236,a,84\n"
"s_add_u32 m0, s44, 139264\n"
"s_mov_b32 s90, 122880\n"
"v_accvgpr_write_b32 a139, v11\n"
"MA v,48,a,236,a,80\n"
"ds_read_b128 a[212:215], v245 offset:32768\n"
"v_exp_f32 v12, v12\n"
"MA v,48,a,232,a,64\n"
"v_exp_f32 v13, v13\n"
"v_exp_f32 v14, v14\n"
"v_mfma_f32_16x16x32_bf16 v[0:3], a[192:195], a[0:3], v[208:211]\n"
"v_exp_f32 v15, v15\n"
"v_cvt_pk_bf16_f32 v12, v12, v13\n"
"MA v,0,a,196,a,16\n"
"v_exp_f32 v28, v28\n"
"v_cvt_pk_bf16_f32 v13, v14, v15\n"
"v_mfma_f32_16x16x32_bf16 v[4:7], a[196:199], a[20:23], v[212:215]\n"
"v_exp_f32 v29, v29\n"
"v_accvgpr_write_b32 a140, v12\n"
"MA v,4,a,192,a,4\n"
"v_exp_f32 v30, v30\n"
"v_exp_f32 v31, v31\n"
"v_mfma_f32_16x16x32_bf16 v[8:11], a[192:195], a[8:11], v[216:219]\n"
"v_cvt_pk_bf16_f32 v14, v28, v29\n"
"v_cvt_pk_bf16_f32 v15, v30, v31\n"
"MA v,8,a,196,a,24\n"
"v_accvgpr_write_b32 a141, v13\n"
"v_accvgpr_write_b32 a142, v14\n"
"v_accvgpr_write_b32 a143, v15\n"
"v_mfma_f32_16x16x32_bf16 v[12:15], a[196:199], a[28:31], v[220:223]\n"
"buffer_load_dwordx4 v248, s[20:23], s90 offen lds\n"
"MA v,12,a,192,a,12\n"
"s_waitcnt lgkmcnt(0)\n"
"ds_read_b128 a[216:219], v244 offset:34816\n"
"v_mfma_f32_16x16x32_bf16 v[28:31], a[200:203], a[12:15], v[220:223]\n"
"s_mov_b32 s91, 124928\n"
"ds_read_b128 a[220:223], v245 offset:34816\n"
"MA v,28,a,204,a,28\n"
"buffer_load_dwordx4 v248, s[20:23], s91 offen offset:1024 lds\n"
"v_mfma_f32_16x16x32_bf16 v[24:27], a[204:207], a[24:27], v[216:219]\n"
"s_add_u32 m0, s44, 147456\n"
"s_mov_b32 s86, 123008\n"
"ds_read_b128 a[224:227], v244 offset:40960\n"
"MA v,24,a,200,a,8\n"
"buffer_load_dwordx4 v248, s[20:23], s86 offen lds\n"
"v_mfma_f32_16x16x32_bf16 v[20:23], a[200:203], a[4:7], v[212:215]\n"
"s_cmp_eq_u32 s53, 4\n"
"s_cselect_b32 s86, s70, s55\n"
"ds_read_b128 a[228:231], v245 offset:40960\n"
"v_exp_f32 v32, v32\n"
"MA v,20,a,204,a,20\n"
"buffer_load_dword v235, v233, s[24:27], s86 offen\n"
"v_mfma_f32_16x16x32_bf16 v[16:19], a[204:207], a[16:19], v[208:211]\n"
"v_exp_f32 v33, v33\n"
"MA v,16,a,200,a,0\n"
"v_exp_f32 v34, v34\n"
"v_cvt_pk_bf16_f32 v32, v32, v33\n"
"MA v,0,a,208,a,32\n"
"ds_read_b128 a[232:235], v244 offset:43008\n"
"v_exp_f32 v35, v35\n"
"MA v,0,a,212,a,48\n"
"ds_read_b128 a[236:239], v245 offset:43008\n"
"v_exp_f32 v48, v48\n"
"MA v,4,a,212,a,52\n"
"v_exp_f32 v49, v49\n"
"v_cvt_pk_bf16_f32 v33, v34, v35\n"
"MA v,4,a,208,a,36\n"
"v_exp_f32 v50, v50\n"
"MA v,8,a,208,a,40\n"
"v_exp_f32 v51, v51\n"
"v_cvt_pk_bf16_f32 v34, v48, v49\n"
"MA v,8,a,212,a,56\n"
"v_cvt_pk_bf16_f32 v35, v50, v51\n"
"v_accvgpr_write_b32 a144, v32\n"
"v_accvgpr_write_b32 a145, v33\n"
"MA v,12,a,212,a,60\n"
"v_accvgpr_write_b32 a146, v34\n"
"v_accvgpr_write_b32 a147, v35\n"
"MA v,12,a,208,a,44\n"
"s_waitcnt lgkmcnt(2)\n"
"v_exp_f32 v36, v36\n"
"MA v,28,a,216,a,44\n"
"ds_read_b128 a[192:195], v244 offset:28672\n"
"v_exp_f32 v37, v37\n"
"MA v,28,a,220,a,60\n"
"s_add_u32 s90, s68, 5120\n"
"s_cmp_eq_u32 s53, 0\n"
"s_cselect_b32 s90, s90, s55\n"
"ds_read_b128 a[196:199], v245 offset:28672\n"
"v_exp_f32 v38, v38\n"
"MA v,24,a,220,a,56\n"
"v_exp_f32 v39, v39\n"
"v_cvt_pk_bf16_f32 v36, v36, v37\n"
"MA v,24,a,216,a,40\n"
"v_exp_f32 v52, v52\n"
"v_cvt_pk_bf16_f32 v37, v38, v39\n"
"MA v,20,a,216,a,36\n"
"buffer_load_dword v235, v255, s[36:39], s90 offen\n"
"MA v,20,a,220,a,52\n"
"s_cmp_eq_u32 s53, 0\n"
"s_cselect_b32 s91, s69, s55\n"
"MA v,16,a,220,a,48\n"
"v_exp_f32 v53, v53\n"
"v_accvgpr_write_b32 a148, v36\n"
"MA v,16,a,216,a,32\n"
"v_exp_f32 v54, v54\n"
"v_cvt_pk_bf16_f32 v38, v52, v53\n"
"MA v,0,a,224,a,64\n"
"buffer_load_dword v235, v232, s[40:43], s91 offen\n"
"MA v,0,a,228,a,80\n"
"MA v,4,a,228,a,84\n"
"v_exp_f32 v55, v55\n"
"v_accvgpr_write_b32 a149, v37\n"
"MA v,4,a,224,a,68\n"
"ds_read_b128 a[200:203], v244 offset:30720\n"
"v_cvt_pk_bf16_f32 v39, v54, v55\n"
"v_accvgpr_write_b32 a150, v38\n"
"MA v,8,a,224,a,72\n"
"ds_read_b128 a[204:207], v245 offset:30720\n"
"v_accvgpr_write_b32 a151, v39\n"
"MA v,8,a,228,a,88\n"
"v_exp_f32 v40, v40\n"
"MA v,12,a,228,a,92\n"
"v_exp_f32 v41, v41\n"
"MA v,12,a,224,a,76\n"
"s_waitcnt lgkmcnt(2)\n"
"v_exp_f32 v42, v42\n"
"v_cvt_pk_bf16_f32 v40, v40, v41\n"
"MA v,28,a,232,a,76\n"
"v_exp_f32 v43, v43\n"
"v_accvgpr_write_b32 a152, v40\n"
"MA v,28,a,236,a,92\n"
"s_mov_b32 s87, 125056\n"
"v_exp_f32 v56, v56\n"
"v_cvt_pk_bf16_f32 v41, v42, v43\n"
"MA v,24,a,236,a,88\n"
"v_exp_f32 v57, v57\n"
"v_accvgpr_write_b32 a153, v41\n"
"MA v,24,a,232,a,72\n"
"v_exp_f32 v58, v58\n"
"v_cvt_pk_bf16_f32 v42, v56, v57\n"
"MA v,20,a,232,a,68\n"
"v_exp_f32 v59, v59\n"
"v_accvgpr_write_b32 a154, v42\n"
"MA v,20,a,236,a,84\n"
"v_cvt_pk_bf16_f32 v43, v58, v59\n"
"v_exp_f32 v44, v44\n"
"MA v,16,a,236,a,80\n"
"v_accvgpr_write_b32 a155, v43\n"
"v_exp_f32 v45, v45\n"
"MA v,16,a,232,a,64\n"
"v_exp_f32 v46, v46\n"
"v_cvt_pk_bf16_f32 v44, v44, v45\n"
"v_mfma_f32_16x16x32_bf16 v[32:35], a[192:195], a[0:3], v[208:211]\n"
"v_exp_f32 v47, v47\n"
"MA v,32,a,196,a,16\n"
"v_exp_f32 v60, v60\n"
"v_cvt_pk_bf16_f32 v45, v46, v47\n"
"v_mfma_f32_16x16x32_bf16 v[36:39], a[196:199], a[20:23], v[212:215]\n"
"v_exp_f32 v61, v61\n"
"v_accvgpr_write_b32 a156, v44\n"
"MA v,36,a,192,a,4\n"
"v_exp_f32 v62, v62\n"
"v_exp_f32 v63, v63\n"
"v_mfma_f32_16x16x32_bf16 v[40:43], a[192:195], a[8:11], v[216:219]\n"
"v_cvt_pk_bf16_f32 v46, v60, v61\n"
"v_cvt_pk_bf16_f32 v47, v62, v63\n"
"v_accvgpr_write_b32 a157, v45\n"
"MA v,40,a,196,a,24\n"
"s_mov_b32 s88, 123136\n"
"ds_read_b128 a[208:211], v244 offset:36864\n"
"v_accvgpr_write_b32 a158, v46\n"
"v_accvgpr_write_b32 a159, v47\n"
"v_mfma_f32_16x16x32_bf16 v[44:47], a[196:199], a[28:31], v[220:223]\n"
"buffer_load_dwordx4 v248, s[20:23], s87 offen offset:1024 lds\n"
"MA v,44,a,192,a,12\n"
"s_waitcnt lgkmcnt(1)\n"
"s_add_u32 m0, s44, 155648\n"
"v_mfma_f32_16x16x32_bf16 v[60:63], a[200:203], a[12:15], v[220:223]\n"
"ds_read_b128 a[212:215], v245 offset:36864\n"
"v_exp_f32 v0, v0\n"
"MA v,60,a,204,a,28\n"
"ds_read_b128 a[216:219], v244 offset:38912\n"
"v_exp_f32 v1, v1\n"
"v_mfma_f32_16x16x32_bf16 v[56:59], a[204:207], a[24:27], v[216:219]\n"
"s_mov_b32 s89, 125184\n"
"ds_read_b128 a[220:223], v245 offset:38912\n"
"v_cvt_pk_bf16_f32 v0, v0, v1\n"
"MA v,56,a,200,a,8\n"
"buffer_load_dwordx4 v248, s[20:23], s89 offen offset:1024 lds\n"
"v_mfma_f32_16x16x32_bf16 v[52:55], a[200:203], a[4:7], v[212:215]\n"
"s_cmp_eq_u32 s53, 5\n"
"s_cselect_b32 s89, s70, s55\n"
"buffer_load_dwordx4 v248, s[20:23], s88 offen lds\n"
"MA v,52,a,204,a,20\n"
"buffer_load_dword v235, v233, s[24:27], s89 offen\n"
"v_mfma_f32_16x16x32_bf16 v[48:51], a[204:207], a[16:19], v[208:211]\n"
"ds_read_b128 a[224:227], v244 offset:45056\n"
"MA v,48,a,200,a,0\n"
"s_waitcnt lgkmcnt(4)\n"
"ds_read_b128 a[228:231], v245 offset:45056\n"
"v_exp_f32 v2, v2\n"
"MA v,32,a,208,a,32\n"
"s_waitcnt lgkmcnt(4)\n"
"ds_read_b128 a[232:235], v244 offset:47104\n"
"v_exp_f32 v3, v3\n"
"MA v,32,a,212,a,48\n"
"ds_read_b128 a[236:239], v245 offset:47104\n"
"v_cvt_pk_bf16_f32 v1, v2, v3\n"
"MA v,36,a,212,a,52\n"
"v_exp_f32 v16, v16\n"
"v_accvgpr_write_b32 a160, v0\n"
"MA v,36,a,208,a,36\n"
"v_exp_f32 v17, v17\n"
"v_accvgpr_write_b32 a161, v1\n"
"MA v,40,a,208,a,40\n"
"v_exp_f32 v18, v18\n"
"v_cvt_pk_bf16_f32 v2, v16, v17\n"
"MA v,40,a,212,a,56\n"
"v_exp_f32 v19, v19\n"
"MA v,44,a,212,a,60\n"
"v_cvt_pk_bf16_f32 v3, v18, v19\n"
"v_accvgpr_write_b32 a162, v2\n"
"MA v,44,a,208,a,44\n"
"s_waitcnt vmcnt(10)\n"
"s_barrier\n"
"v_accvgpr_write_b32 a163, v3\n"
"s_waitcnt lgkmcnt(2)\n"
"MA v,60,a,216,a,44\n"
"ds_read_b128 a[192:195], v246 offset:0\n"
"v_exp_f32 v4, v4\n"
"MA v,60,a,220,a,60\n"
"s_add_u32 s87, s68, 5120\n"
"s_cmp_eq_u32 s53, 1\n"
"s_cselect_b32 s87, s87, s55\n"
"ds_read_b128 a[196:199], v247 offset:0\n"
"v_exp_f32 v5, v5\n"
"MA v,56,a,220,a,56\n"
"v_exp_f32 v6, v6\n"
"v_cvt_pk_bf16_f32 v4, v4, v5\n"
"MA v,56,a,216,a,40\n"
"v_exp_f32 v7, v7\n"
"v_accvgpr_write_b32 a164, v4\n"
"MA v,52,a,216,a,36\n"
"buffer_load_dword v235, v255, s[36:39], s87 offen\n"
"MA v,52,a,220,a,52\n"
"s_cmp_eq_u32 s53, 1\n"
"s_cselect_b32 s88, s69, s55\n"
"v_cvt_pk_bf16_f32 v5, v6, v7\n"
"MA v,48,a,220,a,48\n"
"v_exp_f32 v20, v20\n"
"MA v,48,a,216,a,32\n"
"v_exp_f32 v21, v21\n"
"v_accvgpr_write_b32 a165, v5\n"
"MA v,32,a,224,a,64\n"
"buffer_load_dword v235, v232, s[40:43], s88 offen\n"
"MA v,32,a,228,a,80\n"
"v_cvt_pk_bf16_f32 v6, v20, v21\n"
"MA v,36,a,228,a,84\n"
"v_exp_f32 v22, v22\n"
"v_accvgpr_write_b32 a166, v6\n"
"MA v,36,a,224,a,68\n"
"ds_read_b128 a[200:203], v246 offset:2048\n"
"v_exp_f32 v23, v23\n"
"MA v,40,a,224,a,72\n"
"ds_read_b128 a[204:207], v247 offset:2048\n"
"v_cvt_pk_bf16_f32 v7, v22, v23\n"
"MA v,40,a,228,a,88\n"
"v_accvgpr_write_b32 a167, v7\n"
"v_exp_f32 v8, v8\n"
"MA v,44,a,228,a,92\n"
"v_exp_f32 v9, v9\n"
"MA v,44,a,224,a,76\n"
"s_waitcnt lgkmcnt(2)\n"
"v_exp_f32 v10, v10\n"
"v_cvt_pk_bf16_f32 v8, v8, v9\n"
"MA v,60,a,232,a,76\n"
"v_exp_f32 v11, v11\n"
"v_accvgpr_write_b32 a168, v8\n"
"MA v,60,a,236,a,92\n"
"v_exp_f32 v24, v24\n"
"v_cvt_pk_bf16_f32 v9, v10, v11\n"
"MA v,56,a,236,a,88\n"
"v_exp_f32 v25, v25\n"
"v_accvgpr_write_b32 a169, v9\n"
"MA v,56,a,232,a,72\n"
"v_exp_f32 v26, v26\n"
"v_cvt_pk_bf16_f32 v10, v24, v25\n"
"MA v,52,a,232,a,68\n"
"v_exp_f32 v27, v27\n"
"v_accvgpr_write_b32 a170, v10\n"
"MA v,52,a,236,a,84\n"
"s_add_u32 m0, s44, 65536\n"
"v_cvt_pk_bf16_f32 v11, v26, v27\n"
"v_exp_f32 v12, v12\n"
"MA v,48,a,236,a,80\n"
"s_mov_b32 s90, 147456\n"
"v_accvgpr_write_b32 a171, v11\n"
"v_exp_f32 v13, v13\n"
"MA v,48,a,232,a,64\n"
"v_exp_f32 v14, v14\n"
"v_cvt_pk_bf16_f32 v12, v12, v13\n"
"v_mfma_f32_16x16x32_bf16 v[0:3], a[192:195], a[0:3], v[208:211]\n"
"v_exp_f32 v15, v15\n"
"MA v,0,a,196,a,16\n"
"v_exp_f32 v28, v28\n"
"v_cvt_pk_bf16_f32 v13, v14, v15\n"
"v_mfma_f32_16x16x32_bf16 v[4:7], a[196:199], a[20:23], v[212:215]\n"
"v_exp_f32 v29, v29\n"
"v_accvgpr_write_b32 a172, v12\n"
"MA v,4,a,192,a,4\n"
"v_exp_f32 v30, v30\n"
"v_exp_f32 v31, v31\n"
"v_mfma_f32_16x16x32_bf16 v[8:11], a[192:195], a[8:11], v[216:219]\n"
"v_cvt_pk_bf16_f32 v14, v28, v29\n"
"v_cvt_pk_bf16_f32 v15, v30, v31\n"
"v_accvgpr_write_b32 a173, v13\n"
"MA v,8,a,196,a,24\n"
"ds_read_b128 a[208:211], v246 offset:8192\n"
"v_accvgpr_write_b32 a174, v14\n"
"v_accvgpr_write_b32 a175, v15\n"
"v_mfma_f32_16x16x32_bf16 v[12:15], a[196:199], a[28:31], v[220:223]\n"
"buffer_load_dwordx4 v248, s[20:23], s90 offen lds\n"
"MA v,12,a,192,a,12\n"
"s_waitcnt lgkmcnt(1)\n"
"ds_read_b128 a[212:215], v247 offset:8192\n"
"v_mfma_f32_16x16x32_bf16 v[28:31], a[200:203], a[12:15], v[220:223]\n"
"s_mov_b32 s91, 149504\n"
"ds_read_b128 a[216:219], v246 offset:10240\n"
"MA v,28,a,204,a,28\n"
"buffer_load_dwordx4 v248, s[20:23], s91 offen offset:1024 lds\n"
"v_mfma_f32_16x16x32_bf16 v[24:27], a[204:207], a[24:27], v[216:219]\n"
"s_add_u32 m0, s44, 73728\n"
"s_mov_b32 s86, 147584\n"
"ds_read_b128 a[220:223], v247 offset:10240\n"
"MA v,24,a,200,a,8\n"
"buffer_load_dwordx4 v248, s[20:23], s86 offen lds\n"
"v_mfma_f32_16x16x32_bf16 v[20:23], a[200:203], a[4:7], v[212:215]\n"
"s_cmp_eq_u32 s53, 6\n"
"s_cselect_b32 s86, s70, s55\n"
"ds_read_b128 a[224:227], v246 offset:16384\n"
"v_exp_f32 v32, v32\n"
"MA v,20,a,204,a,20\n"
"buffer_load_dword v235, v233, s[24:27], s86 offen\n"
"v_mfma_f32_16x16x32_bf16 v[16:19], a[204:207], a[16:19], v[208:211]\n"
"ds_read_b128 a[228:231], v247 offset:16384\n"
"v_exp_f32 v33, v33\n"
"MA v,16,a,200,a,0\n"
"s_waitcnt lgkmcnt(4)\n"
"v_exp_f32 v34, v34\n"
"v_cvt_pk_bf16_f32 v32, v32, v33\n"
"MA v,0,a,208,a,32\n"
"ds_read_b128 a[232:235], v246 offset:18432\n"
"v_exp_f32 v35, v35\n"
"MA v,0,a,212,a,48\n"
"ds_read_b128 a[236:239], v247 offset:18432\n"
"v_cvt_pk_bf16_f32 v33, v34, v35\n"
"MA v,4,a,212,a,52\n"
"v_exp_f32 v48, v48\n"
"v_accvgpr_write_b32 a176, v32\n"
"MA v,4,a,208,a,36\n"
"v_exp_f32 v49, v49\n"
"v_accvgpr_write_b32 a177, v33\n"
"MA v,8,a,208,a,40\n"
"v_exp_f32 v50, v50\n"
"v_cvt_pk_bf16_f32 v34, v48, v49\n"
"MA v,8,a,212,a,56\n"
"v_exp_f32 v51, v51\n"
"MA v,12,a,212,a,60\n"
"v_cvt_pk_bf16_f32 v35, v50, v51\n"
"v_accvgpr_write_b32 a178, v34\n"
"MA v,12,a,208,a,44\n"
"s_waitcnt lgkmcnt(2)\n"
"v_accvgpr_write_b32 a179, v35\n"
"v_exp_f32 v36, v36\n"
"MA v,28,a,216,a,44\n"
"ds_read_b128 a[192:195], v246 offset:4096\n"
"v_exp_f32 v37, v37\n"
"MA v,28,a,220,a,60\n"
"s_add_u32 s90, s68, 5120\n"
"s_cmp_eq_u32 s53, 2\n"
"s_cselect_b32 s90, s90, s55\n"
"ds_read_b128 a[196:199], v247 offset:4096\n"
"v_exp_f32 v38, v38\n"
"MA v,24,a,220,a,56\n"
"v_exp_f32 v39, v39\n"
"v_cvt_pk_bf16_f32 v36, v36, v37\n"
"MA v,24,a,216,a,40\n"
"v_exp_f32 v52, v52\n"
"MA v,20,a,216,a,36\n"
"buffer_load_dword v235, v255, s[36:39], s90 offen\n"
"MA v,20,a,220,a,52\n"
"s_cmp_eq_u32 s53, 2\n"
"s_cselect_b32 s91, s69, s55\n"
"v_cvt_pk_bf16_f32 v37, v38, v39\n"
"MA v,16,a,220,a,48\n"
"v_exp_f32 v53, v53\n"
"v_accvgpr_write_b32 a180, v36\n"
"MA v,16,a,216,a,32\n"
"v_exp_f32 v54, v54\n"
"v_cvt_pk_bf16_f32 v38, v52, v53\n"
"MA v,0,a,224,a,64\n"
"buffer_load_dword v235, v232, s[40:43], s91 offen\n"
"MA v,0,a,228,a,80\n"
"MA v,4,a,228,a,84\n"
"v_exp_f32 v55, v55\n"
"v_accvgpr_write_b32 a181, v37\n"
"MA v,4,a,224,a,68\n"
"ds_read_b128 a[200:203], v246 offset:6144\n"
"v_cvt_pk_bf16_f32 v39, v54, v55\n"
"v_accvgpr_write_b32 a182, v38\n"
"MA v,8,a,224,a,72\n"
"ds_read_b128 a[204:207], v247 offset:6144\n"
"v_accvgpr_write_b32 a183, v39\n"
"MA v,8,a,228,a,88\n"
"v_exp_f32 v40, v40\n"
"MA v,12,a,228,a,92\n"
"v_exp_f32 v41, v41\n"
"MA v,12,a,224,a,76\n"
"s_waitcnt lgkmcnt(2)\n"
"v_exp_f32 v42, v42\n"
"v_cvt_pk_bf16_f32 v40, v40, v41\n"
"MA v,28,a,232,a,76\n"
"v_exp_f32 v43, v43\n"
"v_accvgpr_write_b32 a184, v40\n"
"MA v,28,a,236,a,92\n"
"s_mov_b32 s87, 149632\n"
"v_exp_f32 v56, v56\n"
"v_cvt_pk_bf16_f32 v41, v42, v43\n"
"MA v,24,a,236,a,88\n"
"v_exp_f32 v57, v57\n"
"v_accvgpr_write_b32 a185, v41\n"
"MA v,24,a,232,a,72\n"
"v_exp_f32 v58, v58\n"
"v_cvt_pk_bf16_f32 v42, v56, v57\n"
"MA v,20,a,232,a,68\n"
"v_exp_f32 v59, v59\n"
"v_accvgpr_write_b32 a186, v42\n"
"MA v,20,a,236,a,84\n"
"v_cvt_pk_bf16_f32 v43, v58, v59\n"
"v_exp_f32 v44, v44\n"
"MA v,16,a,236,a,80\n"
"v_accvgpr_write_b32 a187, v43\n"
"v_exp_f32 v45, v45\n"
"MA v,16,a,232,a,64\n"
"v_exp_f32 v46, v46\n"
"v_cvt_pk_bf16_f32 v44, v44, v45\n"
"v_mfma_f32_16x16x32_bf16 v[32:35], a[192:195], a[0:3], v[208:211]\n"
"v_exp_f32 v47, v47\n"
"MA v,32,a,196,a,16\n"
"v_exp_f32 v60, v60\n"
"v_cvt_pk_bf16_f32 v45, v46, v47\n"
"v_mfma_f32_16x16x32_bf16 v[36:39], a[196:199], a[20:23], v[212:215]\n"
"v_exp_f32 v61, v61\n"
"v_accvgpr_write_b32 a188, v44\n"
"MA v,36,a,192,a,4\n"
"v_exp_f32 v62, v62\n"
"v_exp_f32 v63, v63\n"
"v_mfma_f32_16x16x32_bf16 v[40:43], a[192:195], a[8:11], v[216:219]\n"
"v_cvt_pk_bf16_f32 v46, v60, v61\n"
"v_cvt_pk_bf16_f32 v47, v62, v63\n"
"v_accvgpr_write_b32 a189, v45\n"
"MA v,40,a,196,a,24\n"
"s_mov_b32 s88, 147712\n"
"v_accvgpr_write_b32 a190, v46\n"
"v_accvgpr_write_b32 a191, v47\n"
"v_mfma_f32_16x16x32_bf16 v[44:47], a[196:199], a[28:31], v[220:223]\n"
"buffer_load_dwordx4 v248, s[20:23], s87 offen offset:1024 lds\n"
"MA v,44,a,192,a,12\n"
"s_waitcnt lgkmcnt(0)\n"
"v_mfma_f32_16x16x32_bf16 v[60:63], a[200:203], a[12:15], v[220:223]\n"
"s_add_u32 m0, s44, 81920\n"
"ds_read_b128 a[208:211], v246 offset:12288\n"
"MA v,60,a,204,a,28\n"
"ds_read_b128 a[212:215], v247 offset:12288\n"
"v_mfma_f32_16x16x32_bf16 v[56:59], a[204:207], a[24:27], v[216:219]\n"
"s_mov_b32 s89, 149760\n"
"ds_read_b128 a[216:219], v246 offset:14336\n"
"v_exp_f32 v0, v0\n"
"MA v,56,a,200,a,8\n"
"buffer_load_dwordx4 v248, s[20:23], s89 offen offset:1024 lds\n"
"v_mfma_f32_16x16x32_bf16 v[52:55], a[200:203], a[4:7], v[212:215]\n"
"s_cmp_eq_u32 s53, 7\n"
"s_cselect_b32 s89, s70, s55\n"
"ds_read_b128 a[220:223], v247 offset:14336\n"
"MA v,52,a,204,a,20\n"
"buffer_load_dword v235, v233, s[24:27], s89 offen\n"
"v_mfma_f32_16x16x32_bf16 v[48:51], a[204:207], a[16:19], v[208:211]\n"
"buffer_load_dwordx4 v248, s[20:23], s88 offen lds\n"
"MA v,48,a,200,a,0\n"
"s_waitcnt lgkmcnt(3)\n"
"MA v,32,a,208,a,32\n"
"s_waitcnt lgkmcnt(2)\n"
"ds_read_b128 a[224:227], v246 offset:20480\n"
"MA v,32,a,212,a,48\n"
"ds_read_b128 a[228:231], v247 offset:20480\n"
"v_exp_f32 v1, v1\n"
"MA v,36,a,212,a,52\n"
"ds_read_b128 a[232:235], v246 offset:22528\n"
"v_cvt_pk_bf16_f32 v96, v0, v1\n"
"MA v,36,a,208,a,36\n"
"ds_read_b128 a[236:239], v247 offset:22528\n"
"MA v,40,a,208,a,40\n"
"v_exp_f32 v2, v2\n"
"MA v,40,a,212,a,56\n"
"v_exp_f32 v3, v3\n"
"MA v,44,a,212,a,60\n"
"v_cvt_pk_bf16_f32 v97, v2, v3\n"
"v_exp_f32 v4, v4\n"
"MA v,44,a,208,a,44\n"
"s_waitcnt vmcnt(13)\n"
"s_barrier\n"
"s_waitcnt lgkmcnt(4)\n"
"MA v,60,a,216,a,44\n"
"ds_read_b128 a[192:195], v246 offset:24576\n"
"v_exp_f32 v5, v5\n"
"MA v,60,a,220,a,60\n"
"s_add_u32 s87, s68, 5120\n"
"s_cmp_eq_u32 s53, 3\n"
"s_cselect_b32 s87, s87, s55\n"
"ds_read_b128 a[196:199], v247 offset:24576\n"
"v_cvt_pk_bf16_f32 v100, v4, v5\n"
"MA v,56,a,220,a,56\n"
"v_exp_f32 v6, v6\n"
"MA v,56,a,216,a,40\n"
"v_exp_f32 v7, v7\n"
"MA v,52,a,216,a,36\n"
"buffer_load_dword v235, v255, s[36:39], s87 offen\n"
"MA v,52,a,220,a,52\n"
"s_cmp_eq_u32 s53, 3\n"
"s_cselect_b32 s88, s69, s55\n"
"MA v,48,a,220,a,48\n"
"v_cvt_pk_bf16_f32 v101, v6, v7\n"
"MA v,48,a,216,a,32\n"
"s_waitcnt lgkmcnt(2)\n"
"v_exp_f32 v8, v8\n"
"MA v,32,a,224,a,64\n"
"buffer_load_dword v235, v232, s[40:43], s88 offen\n"
"MA v,32,a,228,a,80\n"
"MA v,36,a,228,a,84\n"
"ds_read_b128 a[200:203], v246 offset:26624\n"
"MA v,36,a,224,a,68\n"
"ds_read_b128 a[204:207], v247 offset:26624\n"
"v_exp_f32 v9, v9\n"
"MA v,40,a,224,a,72\n"
"v_exp_f32 v10, v10\n"
"MA v,40,a,228,a,88\n"
"v_exp_f32 v11, v11\n"
"MA v,44,a,228,a,92\n"
"v_cvt_pk_bf16_f32 v104, v8, v9\n"
"v_cvt_pk_bf16_f32 v105, v10, v11\n"
"MA v,44,a,224,a,76\n"
"v_exp_f32 v12, v12\n"
"MA v,60,a,232,a,76\n"
"ds_read_b128 a[208:211], v246 offset:32768\n"
"v_exp_f32 v13, v13\n"
"MA v,60,a,236,a,92\n"
"v_exp_f32 v14, v14\n"
"MA v,56,a,236,a,88\n"
"v_exp_f32 v15, v15\n"
"MA v,56,a,232,a,72\n"
"ds_read_b128 a[212:215], v247 offset:32768\n"
"v_cvt_pk_bf16_f32 v108, v12, v13\n"
"v_cvt_pk_bf16_f32 v109, v14, v15\n"
"MA v,52,a,232,a,68\n"
"v_exp_f32 v28, v28\n"
"MA v,52,a,236,a,84\n"
"s_add_u32 m0, s44, 90112\n"
"v_exp_f32 v29, v29\n"
"MA v,48,a,236,a,80\n"
"s_mov_b32 s90, 172032\n"
"v_exp_f32 v30, v30\n"
"v_cvt_pk_bf16_f32 v110, v28, v29\n"
"MA v,48,a,232,a,64\n"
"s_waitcnt lgkmcnt(2)\n"
"v_exp_f32 v31, v31\n"
"v_mfma_f32_16x16x32_bf16 v[0:3], a[192:195], a[0:3], v[208:211]\n"
"v_exp_f32 v24, v24\n"
"v_cvt_pk_bf16_f32 v111, v30, v31\n"
"MA v,0,a,196,a,16\n"
"v_exp_f32 v25, v25\n"
"v_mfma_f32_16x16x32_bf16 v[4:7], a[196:199], a[20:23], v[212:215]\n"
"v_exp_f32 v26, v26\n"
"v_cvt_pk_bf16_f32 v106, v24, v25\n"
"MA v,4,a,192,a,4\n"
"v_exp_f32 v27, v27\n"
"v_mfma_f32_16x16x32_bf16 v[8:11], a[192:195], a[8:11], v[216:219]\n"
"v_exp_f32 v20, v20\n"
"v_cvt_pk_bf16_f32 v107, v26, v27\n"
"MA v,8,a,196,a,24\n"
"v_exp_f32 v21, v21\n"
"v_mfma_f32_16x16x32_bf16 v[12:15], a[196:199], a[28:31], v[220:223]\n"
"v_exp_f32 v22, v22\n"
"v_cvt_pk_bf16_f32 v102, v20, v21\n"
"MA v,12,a,192,a,12\n"
"v_exp_f32 v23, v23\n"
"v_mfma_f32_16x16x32_bf16 v[28:31], a[200:203], a[12:15], v[220:223]\n"
"v_exp_f32 v16, v16\n"
"v_cvt_pk_bf16_f32 v103, v22, v23\n"
"MA v,28,a,204,a,28\n"
"v_exp_f32 v17, v17\n"
"v_mfma_f32_16x16x32_bf16 v[24:27], a[204:207], a[24:27], v[216:219]\n"
"v_exp_f32 v18, v18\n"
"v_cvt_pk_bf16_f32 v98, v16, v17\n"
"MA v,24,a,200,a,8\n"
"s_mov_b32 s91, 174080\n"
"v_exp_f32 v19, v19\n"
"v_mfma_f32_16x16x32_bf16 v[20:23], a[200:203], a[4:7], v[212:215]\n"
"ds_read_b128 a[216:219], v246 offset:34816\n"
"v_cvt_pk_bf16_f32 v99, v18, v19\n"
"MA v,20,a,204,a,20\n"
"buffer_load_dwordx4 v248, s[20:23], s90 offen lds\n"
"v_mfma_f32_16x16x32_bf16 v[16:19], a[204:207], a[16:19], v[208:211]\n"
"MA v,16,a,200,a,0\n"
"s_waitcnt lgkmcnt(1)\n"
"ds_read_b128 a[220:223], v247 offset:34816\n"
"v_exp_f32 v32, v32\n"
"MA v,0,a,208,a,32\n"
"ds_read_b128 a[224:227], v246 offset:40960\n"
"MA v,0,a,212,a,48\n"
"ds_read_b128 a[228:231], v247 offset:40960\n"
"v_exp_f32 v33, v33\n"
"MA v,4,a,212,a,52\n"
"buffer_load_dwordx4 v248, s[20:23], s91 offen offset:1024 lds\n"
"MA v,4,a,208,a,36\n"
"MA v,8,a,208,a,40\n"
"ds_read_b128 a[232:235], v246 offset:43008\n"
"v_cvt_pk_bf16_f32 v112, v32, v33\n"
"MA v,8,a,212,a,56\n"
"ds_read_b128 a[236:239], v247 offset:43008\n"
"v_exp_f32 v34, v34\n"
"MA v,12,a,212,a,60\n"
"v_exp_f32 v35, v35\n"
"MA v,12,a,208,a,44\n"
"s_waitcnt lgkmcnt(4)\n"
"v_cvt_pk_bf16_f32 v113, v34, v35\n"
"v_exp_f32 v36, v36\n"
"MA v,28,a,216,a,44\n"
"ds_read_b128 a[192:195], v246 offset:28672\n"
"MA v,28,a,220,a,60\n"
"s_add_u32 m0, s44, 98304\n"
"s_add_u32 s90, s68, 5120\n"
"s_cmp_eq_u32 s53, 4\n"
"s_cselect_b32 s90, s90, s55\n"
"ds_read_b128 a[196:199], v247 offset:28672\n"
"MA v,24,a,220,a,56\n"
"s_mov_b32 s86, 172160\n"
"v_exp_f32 v37, v37\n"
"MA v,24,a,216,a,40\n"
"v_exp_f32 v38, v38\n"
"v_cvt_pk_bf16_f32 v116, v36, v37\n"
"MA v,20,a,216,a,36\n"
"buffer_load_dword v235, v255, s[36:39], s90 offen\n"
"MA v,20,a,220,a,52\n"
"s_cmp_eq_u32 s53, 4\n"
"s_cselect_b32 s91, s69, s55\n"
"MA v,16,a,220,a,48\n"
"buffer_load_dwordx4 v248, s[20:23], s86 offen lds\n"
"MA v,16,a,216,a,32\n"
"s_waitcnt lgkmcnt(2)\n"
"MA v,0,a,224,a,64\n"
"buffer_load_dword v235, v232, s[40:43], s91 offen\n"
"MA v,0,a,228,a,80\n"
"MA v,4,a,228,a,84\n"
"ds_read_b128 a[200:203], v246 offset:30720\n"
"v_exp_f32 v39, v39\n"
"MA v,4,a,224,a,68\n"
"ds_read_b128 a[204:207], v247 offset:30720\n"
"v_cvt_pk_bf16_f32 v117, v38, v39\n"
"MA v,8,a,224,a,72\n"
"v_exp_f32 v40, v40\n"
"MA v,8,a,228,a,88\n"
"v_exp_f32 v41, v41\n"
"MA v,12,a,228,a,92\n"
"v_exp_f32 v42, v42\n"
"v_cvt_pk_bf16_f32 v120, v40, v41\n"
"MA v,12,a,224,a,76\n"
"v_exp_f32 v43, v43\n"
"MA v,28,a,232,a,76\n"
"v_cvt_pk_bf16_f32 v121, v42, v43\n"
"v_exp_f32 v44, v44\n"
"MA v,28,a,236,a,92\n"
"s_mov_b32 s87, 174208\n"
"ds_read_b128 a[208:211], v246 offset:36864\n"
"v_exp_f32 v45, v45\n"
"MA v,24,a,236,a,88\n"
"v_exp_f32 v46, v46\n"
"MA v,24,a,232,a,72\n"
"v_exp_f32 v47, v47\n"
"v_cvt_pk_bf16_f32 v124, v44, v45\n"
"MA v,20,a,232,a,68\n"
"v_exp_f32 v60, v60\n"
"v_cvt_pk_bf16_f32 v125, v46, v47\n"
"MA v,20,a,236,a,84\n"
"v_exp_f32 v61, v61\n"
"MA v,16,a,236,a,80\n"
"v_exp_f32 v62, v62\n"
"v_cvt_pk_bf16_f32 v126, v60, v61\n"
"MA v,16,a,232,a,64\n"
"s_waitcnt lgkmcnt(1)\n"
"ds_read_b128 a[212:215], v247 offset:36864\n"
"v_exp_f32 v63, v63\n"
"v_mfma_f32_16x16x32_bf16 v[32:35], a[192:195], a[0:3], v[208:211]\n"
"v_exp_f32 v56, v56\n"
"v_cvt_pk_bf16_f32 v127, v62, v63\n"
"MA v,32,a,196,a,16\n"
"v_exp_f32 v57, v57\n"
"v_mfma_f32_16x16x32_bf16 v[36:39], a[196:199], a[20:23], v[212:215]\n"
"v_exp_f32 v58, v58\n"
"v_cvt_pk_bf16_f32 v122, v56, v57\n"
"MA v,36,a,192,a,4\n"
"ds_read_b128 a[216:219], v246 offset:38912\n"
"v_exp_f32 v59, v59\n"
"v_mfma_f32_16x16x32_bf16 v[40:43], a[192:195], a[8:11], v[216:219]\n"
"ds_read_b128 a[220:223], v247 offset:38912\n"
"v_cvt_pk_bf16_f32 v123, v58, v59\n"
"MA v,40,a,196,a,24\n"
"s_mov_b32 s88, 172288\n"
"v_exp_f32 v52, v52\n"
"v_mfma_f32_16x16x32_bf16 v[44:47], a[196:199], a[28:31], v[220:223]\n"
"v_exp_f32 v53, v53\n"
"MA v,44,a,192,a,12\n"
"v_exp_f32 v54, v54\n"
"v_exp_f32 v55, v55\n"
"v_mfma_f32_16x16x32_bf16 v[60:63], a[200:203], a[12:15], v[220:223]\n"
"buffer_load_dwordx4 v248, s[20:23], s87 offen offset:1024 lds\n"
"MA v,60,a,204,a,28\n"
"s_add_u32 m0, s44, 106496\n"
"v_mfma_f32_16x16x32_bf16 v[56:59], a[204:207], a[24:27], v[216:219]\n"
"v_exp_f32 v48, v48\n"
"v_cvt_pk_bf16_f32 v118, v52, v53\n"
"v_cvt_pk_bf16_f32 v119, v54, v55\n"
"MA v,56,a,200,a,8\n"
"ds_read_b128 a[224:227], v246 offset:45056\n"
"v_exp_f32 v49, v49\n"
"v_mfma_f32_16x16x32_bf16 v[52:55], a[200:203], a[4:7], v[212:215]\n"
"v_exp_f32 v50, v50\n"
"v_exp_f32 v51, v51\n"
"MA v,52,a,204,a,20\n"
"v_cvt_pk_bf16_f32 v114, v48, v49\n"
"v_cvt_pk_bf16_f32 v115, v50, v51\n"
"v_mfma_f32_16x16x32_bf16 v[48:51], a[204:207], a[16:19], v[208:211]\n"
"buffer_load_dwordx4 v248, s[20:23], s88 offen lds\n"
"MA v,48,a,200,a,0\n"
"s_waitcnt lgkmcnt(1)\n"
"s_mov_b32 s88, 0\n"
"MA v,32,a,208,a,32\n"
"ds_read_b128 a[228:231], v247 offset:45056\n"
"MA v,32,a,212,a,48\n"
"ds_read_b128 a[232:235], v246 offset:47104\n"
"v_exp_f32 v0, v0\n"
"MA v,36,a,212,a,52\n"
"ds_read_b128 a[236:239], v247 offset:47104\n"
"v_exp_f32 v1, v1\n"
"MA v,36,a,208,a,36\n"
"s_mov_b32 s89, 174336\n"
"v_exp_f32 v2, v2\n"
"v_cvt_pk_bf16_f32 v128, v0, v1\n"
"MA v,40,a,208,a,40\n"
"v_exp_f32 v3, v3\n"
"MA v,40,a,212,a,56\n"
"v_cvt_pk_bf16_f32 v129, v2, v3\n"
"v_exp_f32 v4, v4\n"
"MA v,44,a,212,a,60\n"
"buffer_load_dwordx4 v248, s[20:23], s89 offen offset:1024 lds\n"
"MA v,44,a,208,a,44\n"
"MA v,60,a,216,a,44\n"
"s_add_u32 m0, s46, 0\n"
"v_exp_f32 v5, v5\n"
"MA v,60,a,220,a,60\n"
"s_add_u32 s86, s68, 5120\n"
"s_cmp_eq_u32 s53, 5\n"
"s_cselect_b32 s86, s86, s55\n"
"buffer_load_dwordx4 v248, s[24:27], s88 offen lds\n"
"MA v,56,a,220,a,56\n"
"s_mov_b32 s89, 2048\n"
"s_waitcnt vmcnt(11)\n"
"s_barrier\n"
"ds_read_b128 a[192:195], v244 offset:0\n"
"v_cvt_pk_bf16_f32 v132, v4, v5\n"
"MA v,56,a,216,a,40\n"
"buffer_load_dwordx4 v248, s[24:27], s89 offen offset:1024 lds\n"
"MA v,52,a,216,a,36\n"
"MA v,52,a,220,a,52\n"
"s_cmp_eq_u32 s53, 5\n"
"s_cselect_b32 s87, s69, s55\n"
"buffer_load_dword v235, v255, s[36:39], s86 offen\n"
"MA v,48,a,220,a,48\n"
"ds_read_b128 a[196:199], v245 offset:0\n"
"MA v,48,a,216,a,32\n"
"s_waitcnt lgkmcnt(2)\n"
"v_exp_f32 v6, v6\n"
"MA v,32,a,224,a,64\n"
"buffer_load_dword v235, v232, s[40:43], s87 offen\n"
"MA v,32,a,228,a,80\n"
"ds_read_b128 a[200:203], v244 offset:2048\n"
"MA v,36,a,228,a,84\n"
"s_mov_b32 s90, 4096\n"
"v_exp_f32 v7, v7\n"
"MA v,36,a,224,a,68\n"
"v_cvt_pk_bf16_f32 v133, v6, v7\n"
"v_exp_f32 v8, v8\n"
"MA v,40,a,224,a,72\n"
"ds_read_b128 a[204:207], v245 offset:2048\n"
"v_exp_f32 v9, v9\n"
"MA v,40,a,228,a,88\n"
"v_exp_f32 v10, v10\n"
"v_cvt_pk_bf16_f32 v136, v8, v9\n"
"MA v,44,a,228,a,92\n"
"v_exp_f32 v11, v11\n"
"MA v,44,a,224,a,76\n"
"v_cvt_pk_bf16_f32 v137, v10, v11\n"
"v_exp_f32 v12, v12\n"
"MA v,60,a,232,a,76\n"
"ds_read_b128 a[208:211], v244 offset:8192\n"
"v_exp_f32 v13, v13\n"
"MA v,60,a,236,a,92\n"
"v_exp_f32 v14, v14\n"
"v_cvt_pk_bf16_f32 v140, v12, v13\n"
"MA v,56,a,236,a,88\n"
"v_exp_f32 v15, v15\n"
"MA v,56,a,232,a,72\n"
"v_exp_f32 v28, v28\n"
"v_cvt_pk_bf16_f32 v141, v14, v15\n"
"MA v,52,a,232,a,68\n"
"buffer_load_dwordx4 v248, s[24:27], s90 offen offset:2048 lds\n"
"MA v,52,a,236,a,84\n"
"MA v,48,a,236,a,80\n"
"ds_read_b128 a[212:215], v245 offset:8192\n"
"v_exp_f32 v29, v29\n"
"MA v,48,a,232,a,64\n"
"s_waitcnt lgkmcnt(2)\n"
"v_exp_f32 v30, v30\n"
"v_cvt_pk_bf16_f32 v142, v28, v29\n"
"v_mfma_f32_16x16x32_bf16 v[0:3], a[192:195], a[0:3], v[208:211]\n"
"ds_read_b128 a[216:219], v244 offset:10240\n"
"v_exp_f32 v31, v31\n"
"MA v,0,a,196,a,16\n"
"s_mov_b32 s91, 6144\n"
"s_mov_b32 s89, 12288\n"
"v_cvt_pk_bf16_f32 v143, v30, v31\n"
"v_mfma_f32_16x16x32_bf16 v[4:7], a[196:199], a[20:23], v[212:215]\n"
"ds_read_b128 a[220:223], v245 offset:10240\n"
"v_exp_f32 v24, v24\n"
"MA v,4,a,192,a,4\n"
"v_exp_f32 v25, v25\n"
"v_mfma_f32_16x16x32_bf16 v[8:11], a[192:195], a[8:11], v[216:219]\n"
"v_exp_f32 v26, v26\n"
"v_exp_f32 v27, v27\n"
"MA v,8,a,196,a,24\n"
"v_cvt_pk_bf16_f32 v138, v24, v25\n"
"v_cvt_pk_bf16_f32 v139, v26, v27\n"
"v_mfma_f32_16x16x32_bf16 v[12:15], a[196:199], a[28:31], v[220:223]\n"
"v_exp_f32 v20, v20\n"
"v_exp_f32 v21, v21\n"
"MA v,12,a,192,a,12\n"
"v_exp_f32 v22, v22\n"
"v_cvt_pk_bf16_f32 v134, v20, v21\n"
"v_mfma_f32_16x16x32_bf16 v[28:31], a[200:203], a[12:15], v[220:223]\n"
"ds_read_b128 a[224:227], v244 offset:16384\n"
"v_exp_f32 v23, v23\n"
"MA v,28,a,204,a,28\n"
"v_exp_f32 v16, v16\n"
"v_cvt_pk_bf16_f32 v135, v22, v23\n"
"v_mfma_f32_16x16x32_bf16 v[24:27], a[204:207], a[24:27], v[216:219]\n"
"s_mov_b32 s90, 14336\n"
"ds_read_b128 a[228:231], v245 offset:16384\n"
"v_exp_f32 v17, v17\n"
"MA v,24,a,200,a,8\n"
"v_exp_f32 v18, v18\n"
"v_exp_f32 v19, v19\n"
"v_mfma_f32_16x16x32_bf16 v[20:23], a[200:203], a[4:7], v[212:215]\n"
"v_cvt_pk_bf16_f32 v130, v16, v17\n"
"v_cvt_pk_bf16_f32 v131, v18, v19\n"
"MA v,20,a,204,a,20\n"
"buffer_load_dwordx4 v248, s[24:27], s91 offen offset:3072 lds\n"
"v_mfma_f32_16x16x32_bf16 v[16:19], a[204:207], a[16:19], v[208:211]\n"
"s_add_u32 m0, s46, 4096\n"
"v_exp_f32 v32, v32\n"
"MA v,16,a,200,a,0\n"
"s_waitcnt lgkmcnt(2)\n"
"buffer_load_dwordx4 v248, s[24:27], s89 offen lds\n"
"MA v,0,a,208,a,32\n"
"buffer_load_dwordx4 v248, s[24:27], s90 offen offset:1024 lds\n"
"MA v,0,a,212,a,48\n"
"MA v,4,a,212,a,52\n"
"ds_read_b128 a[232:235], v244 offset:18432\n"
"v_exp_f32 v33, v33\n"
"MA v,4,a,208,a,36\n"
"ds_read_b128 a[236:239], v245 offset:18432\n"
"v_exp_f32 v34, v34\n"
"MA v,8,a,208,a,40\n"
"v_exp_f32 v35, v35\n"
"MA v,8,a,212,a,56\n"
"s_mov_b32 s91, 16384\n"
"v_cvt_pk_bf16_f32 v144, v32, v33\n"
"v_cvt_pk_bf16_f32 v145, v34, v35\n"
"MA v,12,a,212,a,60\n"
"v_exp_f32 v36, v36\n"
"MA v,12,a,208,a,44\n"
"v_exp_f32 v37, v37\n"
"MA v,28,a,216,a,44\n"
"buffer_load_dwordx4 v248, s[24:27], s91 offen offset:2048 lds\n"
"MA v,28,a,220,a,60\n"
"s_add_u32 s87, s68, 5120\n"
"s_cmp_eq_u32 s53, 6\n"
"s_cselect_b32 s87, s87, s55\n"
"ds_read_b128 a[192:195], v244 offset:4096\n"
"MA v,24,a,220,a,56\n"
"ds_read_b128 a[196:199], v245 offset:4096\n"
"v_cvt_pk_bf16_f32 v148, v36, v37\n"
"MA v,24,a,216,a,40\n"
"v_exp_f32 v38, v38\n"
"MA v,20,a,216,a,36\n"
"buffer_load_dword v235, v255, s[36:39], s87 offen\n"
"MA v,20,a,220,a,52\n"
"s_cmp_eq_u32 s53, 6\n"
"s_cselect_b32 s88, s69, s55\n"
"MA v,16,a,220,a,48\n"
"v_exp_f32 v39, v39\n"
"MA v,16,a,216,a,32\n"
"s_waitcnt lgkmcnt(2)\n"
"v_cvt_pk_bf16_f32 v149, v38, v39\n"
"v_exp_f32 v40, v40\n"
"MA v,0,a,224,a,64\n"
"buffer_load_dword v235, v232, s[40:43], s88 offen\n"
"MA v,0,a,228,a,80\n"
"MA v,4,a,228,a,84\n"
"ds_read_b128 a[200:203], v244 offset:6144\n"
"v_exp_f32 v41, v41\n"
"MA v,4,a,224,a,68\n"
"v_exp_f32 v42, v42\n"
"MA v,8,a,224,a,72\n"
"ds_read_b128 a[204:207], v245 offset:6144\n"
"v_exp_f32 v43, v43\n"
"MA v,8,a,228,a,88\n"
"s_lshl_b32 s86, s44, 2\n"
"s_add_u32 s86, s86, L_JOIN%=-.Lfused_text0+24000\n"
"s_cmp_eq_u32 s53, 3\n"
"s_cselect_b32 s86, s86, s55\n"
"v_cvt_pk_bf16_f32 v152, v40, v41\n"
"v_cvt_pk_bf16_f32 v153, v42, v43\n"
"MA v,12,a,228,a,92\n"
"v_exp_f32 v44, v44\n"
"MA v,12,a,224,a,76\n"
"v_exp_f32 v45, v45\n"
"MA v,28,a,232,a,76\n"
"buffer_load_dword v235, v232, s[96:99], s86 offen\n"
"MA v,28,a,236,a,92\n"
"s_mov_b32 s86, 18432\n"
"v_cvt_pk_bf16_f32 v156, v44, v45\n"
"MA v,24,a,236,a,88\n"
"buffer_load_dwordx4 v248, s[24:27], s86 offen offset:3072 lds\n"
"MA v,24,a,232,a,72\n"
"MA v,20,a,232,a,68\n"
"v_exp_f32 v46, v46\n"
"MA v,20,a,236,a,84\n"
"ds_read_b128 a[208:211], v244 offset:12288\n"
"v_exp_f32 v47, v47\n"
"MA v,16,a,236,a,80\n"
"v_exp_f32 v60, v60\n"
"v_cvt_pk_bf16_f32 v157, v46, v47\n"
"MA v,16,a,232,a,64\n"
"s_waitcnt lgkmcnt(1)\n"
"v_exp_f32 v61, v61\n"
"v_mfma_f32_16x16x32_bf16 v[32:35], a[192:195], a[0:3], v[208:211]\n"
"v_exp_f32 v62, v62\n"
"v_cvt_pk_bf16_f32 v158, v60, v61\n"
"MA v,32,a,196,a,16\n"
"s_add_u32 m0, s46, 8192\n"
"ds_read_b128 a[212:215], v245 offset:12288\n"
"v_exp_f32 v63, v63\n"
"v_mfma_f32_16x16x32_bf16 v[36:39], a[196:199], a[20:23], v[212:215]\n"
"s_mov_b32 s89, 24576\n"
"ds_read_b128 a[216:219], v244 offset:14336\n"
"v_cvt_pk_bf16_f32 v159, v62, v63\n"
"MA v,36,a,192,a,4\n"
"ds_read_b128 a[220:223], v245 offset:14336\n"
"v_exp_f32 v56, v56\n"
"v_mfma_f32_16x16x32_bf16 v[40:43], a[192:195], a[8:11], v[216:219]\n"
"v_exp_f32 v57, v57\n"
"MA v,40,a,196,a,24\n"
"v_exp_f32 v58, v58\n"
"v_cvt_pk_bf16_f32 v154, v56, v57\n"
"v_mfma_f32_16x16x32_bf16 v[44:47], a[196:199], a[28:31], v[220:223]\n"
"v_exp_f32 v59, v59\n"
"MA v,44,a,192,a,12\n"
"v_exp_f32 v52, v52\n"
"v_cvt_pk_bf16_f32 v155, v58, v59\n"
"v_mfma_f32_16x16x32_bf16 v[60:63], a[200:203], a[12:15], v[220:223]\n"
"v_exp_f32 v53, v53\n"
"v_exp_f32 v54, v54\n"
"MA v,60,a,204,a,28\n"
"v_exp_f32 v55, v55\n"
"v_cvt_pk_bf16_f32 v150, v52, v53\n"
"v_mfma_f32_16x16x32_bf16 v[56:59], a[204:207], a[24:27], v[216:219]\n"
"s_mov_b32 s90, 26624\n"
"v_exp_f32 v48, v48\n"
"v_cvt_pk_bf16_f32 v151, v54, v55\n"
"MA v,56,a,200,a,8\n"
"ds_read_b128 a[224:227], v244 offset:20480\n"
"v_exp_f32 v49, v49\n"
"v_mfma_f32_16x16x32_bf16 v[52:55], a[200:203], a[4:7], v[212:215]\n"
"v_exp_f32 v50, v50\n"
"v_exp_f32 v51, v51\n"
"MA v,52,a,204,a,20\n"
"v_cvt_pk_bf16_f32 v146, v48, v49\n"
"v_cvt_pk_bf16_f32 v147, v50, v51\n"
"v_mfma_f32_16x16x32_bf16 v[48:51], a[204:207], a[16:19], v[208:211]\n"
"buffer_load_dwordx4 v248, s[24:27], s89 offen lds\n"
"MA v,48,a,200,a,0\n"
"s_waitcnt lgkmcnt(1)\n"
"MA v,32,a,208,a,32\n"
"buffer_load_dwordx4 v248, s[24:27], s90 offen offset:1024 lds\n"
"MA v,32,a,212,a,48\n"
"MA v,36,a,212,a,52\n"
"ds_read_b128 a[228:231], v245 offset:20480\n"
"MA v,36,a,208,a,36\n"
"ds_read_b128 a[232:235], v244 offset:22528\n"
"v_exp_f32 v0, v0\n"
"MA v,40,a,208,a,40\n"
"ds_read_b128 a[236:239], v245 offset:22528\n"
"v_exp_f32 v1, v1\n"
"MA v,40,a,212,a,56\n"
"s_mov_b32 s91, 28672\n"
"v_exp_f32 v2, v2\n"
"MA v,44,a,212,a,60\n"
"v_exp_f32 v3, v3\n"
"v_cvt_pk_bf16_f32 v160, v0, v1\n"
"MA v,44,a,208,a,44\n"
"s_waitcnt vmcnt(15)\n"
"s_barrier\n"
"v_cvt_pk_bf16_f32 v161, v2, v3\n"
"MA v,60,a,216,a,44\n"
"buffer_load_dwordx4 v248, s[24:27], s91 offen offset:2048 lds\n"
"MA v,60,a,220,a,60\n"
"s_add_u32 s87, s68, 5120\n"
"s_cmp_eq_u32 s53, 7\n"
"s_cselect_b32 s87, s87, s55\n"
"MA v,56,a,220,a,56\n"
"ds_read_b128 a[192:195], v244 offset:24576\n"
"v_exp_f32 v4, v4\n"
"MA v,56,a,216,a,40\n"
"ds_read_b128 a[196:199], v245 offset:24576\n"
"MA v,52,a,216,a,36\n"
"buffer_load_dword v235, v255, s[36:39], s87 offen\n"
"MA v,52,a,220,a,52\n"
"s_cmp_eq_u32 s53, 7\n"
"s_cselect_b32 s88, s69, s55\n"
"MA v,48,a,220,a,48\n"
"v_exp_f32 v5, v5\n"
"MA v,48,a,216,a,32\n"
"s_waitcnt lgkmcnt(2)\n"
"v_exp_f32 v6, v6\n"
"v_cvt_pk_bf16_f32 v164, v4, v5\n"
"MA v,32,a,224,a,64\n"
"buffer_load_dword v235, v232, s[40:43], s88 offen\n"
"MA v,32,a,228,a,80\n"
"s_mov_b32 s86, 30720\n"
"MA v,36,a,228,a,84\n"
"ds_read_b128 a[200:203], v244 offset:26624\n"
"v_exp_f32 v7, v7\n"
"MA v,36,a,224,a,68\n"
"buffer_load_dwordx4 v248, s[24:27], s86 offen offset:3072 lds\n"
"MA v,40,a,224,a,72\n"
"MA v,40,a,228,a,88\n"
"ds_read_b128 a[204:207], v245 offset:26624\n"
"v_cvt_pk_bf16_f32 v165, v6, v7\n"
"MA v,44,a,228,a,92\n"
"v_exp_f32 v8, v8\n"
"MA v,44,a,224,a,76\n"
"v_exp_f32 v9, v9\n"
"MA v,60,a,232,a,76\n"
"v_exp_f32 v10, v10\n"
"v_cvt_pk_bf16_f32 v168, v8, v9\n"
"MA v,60,a,236,a,92\n"
"ds_read_b128 a[208:211], v244 offset:32768\n"
"v_exp_f32 v11, v11\n"
"MA v,56,a,236,a,88\n"
"ds_read_b128 a[212:215], v245 offset:32768\n"
"v_cvt_pk_bf16_f32 v169, v10, v11\n"
"MA v,56,a,232,a,72\n"
"v_exp_f32 v12, v12\n"
"MA v,52,a,232,a,68\n"
"v_exp_f32 v13, v13\n"
"MA v,52,a,236,a,84\n"
"v_exp_f32 v14, v14\n"
"v_cvt_pk_bf16_f32 v172, v12, v13\n"
"MA v,48,a,236,a,80\n"
"v_exp_f32 v15, v15\n"
"MA v,48,a,232,a,64\n"
"s_waitcnt lgkmcnt(2)\n"
"v_exp_f32 v28, v28\n"
"v_cvt_pk_bf16_f32 v173, v14, v15\n"
"v_mfma_f32_16x16x32_bf16 v[0:3], a[192:195], a[0:3], v[208:211]\n"
"ds_read_b128 a[216:219], v244 offset:34816\n"
"v_exp_f32 v29, v29\n"
"MA v,0,a,196,a,16\n"
"s_add_u32 m0, s46, 12288\n"
"v_exp_f32 v30, v30\n"
"v_cvt_pk_bf16_f32 v174, v28, v29\n"
"v_mfma_f32_16x16x32_bf16 v[4:7], a[196:199], a[20:23], v[212:215]\n"
"s_mov_b32 s87, 36864\n"
"ds_read_b128 a[220:223], v245 offset:34816\n"
"v_exp_f32 v31, v31\n"
"MA v,4,a,192,a,4\n"
"v_exp_f32 v24, v24\n"
"v_cvt_pk_bf16_f32 v175, v30, v31\n"
"v_mfma_f32_16x16x32_bf16 v[8:11], a[192:195], a[8:11], v[216:219]\n"
"v_exp_f32 v25, v25\n"
"MA v,8,a,196,a,24\n"
"v_exp_f32 v26, v26\n"
"v_cvt_pk_bf16_f32 v170, v24, v25\n"
"v_mfma_f32_16x16x32_bf16 v[12:15], a[196:199], a[28:31], v[220:223]\n"
"v_exp_f32 v27, v27\n"
"MA v,12,a,192,a,12\n"
"v_exp_f32 v20, v20\n"
"v_cvt_pk_bf16_f32 v171, v26, v27\n"
"v_mfma_f32_16x16x32_bf16 v[28:31], a[200:203], a[12:15], v[220:223]\n"
"v_exp_f32 v21, v21\n"
"v_exp_f32 v22, v22\n"
"MA v,28,a,204,a,28\n"
"v_exp_f32 v23, v23\n"
"v_cvt_pk_bf16_f32 v166, v20, v21\n"
"v_mfma_f32_16x16x32_bf16 v[24:27], a[204:207], a[24:27], v[216:219]\n"
"s_mov_b32 s88, 38912\n"
"v_exp_f32 v16, v16\n"
"v_cvt_pk_bf16_f32 v167, v22, v23\n"
"MA v,24,a,200,a,8\n"
"ds_read_b128 a[224:227], v244 offset:40960\n"
"v_exp_f32 v17, v17\n"
"v_mfma_f32_16x16x32_bf16 v[20:23], a[200:203], a[4:7], v[212:215]\n"
"v_exp_f32 v18, v18\n"
"v_exp_f32 v19, v19\n"
"MA v,20,a,204,a,20\n"
"v_cvt_pk_bf16_f32 v162, v16, v17\n"
"v_cvt_pk_bf16_f32 v163, v18, v19\n"
"v_mfma_f32_16x16x32_bf16 v[16:19], a[204:207], a[16:19], v[208:211]\n"
"buffer_load_dwordx4 v248, s[24:27], s87 offen lds\n"
"MA v,16,a,200,a,0\n"
"s_waitcnt lgkmcnt(1)\n"
"MA v,0,a,208,a,32\n"
"buffer_load_dwordx4 v248, s[24:27], s88 offen offset:1024 lds\n"
"MA v,0,a,212,a,48\n"
"MA v,4,a,212,a,52\n"
"ds_read_b128 a[228:231], v245 offset:40960\n"
"MA v,4,a,208,a,36\n"
"ds_read_b128 a[232:235], v244 offset:43008\n"
"v_exp_f32 v32, v32\n"
"MA v,8,a,208,a,40\n"
"ds_read_b128 a[236:239], v245 offset:43008\n"
"MA v,8,a,212,a,56\n"
"s_mov_b32 s89, 40960\n"
"v_exp_f32 v33, v33\n"
"MA v,12,a,212,a,60\n"
"v_exp_f32 v34, v34\n"
"v_cvt_pk_bf16_f32 v176, v32, v33\n"
"MA v,12,a,208,a,44\n"
"v_exp_f32 v35, v35\n"
"MA v,28,a,216,a,44\n"
"buffer_load_dwordx4 v248, s[24:27], s89 offen offset:2048 lds\n"
"MA v,28,a,220,a,60\n"
"MA v,24,a,220,a,56\n"
"ds_read_b128 a[192:195], v244 offset:28672\n"
"v_cvt_pk_bf16_f32 v177, v34, v35\n"
"MA v,24,a,216,a,40\n"
"ds_read_b128 a[196:199], v245 offset:28672\n"
"MA v,20,a,216,a,36\n"
"v_exp_f32 v36, v36\n"
"MA v,20,a,220,a,52\n"
"v_exp_f32 v37, v37\n"
"MA v,16,a,220,a,48\n"
"v_exp_f32 v38, v38\n"
"v_cvt_pk_bf16_f32 v180, v36, v37\n"
"MA v,16,a,216,a,32\n"
"s_waitcnt lgkmcnt(2)\n"
"v_exp_f32 v39, v39\n"
"MA v,0,a,224,a,64\n"
"s_mov_b32 s90, 43008\n"
"ds_read_b128 a[200:203], v244 offset:30720\n"
"v_cvt_pk_bf16_f32 v181, v38, v39\n"
"MA v,0,a,228,a,80\n"
"v_exp_f32 v40, v40\n"
"MA v,4,a,228,a,84\n"
"v_exp_f32 v41, v41\n"
"MA v,4,a,224,a,68\n"
"buffer_load_dwordx4 v248, s[24:27], s90 offen offset:3072 lds\n"
"MA v,8,a,224,a,72\n"
"MA v,8,a,228,a,88\n"
"v_exp_f32 v42, v42\n"
"MA v,12,a,228,a,92\n"
"ds_read_b128 a[204:207], v245 offset:30720\n"
"v_exp_f32 v43, v43\n"
"MA v,12,a,224,a,76\n"
"v_cvt_pk_bf16_f32 v184, v40, v41\n"
"v_cvt_pk_bf16_f32 v185, v42, v43\n"
"MA v,28,a,232,a,76\n"
"v_exp_f32 v44, v44\n"
"MA v,28,a,236,a,92\n"
"ds_read_b128 a[208:211], v244 offset:36864\n"
"v_exp_f32 v45, v45\n"
"MA v,24,a,236,a,88\n"
"v_exp_f32 v46, v46\n"
"MA v,24,a,232,a,72\n"
"v_exp_f32 v47, v47\n"
"v_cvt_pk_bf16_f32 v188, v44, v45\n"
"MA v,20,a,232,a,68\n"
"ds_read_b128 a[212:215], v245 offset:36864\n"
"v_cvt_pk_bf16_f32 v189, v46, v47\n"
"MA v,20,a,236,a,84\n"
"v_exp_f32 v60, v60\n"
"MA v,16,a,236,a,80\n"
"v_exp_f32 v61, v61\n"
"MA v,16,a,232,a,64\n"
"s_waitcnt lgkmcnt(2)\n"
"v_exp_f32 v62, v62\n"
"v_cvt_pk_bf16_f32 v190, v60, v61\n"
"v_mfma_f32_16x16x32_bf16 v[32:35], a[192:195], a[0:3], v[208:211]\n"
"v_exp_f32 v63, v63\n"
"MA v,32,a,196,a,16\n"
"v_exp_f32 v56, v56\n"
"v_cvt_pk_bf16_f32 v191, v62, v63\n"
"v_mfma_f32_16x16x32_bf16 v[36:39], a[196:199], a[20:23], v[212:215]\n"
"v_exp_f32 v57, v57\n"
"MA v,36,a,192,a,4\n"
"v_exp_f32 v58, v58\n"
"v_mfma_f32_16x16x32_bf16 v[40:43], a[192:195], a[8:11], v[216:219]\n"
"v_exp_f32 v59, v59\n"
"v_cvt_pk_bf16_f32 v186, v56, v57\n"
"MA v,40,a,196,a,24\n"
"ds_read_b128 a[216:219], v244 offset:38912\n"
"v_cvt_pk_bf16_f32 v187, v58, v59\n"
"v_mfma_f32_16x16x32_bf16 v[44:47], a[196:199], a[28:31], v[220:223]\n"
"ds_read_b128 a[220:223], v245 offset:38912\n"
"v_exp_f32 v52, v52\n"
"MA v,44,a,192,a,12\n"
"v_exp_f32 v53, v53\n"
"v_mfma_f32_16x16x32_bf16 v[60:63], a[200:203], a[12:15], v[220:223]\n"
"v_exp_f32 v54, v54\n"
"v_cvt_pk_bf16_f32 v182, v52, v53\n"
"MA v,60,a,204,a,28\n"
"ds_read_b128 a[224:227], v244 offset:45056\n"
"v_exp_f32 v55, v55\n"
"v_mfma_f32_16x16x32_bf16 v[56:59], a[204:207], a[24:27], v[216:219]\n"
"v_exp_f32 v48, v48\n"
"v_cvt_pk_bf16_f32 v183, v54, v55\n"
"MA v,56,a,200,a,8\n"
"ds_read_b128 a[228:231], v245 offset:45056\n"
"v_exp_f32 v49, v49\n"
"v_mfma_f32_16x16x32_bf16 v[52:55], a[200:203], a[4:7], v[212:215]\n"
"v_exp_f32 v50, v50\n"
"v_exp_f32 v51, v51\n"
"MA v,52,a,204,a,20\n"
"v_cvt_pk_bf16_f32 v178, v48, v49\n"
"v_cvt_pk_bf16_f32 v179, v50, v51\n"
"v_mfma_f32_16x16x32_bf16 v[48:51], a[204:207], a[16:19], v[208:211]\n"
"v_exp_f32 v12, v12\n"
"MA v,48,a,200,a,0\n"
"s_waitcnt lgkmcnt(2)\n"
"v_exp_f32 v13, v13\n"
"MA v,32,a,208,a,32\n"
"ds_read_b128 a[232:235], v244 offset:47104\n"
"v_cvt_pk_bf16_f32 v204, v12, v13\n"
"MA v,32,a,212,a,48\n"
"ds_read_b128 a[236:239], v245 offset:47104\n"
"MA v,36,a,212,a,52\n"
"v_exp_f32 v14, v14\n"
"MA v,36,a,208,a,36\n"
"v_exp_f32 v15, v15\n"
"MA v,40,a,208,a,40\n"
"v_exp_f32 v28, v28\n"
"MA v,40,a,212,a,56\n"
"v_exp_f32 v29, v29\n"
"MA v,44,a,212,a,60\n"
"v_exp_f32 v30, v30\n"
"MA v,44,a,208,a,44\n"
"v_exp_f32 v31, v31\n"
"MA v,60,a,216,a,44\n"
"v_cvt_pk_bf16_f32 v205, v14, v15\n"
"v_cvt_pk_bf16_f32 v206, v28, v29\n"
"MA v,60,a,220,a,60\n"
"v_cvt_pk_bf16_f32 v207, v30, v31\n"
"MA v,56,a,220,a,56\n"
"v_exp_f32 v8, v8\n"
"MA v,56,a,216,a,40\n"
"v_exp_f32 v9, v9\n"
"MA v,52,a,216,a,36\n"
"v_exp_f32 v10, v10\n"
"MA v,52,a,220,a,52\n"
"v_exp_f32 v11, v11\n"
"MA v,48,a,220,a,48\n"
"v_exp_f32 v24, v24\n"
"MA v,48,a,216,a,32\n"
"s_waitcnt lgkmcnt(0)\n"
"v_exp_f32 v25, v25\n"
"MA v,32,a,224,a,64\n"
"v_exp_f32 v26, v26\n"
"MA v,32,a,228,a,80\n"
"v_exp_f32 v27, v27\n"
"MA v,36,a,228,a,84\n"
"v_exp_f32 v4, v4\n"
"MA v,36,a,224,a,68\n"
"v_exp_f32 v5, v5\n"
"MA v,40,a,224,a,72\n"
"v_exp_f32 v6, v6\n"
"MA v,40,a,228,a,88\n"
"v_exp_f32 v7, v7\n"
"MA v,44,a,228,a,92\n"
"v_exp_f32 v20, v20\n"
"MA v,44,a,224,a,76\n"
"v_exp_f32 v21, v21\n"
"MA v,60,a,232,a,76\n"
"s_nop 1\n"
"v_exp_f32 v40, v40\n"
"MA v,60,a,236,a,92\n"
"v_exp_f32 v41, v41\n"
"MA v,56,a,236,a,88\n"
"v_exp_f32 v44, v44\n"
"MA v,56,a,232,a,72\n"
"v_exp_f32 v45, v45\n"
"MA v,52,a,232,a,68\n"
"v_exp_f32 v46, v46\n"
"MA v,52,a,236,a,84\n"
"v_exp_f32 v47, v47\n"
"MA v,48,a,236,a,80\n"
"v_exp_f32 v60, v60\n"
"MA v,48,a,232,a,64\n"
"v_exp_f32 v61, v61\n"
"MZ v,208,v,250,v,64\n"
"v_exp_f32 v62, v62\n"
"MA v,208,v,250,v,80\n"
"v_exp_f32 v63, v63\n"
"MZ v,212,v,250,v,84\n"
"v_exp_f32 v42, v42\n"
"MA v,212,v,250,v,68\n"
"v_exp_f32 v43, v43\n"
"MZ v,216,v,250,v,72\n"
"v_exp_f32 v56, v56\n"
"MA v,216,v,250,v,88\n"
"v_exp_f32 v57, v57\n"
"MZ v,220,v,250,v,92\n"
"v_exp_f32 v58, v58\n"
"MA v,220,v,250,v,76\n"
"v_exp_f32 v59, v59\n"
"MA v,220,v,250,a,108\n"
"v_cvt_pk_bf16_f32 v44, v44, v45\n"
"v_cvt_pk_bf16_f32 v45, v46, v47\n"
"MA v,220,v,250,a,124\n"
"v_cvt_pk_bf16_f32 v46, v60, v61\n"
"v_cvt_pk_bf16_f32 v47, v62, v63\n"
"MA v,216,v,250,a,120\n"
"v_cvt_pk_bf16_f32 v40, v40, v41\n"
"v_cvt_pk_bf16_f32 v41, v42, v43\n"
"MA v,216,v,250,a,104\n"
"v_cvt_pk_bf16_f32 v42, v56, v57\n"
"v_cvt_pk_bf16_f32 v43, v58, v59\n"
"MA v,212,v,250,a,100\n"
"v_exp_f32 v22, v22\n"
"MA v,212,v,250,a,116\n"
"v_exp_f32 v23, v23\n"
"MA v,208,v,250,a,112\n"
"v_cvt_pk_bf16_f32 v200, v8, v9\n"
"v_cvt_pk_bf16_f32 v201, v10, v11\n"
"MA v,208,v,250,a,96\n"
"v_cvt_pk_bf16_f32 v202, v24, v25\n"
"v_cvt_pk_bf16_f32 v203, v26, v27\n"
"MA v,208,v,250,a,128\n"
"v_cvt_pk_bf16_f32 v196, v4, v5\n"
"v_cvt_pk_bf16_f32 v197, v6, v7\n"
"MA v,208,v,250,a,144\n"
"v_cvt_pk_bf16_f32 v198, v20, v21\n"
"v_cvt_pk_bf16_f32 v199, v22, v23\n"
"MA v,212,v,250,a,148\n"
"v_exp_f32 v36, v36\n"
"MA v,212,v,250,a,132\n"
"v_exp_f32 v37, v37\n"
"MA v,216,v,250,a,136\n"
"v_exp_f32 v38, v38\n"
"MA v,216,v,250,a,152\n"
"v_exp_f32 v39, v39\n"
"MA v,220,v,250,a,156\n"
"v_exp_f32 v52, v52\n"
"MA v,220,v,250,a,140\n"
"v_exp_f32 v53, v53\n"
"MA v,220,v,250,a,172\n"
"s_waitcnt vmcnt(0)\n"
"s_barrier\n"
"v_exp_f32 v54, v54\n"
"MA v,220,v,250,a,188\n"
"v_exp_f32 v55, v55\n"
"MA v,216,v,250,a,184\n"
"v_exp_f32 v32, v32\n"
"MA v,216,v,250,a,168\n"
"v_exp_f32 v33, v33\n"
"MA v,212,v,250,a,164\n"
"v_exp_f32 v34, v34\n"
"MA v,212,v,250,a,180\n"
"v_exp_f32 v35, v35\n"
"MA v,208,v,250,a,176\n"
"v_exp_f32 v48, v48\n"
"MA v,208,v,250,a,160\n"
"v_exp_f32 v49, v49\n"
"MA v,208,v,250,v,96\n"
"v_exp_f32 v50, v50\n"
"MA v,208,v,250,v,112\n"
"v_exp_f32 v51, v51\n"
"MA v,212,v,250,v,116\n"
"v_cvt_pk_bf16_f32 v36, v36, v37\n"
"v_cvt_pk_bf16_f32 v37, v38, v39\n"
"MA v,212,v,250,v,100\n"
"v_cvt_pk_bf16_f32 v38, v52, v53\n"
"v_cvt_pk_bf16_f32 v39, v54, v55\n"
"MA v,216,v,250,v,104\n"
"v_exp_f32 v0, v0\n"
"MA v,216,v,250,v,120\n"
"v_exp_f32 v1, v1\n"
"MA v,220,v,250,v,124\n"
"v_exp_f32 v2, v2\n"
"MA v,220,v,250,v,108\n"
"v_exp_f32 v3, v3\n"
"MA v,220,v,250,v,140\n"
"TR 192,240,0\n"
"TR 194,240,2048\n"
"MA v,220,v,250,v,156\n"
"TR 196,240,4096\n"
"TR 198,240,6144\n"
"MA v,216,v,250,v,152\n"
"v_exp_f32 v16, v16\n"
"MA v,216,v,250,v,136\n"
"v_exp_f32 v17, v17\n"
"MA v,212,v,250,v,132\n"
"v_exp_f32 v18, v18\n"
"MA v,212,v,250,v,148\n"
"v_exp_f32 v19, v19\n"
"MA v,208,v,250,v,144\n"
"v_cvt_pk_bf16_f32 v32, v32, v33\n"
"v_cvt_pk_bf16_f32 v33, v34, v35\n"
"MA v,208,v,250,v,128\n"
"s_add_u32 m0, s46, 65536\n"
"s_mov_b32 s91, 128\n"
"v_cvt_pk_bf16_f32 v34, v48, v49\n"
"MA v,208,v,250,v,160\n"
"v_cvt_pk_bf16_f32 v192, v0, v1\n"
"v_cvt_pk_bf16_f32 v35, v50, v51\n"
"MA v,208,v,250,v,176\n"
"v_cvt_pk_bf16_f32 v193, v2, v3\n"
"v_cvt_pk_bf16_f32 v194, v16, v17\n"
"MA v,212,v,250,v,180\n"
"buffer_load_dwordx4 v248, s[24:27], s91 offen lds\n"
"MA v,212,v,250,v,164\n"
"v_cvt_pk_bf16_f32 v195, v18, v19\n"
"MA v,216,v,250,v,168\n"
"TR 200,241,0\n"
"TR 202,241,2048\n"
"MA v,216,v,250,v,184\n"
"TR 204,241,4096\n"
"TR 206,241,6144\n"
"MA v,220,v,250,v,188\n"
"s_mov_b32 s86, 2176\n"
"MA v,220,v,250,v,172\n"
"MA v,220,v,250,v,204\n"
"TR 208,240,8192\n"
"TR 210,240,10240\n"
"MA v,220,v,250,v,44\n"
"buffer_load_dwordx4 v248, s[24:27], s86 offen offset:1024 lds\n"
"MA v,216,v,250,v,40\n"
"TR 212,240,12288\n"
"MA v,216,v,250,v,200\n"
"TR 214,240,14336\n"
"MA v,212,v,250,v,196\n"
"s_nop 1\n"
"v_rcp_f32 v239, v220\n"
"MA v,212,v,250,v,36\n"
"s_mov_b32 s87, 4224\n"
"MA v,208,v,250,v,32\n"
"v_rcp_f32 v238, v216\n"
"MA v,208,v,250,v,192\n"
"s_waitcnt lgkmcnt(4)\n"
"MZ v,0,a,192,v,64\n"
"buffer_load_dwordx4 v248, s[24:27], s87 offen offset:2048 lds\n"
"MA v,0,a,196,v,80\n"
"TR 216,241,8192\n"
"MZ v,4,a,196,v,84\n"
"TR 218,241,10240\n"
"TR 220,241,12288\n"
"MA v,4,a,192,v,68\n"
"TR 222,241,14336\n"
"v_cmp_ngt_f32 vcc, 0x4f800000, v208\n"
"MZ v,8,a,192,v,72\n"
"s_mov_b32 s88, 6272\n"
"v_cndmask_b32 v224, v224, v225, vcc\n"
"v_cmp_ngt_f32 vcc, 0x4f800000, v212\n"
"MA v,8,a,196,v,88\n"
"s_nop 0\n"
"v_cndmask_b32 v224, v224, v225, vcc\n"
"v_cmp_ngt_f32 vcc, 0x4f800000, v216\n"
"MZ v,12,a,196,v,92\n"
"s_nop 0\n"
"v_cndmask_b32 v224, v224, v225, vcc\n"
"v_cmp_ngt_f32 vcc, 0x4f800000, v220\n"
"MA v,12,a,192,v,76\n"
"buffer_load_dwordx4 v248, s[24:27], s88 offen offset:3072 lds\n"
"MZ v,28,a,200,v,76\n"
"TR 224,240,16384\n"
"MA v,28,a,204,v,92\n"
"TR 226,240,18432\n"
"TR 228,240,20480\n"
"MZ v,24,a,204,v,88\n"
"TR 230,240,22528\n"
"v_cndmask_b32 v224, v224, v225, vcc\n"
"MA v,24,a,200,v,72\n"
"s_add_u32 m0, s46, 69632\n"
"s_mov_b32 s89, 12416\n"
"v_lshrrev_b32 v225, 2, v249\n"
"MZ v,20,a,200,v,68\n"
"v_add_u32 v225, s50, v225\n"
"MA v,20,a,204,v,84\n"
"ds_write_b32 v225, v224 offset:0\n"
"MZ v,16,a,204,v,80\n"
"buffer_load_dwordx4 v248, s[24:27], s89 offen lds\n"
"MA v,16,a,200,v,64\n"
"s_waitcnt lgkmcnt(5)\n"
"MA v,0,a,208,a,96\n"
"TR 232,241,16384\n"
"TR 234,241,18432\n"
"MA v,0,a,212,a,112\n"
"TR 236,241,20480\n"
"TR 238,241,22528\n"
"MA v,4,a,212,a,116\n"
"s_mov_b32 s90, 14464\n"
"v_rcp_f32 v237, v212\n"
"MA v,4,a,208,a,100\n"
"v_rcp_f32 v236, v208\n"
"MA v,8,a,208,a,104\n"
"s_waitcnt lgkmcnt(4)\n"
"s_barrier\n"
"ds_read_b128 v[228:231], v226 offset:0\n"
"MA v,8,a,212,a,120\n"
"buffer_load_dwordx4 v248, s[24:27], s90 offen offset:1024 lds\n"
"MA v,12,a,212,a,124\n"
"MA v,12,a,208,a,108\n"
"MA v,28,a,216,a,108\n"
"TR 192,240,24576\n"
"TR 194,240,26624\n"
"MA v,28,a,220,a,124\n"
"s_mov_b32 s91, 16512\n"
"TR 196,240,28672\n"
"TR 198,240,30720\n"
"MA v,24,a,220,a,120\n"
"s_waitcnt lgkmcnt(4)\n"
"v_or3_b32 v228, v228, v229, v230\n"
"MA v,24,a,216,a,104\n"
"v_or_b32 v228, v228, v231\n"
"MA v,20,a,216,a,100\n"
"buffer_load_dwordx4 v248, s[24:27], s91 offen offset:2048 lds\n"
"MA v,20,a,220,a,116\n"
"v_cmp_ne_u32 vcc, 0, v228\n"
"MA v,16,a,220,a,112\n"
"s_nop 2\n"
"s_cbranch_vccnz L_FALLBACK%=\n"
"v_bfe_u32 v246, v249, 4, 2\n"
"MA v,16,a,216,a,96\n"
"MA v,0,a,224,a,128\n"
"s_mov_b32 s86, 18560\n"
"TR 200,241,24576\n"
"MA v,0,a,228,a,144\n"
"TR 202,241,26624\n"
"TR 204,241,28672\n"
"MA v,4,a,228,a,148\n"
"TR 206,241,30720\n"
"MA v,4,a,224,a,132\n"
"buffer_load_dwordx4 v248, s[24:27], s86 offen offset:3072 lds\n"
"MA v,8,a,224,a,136\n"
"MA v,8,a,228,a,152\n"
"MA v,12,a,228,a,156\n"
"MA v,12,a,224,a,140\n"
"s_add_u32 m0, s46, 73728\n"
"MA v,28,a,232,a,140\n"
"s_mov_b32 s87, 24704\n"
"TR 208,240,32768\n"
"MA v,28,a,236,a,156\n"
"TR 210,240,34816\n"
"TR 212,240,36864\n"
"MA v,24,a,236,a,152\n"
"buffer_load_dwordx4 v248, s[24:27], s87 offen lds\n"
"MA v,24,a,232,a,136\n"
"TR 214,240,38912\n"
"MA v,20,a,232,a,132\n"
"MA v,20,a,236,a,148\n"
"MA v,16,a,236,a,144\n"
"s_mov_b32 s88, 26752\n"
"MA v,16,a,232,a,128\n"
"s_waitcnt lgkmcnt(4)\n"
"MA v,0,a,192,a,160\n"
"TR 216,241,32768\n"
"TR 218,241,34816\n"
"MA v,0,a,196,a,176\n"
"buffer_load_dwordx4 v248, s[24:27], s88 offen offset:1024 lds\n"
"MA v,4,a,196,a,180\n"
"TR 220,241,36864\n"
"MA v,4,a,192,a,164\n"
"TR 222,241,38912\n"
"MA v,8,a,192,a,168\n"
"MA v,8,a,196,a,184\n"
"s_mov_b32 s89, 28800\n"
"MA v,12,a,196,a,188\n"
"MA v,12,a,192,a,172\n"
"MA v,28,a,200,a,172\n"
"buffer_load_dwordx4 v248, s[24:27], s89 offen offset:2048 lds\n"
"MA v,28,a,204,a,188\n"
"TR 224,240,40960\n"
"MA v,24,a,204,a,184\n"
"TR 226,240,43008\n"
"TR 228,240,45056\n"
"MA v,24,a,200,a,168\n"
"TR 230,240,47104\n"
"MA v,20,a,200,a,164\n"
"s_mov_b32 s90, 30848\n"
"MA v,20,a,204,a,180\n"
"MA v,16,a,204,a,176\n"
"MA v,16,a,200,a,160\n"
"s_waitcnt lgkmcnt(4)\n"
"buffer_load_dwordx4 v248, s[24:27], s90 offen offset:3072 lds\n"
"MA v,0,a,208,v,96\n"
"TR 232,241,40960\n"
"MA v,0,a,212,v,112\n"
"TR 234,241,43008\n"
"TR 236,241,45056\n"
"MA v,4,a,212,v,116\n"
"TR 238,241,47104\n"
"MA v,4,a,208,v,100\n"
"s_add_u32 m0, s46, 77824\n"
"s_mov_b32 s91, 36992\n"
"MA v,8,a,208,v,104\n"
"MA v,8,a,212,v,120\n"
"MA v,12,a,212,v,124\n"
"buffer_load_dwordx4 v248, s[24:27], s91 offen lds\n"
"MA v,12,a,208,v,108\n"
"MA v,28,a,216,v,108\n"
"TR 192,240,49152\n"
"TR 194,240,51200\n"
"MA v,28,a,220,v,124\n"
"TR 196,240,53248\n"
"TR 198,240,55296\n"
"MA v,24,a,220,v,120\n"
"s_mov_b32 s86, 39040\n"
"MA v,24,a,216,v,104\n"
"MA v,20,a,216,v,100\n"
"MA v,20,a,220,v,116\n"
"buffer_load_dwordx4 v248, s[24:27], s86 offen offset:1024 lds\n"
"MA v,16,a,220,v,112\n"
"MA v,16,a,216,v,96\n"
"s_waitcnt lgkmcnt(4)\n"
"MA v,0,a,224,v,128\n"
"TR 200,241,49152\n"
"TR 202,241,51200\n"
"MA v,0,a,228,v,144\n"
"s_mov_b32 s87, 41088\n"
"TR 204,241,53248\n"
"TR 206,241,55296\n"
"MA v,4,a,228,v,148\n"
"MA v,4,a,224,v,132\n"
"MA v,8,a,224,v,136\n"
"buffer_load_dwordx4 v248, s[24:27], s87 offen offset:2048 lds\n"
"MA v,8,a,228,v,152\n"
"MA v,12,a,228,v,156\n"
"MA v,12,a,224,v,140\n"
"MA v,28,a,232,v,140\n"
"s_mov_b32 s88, 43136\n"
"TR 208,240,57344\n"
"MA v,28,a,236,v,156\n"
"TR 210,240,59392\n"
"TR 212,240,61440\n"
"MA v,24,a,236,v,152\n"
"TR 214,240,63488\n"
"v_xor_b32 v240, 0x10000, v240\n"
"MA v,24,a,232,v,136\n"
"buffer_load_dwordx4 v248, s[24:27], s88 offen offset:3072 lds\n"
"MA v,20,a,232,v,132\n"
"MA v,20,a,236,v,148\n"
"MA v,16,a,236,v,144\n"
"MA v,16,a,232,v,128\n"
"s_waitcnt lgkmcnt(4)\n"
"MA v,0,a,192,v,160\n"
"TR 216,241,57344\n"
"TR 218,241,59392\n"
"MA v,0,a,196,v,176\n"
"TR 220,241,61440\n"
"TR 222,241,63488\n"
"MA v,4,a,196,v,180\n"
"v_xor_b32 v241, 0x10000, v241\n"
"MA v,4,a,192,v,164\n"
"MA v,8,a,192,v,168\n"
"MA v,8,a,196,v,184\n"
"MA v,12,a,196,v,188\n"
"MA v,12,a,192,v,172\n"
"MA v,28,a,200,v,172\n"
"TR 224,242,0\n"
"TR 226,242,2048\n"
"MA v,28,a,204,v,188\n"
"TR 228,242,4096\n"
"TR 230,242,6144\n"
"MA v,24,a,204,v,184\n"
"MA v,24,a,200,v,168\n"
"MA v,20,a,200,v,164\n"
"MA v,20,a,204,v,180\n"
"MA v,16,a,204,v,176\n"
"MA v,16,a,200,v,160\n"
"s_waitcnt lgkmcnt(4)\n"
"MA v,0,a,208,v,192\n"
"TR 232,243,0\n"
"TR 234,243,2048\n"
"MA v,0,a,212,v,32\n"
"TR 236,243,4096\n"
"TR 238,243,6144\n"
"MA v,4,a,212,v,36\n"
"MA v,4,a,208,v,196\n"
"MA v,8,a,208,v,200\n"
"s_nop 2\n"
"v_mul_f32 v0, v0, v236\n"
"v_mul_f32 v1, v1, v236\n"
"MA v,8,a,212,v,40\n"
"v_mul_f32 v2, v2, v236\n"
"v_mul_f32 v3, v3, v236\n"
"MA v,12,a,212,v,44\n"
"v_cvt_pk_bf16_f32 v224, v0, v1\n"
"v_cvt_pk_bf16_f32 v225, v2, v3\n"
"MA v,12,a,208,v,204\n"
"v_mul_f32 v4, v4, v237\n"
"v_mul_f32 v5, v5, v237\n"
"MA v,28,a,216,v,204\n"
"TR 192,242,8192\n"
"TR 194,242,10240\n"
"MA v,28,a,220,v,44\n"
"TR 196,242,12288\n"
"TR 198,242,14336\n"
"MA v,24,a,220,v,40\n"
"v_mul_f32 v6, v6, v237\n"
"v_mul_f32 v7, v7, v237\n"
"MA v,24,a,216,v,200\n"
"v_cvt_pk_bf16_f32 v228, v4, v5\n"
"v_cvt_pk_bf16_f32 v229, v6, v7\n"
"MA v,20,a,216,v,196\n"
"v_mul_f32 v8, v8, v238\n"
"v_mul_f32 v9, v9, v238\n"
"MA v,20,a,220,v,36\n"
"v_mul_f32 v10, v10, v238\n"
"v_mul_f32 v11, v11, v238\n"
"MA v,16,a,220,v,32\n"
"v_mul_f32 v24, v24, v238\n"
"v_mul_f32 v25, v25, v238\n"
"MA v,16,a,216,v,192\n"
"s_waitcnt lgkmcnt(4)\n"
"v_mul_f32 v26, v26, v238\n"
"v_mul_f32 v27, v27, v238\n"
"MZ v,48,a,224,v,64\n"
"TR 200,243,8192\n"
"TR 202,243,10240\n"
"MA v,48,a,228,v,80\n"
"s_add_u32 s9, s47, 0\n"
"TR 204,243,12288\n"
"TR 206,243,14336\n"
"MZ v,52,a,228,v,84\n"
"v_mul_f32 v16, v16, v236\n"
"v_mul_f32 v17, v17, v236\n"
"MA v,52,a,224,v,68\n"
"v_mul_f32 v18, v18, v236\n"
"v_mul_f32 v19, v19, v236\n"
"MZ v,56,a,224,v,72\n"
"v_cvt_pk_bf16_f32 v226, v16, v17\n"
"v_cvt_pk_bf16_f32 v227, v18, v19\n"
"MA v,56,a,228,v,88\n"
"buffer_store_dwordx4 v[224:227], v249, s[16:19], s9 offen sc1\n"
"MZ v,60,a,228,v,92\n"
"v_mul_f32 v20, v20, v237\n"
"MA v,60,a,224,v,76\n"
"v_mul_f32 v21, v21, v237\n"
"v_mul_f32 v22, v22, v237\n"
"MZ v,220,a,232,v,76\n"
"TR 208,242,16384\n"
"TR 210,242,18432\n"
"MA v,220,a,236,v,92\n"
"TR 212,242,20480\n"
"TR 214,242,22528\n"
"MZ v,216,a,236,v,88\n"
"v_mul_f32 v23, v23, v237\n"
"v_cvt_pk_bf16_f32 v230, v20, v21\n"
"MA v,216,a,232,v,72\n"
"v_cvt_pk_bf16_f32 v231, v22, v23\n"
"v_cvt_pk_bf16_f32 v224, v8, v9\n"
"MZ v,212,a,232,v,68\n"
"buffer_store_dwordx4 v[228:231], v249, s[16:19], s9 offen offset:1024 sc1\n"
"MA v,212,a,236,v,84\n"
"v_cvt_pk_bf16_f32 v225, v10, v11\n"
"MZ v,208,a,236,v,80\n"
"v_cvt_pk_bf16_f32 v226, v24, v25\n"
"v_cvt_pk_bf16_f32 v227, v26, v27\n"
"MA v,208,a,232,v,64\n"
"s_waitcnt lgkmcnt(4)\n"
"buffer_store_dwordx4 v[224:227], v249, s[16:19], s9 offen offset:2048 sc1\n"
"MA v,48,a,192,a,96\n"
"TR 216,243,16384\n"
"MA v,48,a,196,a,112\n"
"TR 218,243,18432\n"
"TR 220,243,20480\n"
"MA v,52,a,196,a,116\n"
"TR 222,243,22528\n"
"v_mul_f32 v12, v12, v239\n"
"MA v,52,a,192,a,100\n"
"v_mul_f32 v13, v13, v239\n"
"v_mul_f32 v14, v14, v239\n"
"MA v,56,a,192,a,104\n"
"v_mul_f32 v15, v15, v239\n"
"v_mul_f32 v28, v28, v239\n"
"MA v,56,a,196,a,120\n"
"v_mul_f32 v29, v29, v239\n"
"v_mul_f32 v30, v30, v239\n"
"MA v,60,a,196,a,124\n"
"v_cvt_pk_bf16_f32 v228, v12, v13\n"
"v_mul_f32 v31, v31, v239\n"
"MA v,60,a,192,a,108\n"
"v_cvt_pk_bf16_f32 v229, v14, v15\n"
"v_cvt_pk_bf16_f32 v230, v28, v29\n"
"MA v,220,a,200,a,108\n"
"TR 224,242,24576\n"
"TR 226,242,26624\n"
"MA v,220,a,204,a,124\n"
"TR 228,242,28672\n"
"TR 230,242,30720\n"
"MA v,216,a,204,a,120\n"
"v_cvt_pk_bf16_f32 v231, v30, v31\n"
"MA v,216,a,200,a,104\n"
"buffer_store_dwordx4 v[228:231], v249, s[16:19], s9 offen offset:3072 sc1\n"
"MA v,212,a,200,a,100\n"
"s_cmp_lg_u32 s66, 0\n"
"s_cselect_b32 s11, 16384, s55\n"
"v_lshlrev_b32 v253, 4, v249\n"
"MA v,212,a,204,a,116\n"
"MA v,208,a,204,a,112\n"
"MA v,208,a,200,a,96\n"
"s_waitcnt lgkmcnt(4)\n"
"MA v,48,a,208,a,128\n"
"TR 232,243,24576\n"
"TR 234,243,26624\n"
"MA v,48,a,212,a,144\n"
"TR 236,243,28672\n"
"TR 238,243,30720\n"
"MA v,52,a,212,a,148\n"
"MA v,52,a,208,a,132\n"
"MA v,56,a,208,a,136\n"
"MA v,56,a,212,a,152\n"
"MA v,60,a,212,a,156\n"
"buffer_load_dwordx2 v[254:255], v253, s[16:19], s11 offen sc1\n"
"MA v,60,a,208,a,140\n"
"MA v,220,a,216,a,140\n"
"TR 192,242,32768\n"
"TR 194,242,34816\n"
"MA v,220,a,220,a,156\n"
"TR 196,242,36864\n"
"TR 198,242,38912\n"
"MA v,216,a,220,a,152\n"
"MA v,216,a,216,a,136\n"
"MA v,212,a,216,a,132\n"
"MA v,212,a,220,a,148\n"
"MA v,208,a,220,a,144\n"
"MA v,208,a,216,a,128\n"
"s_waitcnt lgkmcnt(4)\n"
"MA v,48,a,224,a,160\n"
"TR 200,243,32768\n"
"TR 202,243,34816\n"
"MA v,48,a,228,a,176\n"
"TR 204,243,36864\n"
"TR 206,243,38912\n"
"MA v,52,a,228,a,180\n"
"MA v,52,a,224,a,164\n"
"MA v,56,a,224,a,168\n"
"MA v,56,a,228,a,184\n"
"MA v,60,a,228,a,188\n"
"MA v,60,a,224,a,172\n"
"MA v,220,a,232,a,172\n"
"TR 208,242,40960\n"
"TR 210,242,43008\n"
"MA v,220,a,236,a,188\n"
"TR 212,242,45056\n"
"TR 214,242,47104\n"
"MA v,216,a,236,a,184\n"
"MA v,216,a,232,a,168\n"
"MA v,212,a,232,a,164\n"
"MA v,212,a,236,a,180\n"
"MA v,208,a,236,a,176\n"
"MA v,208,a,232,a,160\n"
"s_waitcnt lgkmcnt(4)\n"
"MA v,48,a,192,v,96\n"
"TR 216,243,40960\n"
"TR 218,243,43008\n"
"MA v,48,a,196,v,112\n"
"TR 220,243,45056\n"
"TR 222,243,47104\n"
"MA v,52,a,196,v,116\n"
"MA v,52,a,192,v,100\n"
"MA v,56,a,192,v,104\n"
"MA v,56,a,196,v,120\n"
"MA v,60,a,196,v,124\n"
"MA v,60,a,192,v,108\n"
"MA v,220,a,200,v,108\n"
"TR 224,242,49152\n"
"TR 226,242,51200\n"
"MA v,220,a,204,v,124\n"
"TR 228,242,53248\n"
"TR 230,242,55296\n"
"MA v,216,a,204,v,120\n"
"MA v,216,a,200,v,104\n"
"MA v,212,a,200,v,100\n"
"MA v,212,a,204,v,116\n"
"MA v,208,a,204,v,112\n"
"MA v,208,a,200,v,96\n"
"s_waitcnt lgkmcnt(4)\n"
"MA v,48,a,208,v,128\n"
"TR 232,243,49152\n"
"TR 234,243,51200\n"
"MA v,48,a,212,v,144\n"
"TR 236,243,53248\n"
"TR 238,243,55296\n"
"MA v,52,a,212,v,148\n"
"MA v,52,a,208,v,132\n"
"MA v,56,a,208,v,136\n"
"MA v,56,a,212,v,152\n"
"MA v,60,a,212,v,156\n"
"MA v,60,a,208,v,140\n"
"MA v,220,a,216,v,140\n"
"TR 192,242,57344\n"
"TR 194,242,59392\n"
"MA v,220,a,220,v,156\n"
"TR 196,242,61440\n"
"TR 198,242,63488\n"
"MA v,216,a,220,v,152\n"
"v_xor_b32 v242, 0x10000, v242\n"
"MA v,216,a,216,v,136\n"
"MA v,212,a,216,v,132\n"
"MA v,212,a,220,v,148\n"
"MA v,208,a,220,v,144\n"
"MA v,208,a,216,v,128\n"
"s_waitcnt lgkmcnt(4)\n"
"MA v,48,a,224,v,160\n"
"TR 200,243,57344\n"
"TR 202,243,59392\n"
"MA v,48,a,228,v,176\n"
"TR 204,243,61440\n"
"TR 206,243,63488\n"
"MA v,52,a,228,v,180\n"
"v_xor_b32 v243, 0x10000, v243\n"
"MA v,52,a,224,v,164\n"
"MA v,56,a,224,v,168\n"
"MA v,56,a,228,v,184\n"
"MA v,60,a,228,v,188\n"
"MA v,60,a,224,v,172\n"
"MA v,220,a,232,v,172\n"
"s_waitcnt vmcnt(5) lgkmcnt(0)\n"
"s_barrier\n"
"TR 208,240,0\n"
"MA v,220,a,236,v,188\n"
"TR 210,240,2048\n"
"TR 212,240,4096\n"
"MA v,216,a,236,v,184\n"
"TR 214,240,6144\n"
"MA v,216,a,232,v,168\n"
"MA v,212,a,232,v,164\n"
"MA v,212,a,236,v,180\n"
"MA v,208,a,236,v,176\n"
"MA v,208,a,232,v,160\n"
"MA v,48,a,192,v,192\n"
"TR 216,241,0\n"
"TR 218,241,2048\n"
"MA v,48,a,196,v,32\n"
"TR 220,241,4096\n"
"TR 222,241,6144\n"
"MA v,52,a,196,v,36\n"
"MA v,52,a,192,v,196\n"
"MA v,56,a,192,v,200\n"
"s_nop 2\n"
"v_mul_f32 v48, v48, v236\n"
"v_mul_f32 v49, v49, v236\n"
"MA v,56,a,196,v,40\n"
"v_mul_f32 v50, v50, v236\n"
"v_mul_f32 v51, v51, v236\n"
"MA v,60,a,196,v,44\n"
"v_cvt_pk_bf16_f32 v224, v48, v49\n"
"v_cvt_pk_bf16_f32 v225, v50, v51\n"
"MA v,60,a,192,v,204\n"
"v_mul_f32 v52, v52, v237\n"
"v_mul_f32 v53, v53, v237\n"
"MA v,220,a,200,v,204\n"
"TR 224,240,8192\n"
"TR 226,240,10240\n"
"MA v,220,a,204,v,44\n"
"TR 228,240,12288\n"
"TR 230,240,14336\n"
"MA v,216,a,204,v,40\n"
"v_mul_f32 v54, v54, v237\n"
"v_mul_f32 v55, v55, v237\n"
"MA v,216,a,200,v,200\n"
"v_cvt_pk_bf16_f32 v228, v52, v53\n"
"v_cvt_pk_bf16_f32 v229, v54, v55\n"
"MA v,212,a,200,v,196\n"
"v_mul_f32 v56, v56, v238\n"
"v_mul_f32 v57, v57, v238\n"
"MA v,212,a,204,v,36\n"
"v_mul_f32 v58, v58, v238\n"
"v_mul_f32 v59, v59, v238\n"
"MA v,208,a,204,v,32\n"
"v_mul_f32 v216, v216, v238\n"
"v_mul_f32 v217, v217, v238\n"
"MA v,208,a,200,v,192\n"
"s_waitcnt lgkmcnt(4)\n"
"v_mul_f32 v218, v218, v238\n"
"v_mul_f32 v219, v219, v238\n"
"MZ v,0,a,208,v,64\n"
"TR 232,241,8192\n"
"TR 234,241,10240\n"
"MA v,0,a,212,v,80\n"
"s_add_u32 m0, s46, 0\n"
"s_mov_b32 s89, 256\n"
"TR 236,241,12288\n"
"MZ v,4,a,212,v,84\n"
"s_add_u32 s10, s47, 4096\n"
"TR 238,241,14336\n"
"v_mul_f32 v208, v208, v236\n"
"MA v,4,a,208,v,68\n"
"v_mul_f32 v209, v209, v236\n"
"v_mul_f32 v210, v210, v236\n"
"MZ v,8,a,208,v,72\n"
"buffer_load_dwordx4 v248, s[24:27], s89 offen lds\n"
"MA v,8,a,212,v,88\n"
"v_mul_f32 v211, v211, v236\n"
"MZ v,12,a,212,v,92\n"
"v_cvt_pk_bf16_f32 v226, v208, v209\n"
"v_cvt_pk_bf16_f32 v227, v210, v211\n"
"MA v,12,a,208,v,76\n"
"buffer_store_dwordx4 v[224:227], v249, s[16:19], s10 offen sc1\n"
"MZ v,28,a,216,v,76\n"
"s_mov_b32 s90, 2304\n"
"MA v,28,a,220,v,92\n"
"TR 192,240,16384\n"
"TR 194,240,18432\n"
"MZ v,24,a,220,v,88\n"
"TR 196,240,20480\n"
"TR 198,240,22528\n"
"MA v,24,a,216,v,72\n"
"buffer_load_dwordx4 v248, s[24:27], s90 offen offset:1024 lds\n"
"MZ v,20,a,216,v,68\n"
"v_mul_f32 v212, v212, v237\n"
"MA v,20,a,220,v,84\n"
"v_mul_f32 v213, v213, v237\n"
"v_mul_f32 v214, v214, v237\n"
"MZ v,16,a,220,v,80\n"
"v_mul_f32 v215, v215, v237\n"
"v_cvt_pk_bf16_f32 v230, v212, v213\n"
"MA v,16,a,216,v,64\n"
"s_waitcnt lgkmcnt(4)\n"
"s_mov_b32 s91, 4352\n"
"v_cvt_pk_bf16_f32 v231, v214, v215\n"
"MA v,0,a,224,a,96\n"
"TR 200,241,16384\n"
"TR 202,241,18432\n"
"MA v,0,a,228,a,112\n"
"TR 204,241,20480\n"
"TR 206,241,22528\n"
"MA v,4,a,228,a,116\n"
"buffer_load_dwordx4 v248, s[24:27], s91 offen offset:2048 lds\n"
"MA v,4,a,224,a,100\n"
"v_cvt_pk_bf16_f32 v224, v56, v57\n"
"MA v,8,a,224,a,104\n"
"buffer_store_dwordx4 v[228:231], v249, s[16:19], s10 offen offset:1024 sc1\n"
"MA v,8,a,228,a,120\n"
"v_cvt_pk_bf16_f32 v225, v58, v59\n"
"MA v,12,a,228,a,124\n"
"s_mov_b32 s86, 6400\n"
"v_cvt_pk_bf16_f32 v226, v216, v217\n"
"v_cvt_pk_bf16_f32 v227, v218, v219\n"
"MA v,12,a,224,a,108\n"
"buffer_store_dwordx4 v[224:227], v249, s[16:19], s10 offen offset:2048 sc1\n"
"MA v,28,a,232,a,108\n"
"TR 208,240,24576\n"
"MA v,28,a,236,a,124\n"
"buffer_load_dwordx4 v248, s[24:27], s86 offen offset:3072 lds\n"
"MA v,24,a,236,a,120\n"
"TR 210,240,26624\n"
"MA v,24,a,232,a,104\n"
"TR 212,240,28672\n"
"TR 214,240,30720\n"
"MA v,20,a,232,a,100\n"
"v_mul_f32 v60, v60, v239\n"
"v_mul_f32 v61, v61, v239\n"
"MA v,20,a,236,a,116\n"
"s_add_u32 m0, s46, 4096\n"
"s_mov_b32 s87, 12544\n"
"v_mul_f32 v62, v62, v239\n"
"MA v,16,a,236,a,112\n"
"v_mul_f32 v63, v63, v239\n"
"v_mul_f32 v220, v220, v239\n"
"MA v,16,a,232,a,96\n"
"s_waitcnt lgkmcnt(4)\n"
"v_mul_f32 v221, v221, v239\n"
"v_mul_f32 v222, v222, v239\n"
"MA v,0,a,192,a,128\n"
"s_add_u32 s12, s56, 1\n"
"TR 216,241,24576\n"
"MA v,0,a,196,a,144\n"
"buffer_load_dwordx4 v248, s[24:27], s87 offen lds\n"
"MA v,4,a,196,a,148\n"
"TR 218,241,26624\n"
"MA v,4,a,192,a,132\n"
"TR 220,241,28672\n"
"TR 222,241,30720\n"
"MA v,8,a,192,a,136\n"
"s_mov_b32 s88, 14592\n"
"s_waitcnt vmcnt(8)\n"
"v_subrev_u32 v254, s12, v254\n"
"v_cmp_eq_u32 s[94:95], s57, v255\n"
"MA v,8,a,196,a,152\n"
"v_mul_f32 v223, v223, v239\n"
"v_cmp_gt_u32 vcc, 8, v254\n"
"MA v,12,a,196,a,156\n"
"v_cvt_pk_bf16_f32 v228, v60, v61\n"
"v_cvt_pk_bf16_f32 v229, v62, v63\n"
"MA v,12,a,192,a,140\n"
"buffer_load_dwordx4 v248, s[24:27], s88 offen offset:1024 lds\n"
"MA v,28,a,200,a,140\n"
"s_and_b32 vcc_lo, vcc_lo, s94\n"
"s_andn2_b32 vcc_lo, s66, vcc_lo\n"
"s_cbranch_scc1 L_SLOW1_%=\n"
"L_BACK1_%=:\n"
"MA v,28,a,204,a,156\n"
"s_cmp_lg_u32 s66, 0\n"
"s_cselect_b32 s13, 0, s55\n"
"TR 224,240,32768\n"
"TR 226,240,34816\n"
"MA v,24,a,204,a,152\n"
"TR 228,240,36864\n"
"TR 230,240,38912\n"
"MA v,24,a,200,a,136\n"
"s_mov_b32 s89, 16640\n"
"v_cvt_pk_bf16_f32 v230, v220, v221\n"
"v_cvt_pk_bf16_f32 v231, v222, v223\n"
"MA v,20,a,200,a,132\n"
"buffer_store_dwordx4 v[228:231], v249, s[16:19], s10 offen offset:3072 sc1\n"
"MA v,20,a,204,a,148\n"
"MA v,16,a,204,a,144\n"
"buffer_load_dwordx4 v248, s[24:27], s89 offen offset:2048 lds\n"
"MA v,16,a,200,a,128\n"
"s_waitcnt lgkmcnt(4)\n"
"MA v,0,a,208,a,160\n"
"TR 232,241,32768\n"
"TR 234,241,34816\n"
"MA v,0,a,212,a,176\n"
"TR 236,241,36864\n"
"TR 238,241,38912\n"
"MA v,4,a,212,a,180\n"
"s_mov_b32 s90, 18688\n"
"MA v,4,a,208,a,164\n"
"MA v,8,a,208,a,168\n"
"s_add_u32 s14, s56, 16\n"
"v_mov_b32 v255, s57\n"
"MA v,8,a,212,a,184\n"
"buffer_load_dwordx4 v248, s[24:27], s90 offen offset:3072 lds\n"
"MA v,12,a,212,a,188\n"
"v_mov_b32 v254, s14\n"
"MA v,12,a,208,a,172\n"
"MA v,28,a,216,a,172\n"
"TR 192,240,40960\n"
"TR 194,240,43008\n"
"MA v,28,a,220,a,188\n"
"s_add_u32 m0, s46, 8192\n"
"s_mov_b32 s91, 24832\n"
"TR 196,240,45056\n"
"MA v,24,a,220,a,184\n"
"TR 198,240,47104\n"
"MA v,24,a,216,a,168\n"
"MA v,20,a,216,a,164\n"
"buffer_store_dwordx2 v[254:255], v234, s[16:19], s13 offen sc1\n"
"MA v,20,a,220,a,180\n"
"MA v,16,a,220,a,176\n"
"buffer_load_dwordx4 v248, s[24:27], s91 offen lds\n"
"MA v,16,a,216,a,160\n"
"s_waitcnt lgkmcnt(4)\n"
"MA v,0,a,224,v,96\n"
"s_mov_b32 s86, 26880\n"
"TR 200,241,40960\n"
"MA v,0,a,228,v,112\n"
"TR 202,241,43008\n"
"TR 204,241,45056\n"
"MA v,4,a,228,v,116\n"
"TR 206,241,47104\n"
"MA v,4,a,224,v,100\n"
"buffer_load_dwordx4 v248, s[24:27], s86 offen offset:1024 lds\n"
"MA v,8,a,224,v,104\n"
"MA v,8,a,228,v,120\n"
"MA v,12,a,228,v,124\n"
"MA v,12,a,224,v,108\n"
"s_mov_b32 s87, 28928\n"
"MA v,28,a,232,v,108\n"
"TR 208,240,49152\n"
"TR 210,240,51200\n"
"MA v,28,a,236,v,124\n"
"TR 212,240,53248\n"
"TR 214,240,55296\n"
"MA v,24,a,236,v,120\n"
"buffer_load_dwordx4 v248, s[24:27], s87 offen offset:2048 lds\n"
"MA v,24,a,232,v,104\n"
"MA v,20,a,232,v,100\n"
"MA v,20,a,236,v,116\n"
"MA v,16,a,236,v,112\n"
"s_mov_b32 s88, 30976\n"
"MA v,16,a,232,v,96\n"
"s_waitcnt lgkmcnt(4)\n"
"MA v,0,a,192,v,128\n"
"TR 216,241,49152\n"
"TR 218,241,51200\n"
"MA v,0,a,196,v,144\n"
"buffer_load_dwordx4 v248, s[24:27], s88 offen offset:3072 lds\n"
"MA v,4,a,196,v,148\n"
"TR 220,241,53248\n"
"MA v,4,a,192,v,132\n"
"TR 222,241,55296\n"
"MA v,8,a,192,v,136\n"
"MA v,8,a,196,v,152\n"
"s_add_u32 m0, s46, 12288\n"
"s_mov_b32 s89, 37120\n"
"MA v,12,a,196,v,156\n"
"MA v,12,a,192,v,140\n"
"MA v,28,a,200,v,140\n"
"buffer_load_dwordx4 v248, s[24:27], s89 offen lds\n"
"MA v,28,a,204,v,156\n"
"TR 224,240,57344\n"
"MA v,24,a,204,v,152\n"
"TR 226,240,59392\n"
"TR 228,240,61440\n"
"MA v,24,a,200,v,136\n"
"TR 230,240,63488\n"
"v_xor_b32 v240, 0x10000, v240\n"
"MA v,20,a,200,v,132\n"
"s_mov_b32 s90, 39168\n"
"MA v,20,a,204,v,148\n"
"MA v,16,a,204,v,144\n"
"MA v,16,a,200,v,128\n"
"s_waitcnt lgkmcnt(4)\n"
"buffer_load_dwordx4 v248, s[24:27], s90 offen offset:1024 lds\n"
"MA v,0,a,208,v,160\n"
"TR 232,241,57344\n"
"MA v,0,a,212,v,176\n"
"TR 234,241,59392\n"
"TR 236,241,61440\n"
"MA v,4,a,212,v,180\n"
"TR 238,241,63488\n"
"v_xor_b32 v241, 0x10000, v241\n"
"MA v,4,a,208,v,164\n"
"s_mov_b32 s91, 41216\n"
"MA v,8,a,208,v,168\n"
"MA v,8,a,212,v,184\n"
"MA v,12,a,212,v,188\n"
"buffer_load_dwordx4 v248, s[24:27], s91 offen offset:2048 lds\n"
"MA v,12,a,208,v,172\n"
"MA v,28,a,216,v,172\n"
"TR 192,242,0\n"
"TR 194,242,2048\n"
"MA v,28,a,220,v,188\n"
"TR 196,242,4096\n"
"TR 198,242,6144\n"
"MA v,24,a,220,v,184\n"
"s_mov_b32 s86, 43264\n"
"MA v,24,a,216,v,168\n"
"MA v,20,a,216,v,164\n"
"MA v,20,a,220,v,180\n"
"buffer_load_dwordx4 v248, s[24:27], s86 offen offset:3072 lds\n"
"MA v,16,a,220,v,176\n"
"MA v,16,a,216,v,160\n"
"s_waitcnt lgkmcnt(4)\n"
"MA v,0,a,224,v,192\n"
"TR 200,243,0\n"
"TR 202,243,2048\n"
"MA v,0,a,228,v,32\n"
"TR 204,243,4096\n"
"TR 206,243,6144\n"
"MA v,4,a,228,v,36\n"
"MA v,4,a,224,v,196\n"
"MA v,8,a,224,v,200\n"
"s_nop 2\n"
"v_mul_f32 v0, v0, v236\n"
"v_mul_f32 v1, v1, v236\n"
"MA v,8,a,228,v,40\n"
"v_mul_f32 v2, v2, v236\n"
"v_mul_f32 v3, v3, v236\n"
"MA v,12,a,228,v,44\n"
"v_cvt_pk_bf16_f32 v224, v0, v1\n"
"v_cvt_pk_bf16_f32 v225, v2, v3\n"
"MA v,12,a,224,v,204\n"
"v_mul_f32 v4, v4, v237\n"
"v_mul_f32 v5, v5, v237\n"
"MA v,28,a,232,v,204\n"
"TR 208,242,8192\n"
"TR 210,242,10240\n"
"MA v,28,a,236,v,44\n"
"TR 212,242,12288\n"
"TR 214,242,14336\n"
"MA v,24,a,236,v,40\n"
"v_mul_f32 v6, v6, v237\n"
"v_mul_f32 v7, v7, v237\n"
"MA v,24,a,232,v,200\n"
"v_cvt_pk_bf16_f32 v228, v4, v5\n"
"v_cvt_pk_bf16_f32 v229, v6, v7\n"
"MA v,20,a,232,v,196\n"
"v_mul_f32 v8, v8, v238\n"
"v_mul_f32 v9, v9, v238\n"
"MA v,20,a,236,v,36\n"
"v_mul_f32 v10, v10, v238\n"
"v_mul_f32 v11, v11, v238\n"
"MA v,16,a,236,v,32\n"
"v_mul_f32 v24, v24, v238\n"
"v_mul_f32 v25, v25, v238\n"
"MA v,16,a,232,v,192\n"
"s_waitcnt lgkmcnt(4)\n"
"v_mul_f32 v26, v26, v238\n"
"v_mul_f32 v27, v27, v238\n"
"MZ v,48,a,192,v,64\n"
"TR 216,243,8192\n"
"TR 218,243,10240\n"
"MA v,48,a,196,v,80\n"
"s_add_u32 s15, s47, 8192\n"
"TR 220,243,12288\n"
"TR 222,243,14336\n"
"MZ v,52,a,196,v,84\n"
"s_add_u32 s1, s44, 65536\n"
"v_mul_f32 v16, v16, v236\n"
"v_mul_f32 v17, v17, v236\n"
"MA v,52,a,192,v,68\n"
"s_bfe_u32 s3, s54, 0x10010\n"
"v_mul_f32 v18, v18, v236\n"
"v_mul_f32 v19, v19, v236\n"
"MZ v,56,a,192,v,72\n"
"s_and_b32 s45, s54, 0xffff\n"
"v_cvt_pk_bf16_f32 v226, v16, v17\n"
"v_cvt_pk_bf16_f32 v227, v18, v19\n"
"MA v,56,a,196,v,88\n"
"buffer_store_dwordx4 v[224:227], v249, s[16:19], s15 offen sc1\n"
"MZ v,60,a,196,v,92\n"
"v_mul_f32 v20, v20, v237\n"
"MA v,60,a,192,v,76\n"
"v_mul_f32 v21, v21, v237\n"
"v_mul_f32 v22, v22, v237\n"
"MZ v,220,a,200,v,76\n"
"TR 224,242,16384\n"
"TR 226,242,18432\n"
"MA v,220,a,204,v,92\n"
"TR 228,242,20480\n"
"TR 230,242,22528\n"
"MZ v,216,a,204,v,88\n"
"v_mul_f32 v23, v23, v237\n"
"v_cvt_pk_bf16_f32 v230, v20, v21\n"
"MA v,216,a,200,v,72\n"
"v_cvt_pk_bf16_f32 v231, v22, v23\n"
"v_cvt_pk_bf16_f32 v224, v8, v9\n"
"MZ v,212,a,200,v,68\n"
"buffer_store_dwordx4 v[228:231], v249, s[16:19], s15 offen offset:1024 sc1\n"
"MA v,212,a,204,v,84\n"
"v_cvt_pk_bf16_f32 v225, v10, v11\n"
"MZ v,208,a,204,v,80\n"
"v_cvt_pk_bf16_f32 v226, v24, v25\n"
"v_cvt_pk_bf16_f32 v227, v26, v27\n"
"MA v,208,a,200,v,64\n"
"s_waitcnt lgkmcnt(4)\n"
"buffer_store_dwordx4 v[224:227], v249, s[16:19], s15 offen offset:2048 sc1\n"
"MA v,48,a,208,a,96\n"
"TR 232,243,16384\n"
"MA v,48,a,212,a,112\n"
"TR 234,243,18432\n"
"TR 236,243,20480\n"
"MA v,52,a,212,a,116\n"
"TR 238,243,22528\n"
"v_mul_f32 v12, v12, v239\n"
"MA v,52,a,208,a,100\n"
"v_mul_f32 v13, v13, v239\n"
"v_mul_f32 v14, v14, v239\n"
"MA v,56,a,208,a,104\n"
"v_mul_f32 v15, v15, v239\n"
"v_mul_f32 v28, v28, v239\n"
"MA v,56,a,212,a,120\n"
"v_mul_f32 v29, v29, v239\n"
"v_mul_f32 v30, v30, v239\n"
"MA v,60,a,212,a,124\n"
"v_cvt_pk_bf16_f32 v228, v12, v13\n"
"v_mul_f32 v31, v31, v239\n"
"MA v,60,a,208,a,108\n"
"v_cvt_pk_bf16_f32 v229, v14, v15\n"
"v_cvt_pk_bf16_f32 v230, v28, v29\n"
"MA v,220,a,216,a,108\n"
"TR 192,242,24576\n"
"TR 194,242,26624\n"
"MA v,220,a,220,a,124\n"
"TR 196,242,28672\n"
"TR 198,242,30720\n"
"MA v,216,a,220,a,120\n"
"v_cvt_pk_bf16_f32 v231, v30, v31\n"
"v_lshrrev_b32 v233, 1, v249\n"
"MA v,216,a,216,a,104\n"
"buffer_store_dwordx4 v[228:231], v249, s[16:19], s15 offen offset:3072 sc1\n"
"MA v,212,a,216,a,100\n"
"v_lshrrev_b32 v252, 6, v249\n"
"MA v,212,a,220,a,116\n"
"v_and_b32 v253, 6, v252\n"
"MA v,208,a,220,a,112\n"
"v_xor_b32 v253, v253, v246\n"
"v_lshrrev_b32 v246, 7, v249\n"
"MA v,208,a,216,a,96\n"
"s_waitcnt lgkmcnt(4)\n"
"v_lshlrev_b32 v253, 4, v253\n"
"v_mul_u32_u24 v246, 896, v246\n"
"MA v,48,a,224,a,128\n"
"TR 200,243,24576\n"
"TR 202,243,26624\n"
"MA v,48,a,228,a,144\n"
"TR 204,243,28672\n"
"TR 206,243,30720\n"
"MA v,52,a,228,a,148\n"
"v_lshl_add_u32 v244, v252, 7, v253\n"
"v_add3_u32 v246, v246, v248, s76\n"
"MA v,52,a,224,a,132\n"
"v_add_u32 v247, 9216, v246\n"
"v_add_u32 v250, 128, v246\n"
"MA v,56,a,224,a,136\n"
"v_add_u32 v251, 9344, v246\n"
"v_bfe_u32 v253, v249, 4, 4\n"
"MA v,56,a,228,a,152\n"
"v_mul_u32_u24 v253, 1280, v253\n"
"v_lshrrev_b32 v252, 8, v249\n"
"MA v,60,a,228,a,156\n"
"v_lshl_add_u32 v253, v252, 4, v253\n"
"v_add_u32 v252, 4096, v249\n"
"MA v,60,a,224,a,140\n"
"v_add_u32 v244, 65536, v244\n"
"MA v,220,a,232,a,140\n"
"TR 208,242,32768\n"
"TR 210,242,34816\n"
"MA v,220,a,236,a,156\n"
"TR 212,242,36864\n"
"TR 214,242,38912\n"
"MA v,216,a,236,a,152\n"
"s_add_u32 s8, s56, 2\n"
"v_mov_b32 v255, s57\n"
"v_xor_b32 v245, 64, v244\n"
"MA v,216,a,232,a,136\n"
"v_mov_b32 v254, s8\n"
"MA v,212,a,232,a,132\n"
"MA v,212,a,236,a,148\n"
"MA v,208,a,236,a,144\n"
"MA v,208,a,232,a,128\n"
"s_waitcnt lgkmcnt(4)\n"
"MA v,48,a,192,a,160\n"
"TR 216,243,32768\n"
"TR 218,243,34816\n"
"MA v,48,a,196,a,176\n"
"TR 220,243,36864\n"
"TR 222,243,38912\n"
"MA v,52,a,196,a,180\n"
"s_waitcnt vmcnt(15)\n"
"buffer_store_dwordx2 v[254:255], v234, s[16:19], s60 offen sc1\n"
"MA v,52,a,192,a,164\n"
"MA v,56,a,192,a,168\n"
"MA v,56,a,196,a,184\n"
"MA v,60,a,196,a,188\n"
"MA v,60,a,192,a,172\n"
"MA v,220,a,200,a,172\n"
"TR 224,242,40960\n"
"TR 226,242,43008\n"
"MA v,220,a,204,a,188\n"
"TR 228,242,45056\n"
"TR 230,242,47104\n"
"MA v,216,a,204,a,184\n"
"MA v,216,a,200,a,168\n"
"MA v,212,a,200,a,164\n"
"s_bfe_u32 s2, s54, 0x30012\n"
"s_lshl_b32 s0, s2, 29\n"
"s_mul_i32 s2, s2, s77\n"
"s_add_u32 s20, s71, s2\n"
"s_mul_i32 s23, s2, 10\n"
"s_mov_b32 s46, 0\n"
"s_bitcmp1_b32 s54, 16\n"
"s_cselect_b32 s48, 8, 64\n"
"s_lshl_b32 s48, s48, 10\n"
"MA v,212,a,204,a,180\n"
"MA v,208,a,204,a,176\n"
"MA v,208,a,200,a,160\n"
"s_waitcnt lgkmcnt(4)\n"
"MA v,48,a,208,v,96\n"
"TR 232,243,40960\n"
"TR 234,243,43008\n"
"MA v,48,a,212,v,112\n"
"TR 236,243,45056\n"
"TR 238,243,47104\n"
"MA v,52,a,212,v,116\n"
"MA v,52,a,208,v,100\n"
"MA v,56,a,208,v,104\n"
"MA v,56,a,212,v,120\n"
"MA v,60,a,212,v,124\n"
"MA v,60,a,208,v,108\n"
"MA v,220,a,216,v,108\n"
"TR 192,242,49152\n"
"TR 194,242,51200\n"
"MA v,220,a,220,v,124\n"
"TR 196,242,53248\n"
"TR 198,242,55296\n"
"MA v,216,a,220,v,120\n"
"MA v,216,a,216,v,104\n"
"MA v,212,a,216,v,100\n"
"MA v,212,a,220,v,116\n"
"MA v,208,a,220,v,112\n"
"MA v,208,a,216,v,96\n"
"s_waitcnt lgkmcnt(4)\n"
"MA v,48,a,224,v,128\n"
"s_add_u32 s0, s0, 0x20000000\n"
"s_cselect_b32 s2, s84, s77\n"
"TR 200,243,49152\n"
"MA v,48,a,228,v,144\n"
"s_add_u32 s46, s46, 0x20000000\n"
"s_cselect_b32 s49, s48, 0\n"
"TR 202,243,51200\n"
"TR 204,243,53248\n"
"MA v,52,a,228,v,148\n"
"s_add_u32 s2, s2, s49\n"
"TR 206,243,55296\n"
"MA v,52,a,224,v,132\n"
"s_add_u32 s21, s20, s2\n"
"s_mul_i32 s2, s2, 10\n"
"MA v,56,a,224,v,136\n"
"s_add_u32 s4, s23, s2\n"
"MA v,56,a,228,v,152\n"
"MA v,60,a,228,v,156\n"
"MA v,60,a,224,v,140\n"
"MA v,220,a,232,v,140\n"
"TR 208,242,57344\n"
"TR 210,242,59392\n"
"MA v,220,a,236,v,156\n"
"TR 212,242,61440\n"
"TR 214,242,63488\n"
"MA v,216,a,236,v,152\n"
"v_xor_b32 v242, 0x10000, v242\n"
"MA v,216,a,232,v,136\n"
"MA v,212,a,232,v,132\n"
"MA v,212,a,236,v,148\n"
"MA v,208,a,236,v,144\n"
"MA v,208,a,232,v,128\n"
"s_waitcnt lgkmcnt(4)\n"
"MA v,48,a,192,v,160\n"
"TR 216,243,57344\n"
"TR 218,243,59392\n"
"MA v,48,a,196,v,176\n"
"TR 220,243,61440\n"
"TR 222,243,63488\n"
"MA v,52,a,196,v,180\n"
"v_xor_b32 v243, 0x10000, v243\n"
"MA v,52,a,192,v,164\n"
"MA v,56,a,192,v,168\n"
"s_add_u32 s0, s0, 0x20000000\n"
"s_cselect_b32 s2, s84, s77\n"
"s_add_u32 s46, s46, 0x20000000\n"
"s_cselect_b32 s49, s48, 0\n"
"MA v,56,a,196,v,184\n"
"s_add_u32 s2, s2, s49\n"
"MA v,60,a,196,v,188\n"
"s_add_u32 s22, s21, s2\n"
"s_mul_i32 s2, s2, 10\n"
"MA v,60,a,192,v,172\n"
"s_add_u32 s5, s4, s2\n"
"MA v,220,a,200,v,172\n"
"s_waitcnt vmcnt(5)\n"
"s_barrier\n"
"TR 224,240,0\n"
"MA v,220,a,204,v,188\n"
"TR 226,240,2048\n"
"TR 228,240,4096\n"
"MA v,216,a,204,v,184\n"
"TR 230,240,6144\n"
"MA v,216,a,200,v,168\n"
"MA v,212,a,200,v,164\n"
"MA v,212,a,204,v,180\n"
"MA v,208,a,204,v,176\n"
"MA v,208,a,200,v,160\n"
"s_waitcnt lgkmcnt(4)\n"
"MA v,48,a,208,v,192\n"
"TR 232,241,0\n"
"TR 234,241,2048\n"
"MA v,48,a,212,v,32\n"
"TR 236,241,4096\n"
"TR 238,241,6144\n"
"MA v,52,a,212,v,36\n"
"MA v,52,a,208,v,196\n"
"MA v,56,a,208,v,200\n"
"s_nop 2\n"
"v_mul_f32 v48, v48, v236\n"
"v_mul_f32 v49, v49, v236\n"
"MA v,56,a,212,v,40\n"
"v_mul_f32 v50, v50, v236\n"
"v_mul_f32 v51, v51, v236\n"
"MA v,60,a,212,v,44\n"
"v_cvt_pk_bf16_f32 v224, v48, v49\n"
"v_cvt_pk_bf16_f32 v225, v50, v51\n"
"MA v,60,a,208,v,204\n"
"v_mul_f32 v52, v52, v237\n"
"v_mul_f32 v53, v53, v237\n"
"MA v,220,a,216,v,204\n"
"TR 192,240,8192\n"
"TR 194,240,10240\n"
"MA v,220,a,220,v,44\n"
"TR 196,240,12288\n"
"TR 198,240,14336\n"
"MA v,216,a,220,v,40\n"
"buffer_load_dwordx2 v[254:255], v233, s[16:19], s61 offen sc1\n"
"MA v,216,a,216,v,200\n"
"v_mul_f32 v54, v54, v237\n"
"MA v,212,a,216,v,196\n"
"v_mul_f32 v55, v55, v237\n"
"v_cvt_pk_bf16_f32 v228, v52, v53\n"
"MA v,212,a,220,v,36\n"
"v_cvt_pk_bf16_f32 v229, v54, v55\n"
"v_mul_f32 v56, v56, v238\n"
"MA v,208,a,220,v,32\n"
"v_mul_f32 v57, v57, v238\n"
"v_mul_f32 v58, v58, v238\n"
"MA v,208,a,216,v,192\n"
"s_waitcnt lgkmcnt(4)\n"
"v_mul_f32 v59, v59, v238\n"
"v_mul_f32 v216, v216, v238\n"
"MZ v,0,a,224,v,64\n"
"TR 200,241,8192\n"
"TR 202,241,10240\n"
"MA v,0,a,228,v,80\n"
"s_add_u32 s9, s47, 12288\n"
"TR 204,241,12288\n"
"TR 206,241,14336\n"
"MZ v,4,a,228,v,84\n"
"v_mul_f32 v208, v208, v236\n"
"v_mul_f32 v209, v209, v236\n"
"MA v,4,a,224,v,68\n"
"v_mul_f32 v210, v210, v236\n"
"v_mul_f32 v211, v211, v236\n"
"MZ v,8,a,224,v,72\n"
"v_cvt_pk_bf16_f32 v226, v208, v209\n"
"v_cvt_pk_bf16_f32 v227, v210, v211\n"
"MA v,8,a,228,v,88\n"
"buffer_store_dwordx4 v[224:227], v249, s[16:19], s9 offen sc1\n"
"MZ v,12,a,228,v,92\n"
"v_mul_f32 v212, v212, v237\n"
"MA v,12,a,224,v,76\n"
"v_mul_f32 v213, v213, v237\n"
"v_mul_f32 v214, v214, v237\n"
"MZ v,28,a,232,v,76\n"
"s_barrier\n"
"TR 208,240,16384\n"
"MA v,28,a,236,v,92\n"
"TR 210,240,18432\n"
"TR 212,240,20480\n"
"MZ v,24,a,236,v,88\n"
"TR 214,240,22528\n"
"v_mul_f32 v215, v215, v237\n"
"MA v,24,a,232,v,72\n"
"s_add_u32 m0, s1, 0\n"
"v_cvt_pk_bf16_f32 v230, v212, v213\n"
"v_cvt_pk_bf16_f32 v231, v214, v215\n"
"MZ v,20,a,232,v,68\n"
"buffer_store_dwordx4 v[228:231], v249, s[16:19], s9 offen offset:1024 sc1\n"
"MA v,20,a,236,v,84\n"
"v_mul_f32 v217, v217, v238\n"
"MZ v,16,a,236,v,80\n"
"buffer_load_dwordx4 v246, s[72:75], s23 offen lds\n"
"MA v,16,a,232,v,64\n"
"s_waitcnt lgkmcnt(4)\n"
"v_mul_f32 v218, v218, v238\n"
"MA v,0,a,192,a,96\n"
"TR 216,241,16384\n"
"TR 218,241,18432\n"
"MA v,0,a,196,a,112\n"
"TR 220,241,20480\n"
"TR 222,241,22528\n"
"MA v,4,a,196,a,116\n"
"v_cvt_pk_bf16_f32 v224, v56, v57\n"
"v_mul_f32 v219, v219, v238\n"
"MA v,4,a,192,a,100\n"
"v_cvt_pk_bf16_f32 v225, v58, v59\n"
"v_cvt_pk_bf16_f32 v226, v216, v217\n"
"MA v,8,a,192,a,104\n"
"buffer_load_dwordx4 v247, s[72:75], s23 offen offset:1024 lds\n"
"MA v,8,a,196,a,120\n"
"v_cvt_pk_bf16_f32 v227, v218, v219\n"
"MA v,12,a,196,a,124\n"
"buffer_store_dwordx4 v[224:227], v249, s[16:19], s9 offen offset:2048 sc1\n"
"MA v,12,a,192,a,108\n"
"s_add_u32 m0, s1, 8192\n"
"MA v,28,a,200,a,108\n"
"TR 224,240,24576\n"
"TR 226,240,26624\n"
"MA v,28,a,204,a,124\n"
"TR 228,240,28672\n"
"TR 230,240,30720\n"
"MA v,24,a,204,a,120\n"
"buffer_load_dwordx4 v250, s[72:75], s23 offen lds\n"
"MA v,24,a,200,a,104\n"
"v_mul_f32 v60, v60, v239\n"
"MA v,20,a,200,a,100\n"
"v_mul_f32 v61, v61, v239\n"
"v_mul_f32 v62, v62, v239\n"
"MA v,20,a,204,a,116\n"
"v_mul_f32 v63, v63, v239\n"
"v_mul_f32 v220, v220, v239\n"
"MA v,16,a,204,a,112\n"
"v_mul_f32 v221, v221, v239\n"
"v_mul_f32 v222, v222, v239\n"
"MA v,16,a,200,a,96\n"
"s_waitcnt lgkmcnt(4)\n"
"v_cvt_pk_bf16_f32 v228, v60, v61\n"
"v_mul_f32 v223, v223, v239\n"
"MA v,0,a,208,a,128\n"
"buffer_load_dwordx4 v251, s[72:75], s23 offen offset:1024 lds\n"
"MA v,0,a,212,a,144\n"
"TR 232,241,24576\n"
"MA v,4,a,212,a,148\n"
"TR 234,241,26624\n"
"TR 236,241,28672\n"
"MA v,4,a,208,a,132\n"
"TR 238,241,30720\n"
"v_cvt_pk_bf16_f32 v229, v62, v63\n"
"MA v,8,a,208,a,136\n"
"v_cvt_pk_bf16_f32 v230, v220, v221\n"
"v_cvt_pk_bf16_f32 v231, v222, v223\n"
"MA v,8,a,212,a,152\n"
"s_add_u32 m0, s1, 16384\n"
"buffer_store_dwordx4 v[228:231], v249, s[16:19], s9 offen offset:3072 sc1\n"
"MA v,12,a,212,a,156\n"
"MA v,12,a,208,a,140\n"
"MA v,28,a,216,a,140\n"
"buffer_load_dwordx4 v246, s[72:75], s4 offen lds\n"
"MA v,28,a,220,a,156\n"
"TR 192,240,32768\n"
"MA v,24,a,220,a,152\n"
"TR 194,240,34816\n"
"TR 196,240,36864\n"
"MA v,24,a,216,a,136\n"
"TR 198,240,38912\n"
"MA v,20,a,216,a,132\n"
"MA v,20,a,220,a,148\n"
"MA v,16,a,220,a,144\n"
"buffer_load_dwordx4 v247, s[72:75], s4 offen offset:1024 lds\n"
"MA v,16,a,216,a,128\n"
"s_waitcnt lgkmcnt(4)\n"
"MA v,0,a,224,a,160\n"
"TR 200,241,32768\n"
"TR 202,241,34816\n"
"MA v,0,a,228,a,176\n"
"s_add_u32 m0, s1, 24576\n"
"TR 204,241,36864\n"
"TR 206,241,38912\n"
"MA v,4,a,228,a,180\n"
"MA v,4,a,224,a,164\n"
"MA v,8,a,224,a,168\n"
"buffer_load_dwordx4 v250, s[72:75], s4 offen lds\n"
"MA v,8,a,228,a,184\n"
"MA v,12,a,228,a,188\n"
"MA v,12,a,224,a,172\n"
"MA v,28,a,232,a,172\n"
"TR 208,240,40960\n"
"TR 210,240,43008\n"
"MA v,28,a,236,a,188\n"
"TR 212,240,45056\n"
"TR 214,240,47104\n"
"MA v,24,a,236,a,184\n"
"buffer_load_dwordx4 v251, s[72:75], s4 offen offset:1024 lds\n"
"MA v,24,a,232,a,168\n"
"MA v,20,a,232,a,164\n"
"MA v,20,a,236,a,180\n"
"MA v,16,a,236,a,176\n"
"MA v,16,a,232,a,160\n"
"s_waitcnt lgkmcnt(4)\n"
"s_add_u32 m0, s1, 32768\n"
"MA v,0,a,192,v,96\n"
"TR 216,241,40960\n"
"TR 218,241,43008\n"
"MA v,0,a,196,v,112\n"
"TR 220,241,45056\n"
"TR 222,241,47104\n"
"MA v,4,a,196,v,116\n"
"buffer_load_dwordx4 v246, s[72:75], s5 offen lds\n"
"MA v,4,a,192,v,100\n"
"MA v,8,a,192,v,104\n"
"MA v,8,a,196,v,120\n"
"MA v,12,a,196,v,124\n"
"MA v,12,a,192,v,108\n"
"MA v,28,a,200,v,108\n"
"buffer_load_dwordx4 v247, s[72:75], s5 offen offset:1024 lds\n"
"MA v,28,a,204,v,124\n"
"TR 224,240,49152\n"
"MA v,24,a,204,v,120\n"
"TR 226,240,51200\n"
"TR 228,240,53248\n"
"MA v,24,a,200,v,104\n"
"s_add_u32 m0, s1, 40960\n"
"TR 230,240,55296\n"
"MA v,20,a,200,v,100\n"
"MA v,20,a,204,v,116\n"
"MA v,16,a,204,v,112\n"
"buffer_load_dwordx4 v250, s[72:75], s5 offen lds\n"
"MA v,16,a,200,v,96\n"
"s_waitcnt lgkmcnt(4)\n"
"MA v,0,a,208,v,128\n"
"TR 232,241,49152\n"
"TR 234,241,51200\n"
"MA v,0,a,212,v,144\n"
"TR 236,241,53248\n"
"TR 238,241,55296\n"
"MA v,4,a,212,v,148\n"
"s_mul_i32 s10, s3, 1\n"
"s_add_u32 s10, s10, s56\n"
"s_add_u32 s10, s10, 1\n"
"MA v,4,a,208,v,132\n"
"MA v,8,a,208,v,136\n"
"buffer_load_dwordx4 v251, s[72:75], s5 offen offset:1024 lds\n"
"MA v,8,a,212,v,152\n"
"MA v,12,a,212,v,156\n"
"MA v,12,a,208,v,140\n"
"MA v,28,a,216,v,140\n"
"s_waitcnt vmcnt(16)\n"
"v_subrev_u32 v254, s10, v254\n"
"v_cmp_eq_u32 s[94:95], s57, v255\n"
"MA v,28,a,220,v,156\n"
"TR 192,240,57344\n"
"v_cmp_gt_u32 vcc, 8, v254\n"
"MA v,24,a,220,v,152\n"
"TR 194,240,59392\n"
"TR 196,240,61440\n"
"MA v,24,a,216,v,136\n"
"TR 198,240,63488\n"
"MA v,20,a,216,v,132\n"
"s_and_b32 vcc_lo, vcc_lo, s94\n"
"s_andn2_b32 vcc_lo, s64, vcc_lo\n"
"s_cbranch_scc1 L_SLOW2_%=\n"
"L_BACK2_%=:\n"
"MA v,20,a,220,v,148\n"
"MA v,16,a,220,v,144\n"
"MA v,16,a,216,v,128\n"
"s_waitcnt lgkmcnt(4)\n"
"MA v,0,a,224,v,160\n"
"buffer_load_dwordx4 a[0:3], v249, s[16:19], s20 offen sc1\n"
"MA v,0,a,228,v,176\n"
"TR 200,241,57344\n"
"MA v,4,a,228,v,180\n"
"TR 202,241,59392\n"
"TR 204,241,61440\n"
"MA v,4,a,224,v,164\n"
"TR 206,241,63488\n"
"MA v,8,a,224,v,168\n"
"MA v,8,a,228,v,184\n"
"MA v,12,a,228,v,188\n"
"MA v,12,a,224,v,172\n"
"MA v,28,a,232,v,172\n"
"TR 208,242,0\n"
"TR 210,242,2048\n"
"MA v,28,a,236,v,188\n"
"TR 212,242,4096\n"
"TR 214,242,6144\n"
"MA v,24,a,236,v,184\n"
"buffer_load_dwordx4 a[4:7], v249, s[16:19], s20 offen offset:1024 sc1\n"
"MA v,24,a,232,v,168\n"
"MA v,20,a,232,v,164\n"
"MA v,20,a,236,v,180\n"
"MA v,16,a,236,v,176\n"
"MA v,16,a,232,v,160\n"
"s_waitcnt lgkmcnt(4)\n"
"MA v,0,a,192,v,192\n"
"TR 216,243,0\n"
"TR 218,243,2048\n"
"MA v,0,a,196,v,32\n"
"TR 220,243,4096\n"
"TR 222,243,6144\n"
"MA v,4,a,196,v,36\n"
"MA v,4,a,192,v,196\n"
"MA v,8,a,192,v,200\n"
"buffer_load_dwordx4 a[8:11], v249, s[16:19], s20 offen offset:2048 sc1\n"
"MA v,8,a,196,v,40\n"
"s_nop 0\n"
"v_mul_f32 v0, v0, v236\n"
"MA v,12,a,196,v,44\n"
"v_mul_f32 v1, v1, v236\n"
"v_mul_f32 v2, v2, v236\n"
"MA v,12,a,192,v,204\n"
"v_mul_f32 v3, v3, v236\n"
"v_cvt_pk_bf16_f32 v224, v0, v1\n"
"MA v,28,a,200,v,204\n"
"TR 224,242,8192\n"
"TR 226,242,10240\n"
"MA v,28,a,204,v,44\n"
"TR 228,242,12288\n"
"TR 230,242,14336\n"
"MA v,24,a,204,v,40\n"
"v_cvt_pk_bf16_f32 v225, v2, v3\n"
"v_mul_f32 v4, v4, v237\n"
"MA v,24,a,200,v,200\n"
"v_mul_f32 v5, v5, v237\n"
"v_mul_f32 v6, v6, v237\n"
"MA v,20,a,200,v,196\n"
"v_mul_f32 v7, v7, v237\n"
"v_cvt_pk_bf16_f32 v228, v4, v5\n"
"MA v,20,a,204,v,36\n"
"v_cvt_pk_bf16_f32 v229, v6, v7\n"
"v_mul_f32 v8, v8, v238\n"
"MA v,16,a,204,v,32\n"
"buffer_load_dwordx4 a[12:15], v249, s[16:19], s20 offen offset:3072 sc1\n"
"MA v,16,a,200,v,192\n"
"s_waitcnt lgkmcnt(4)\n"
"v_mul_f32 v9, v9, v238\n"
"MZ v,48,a,208,v,64\n"
"TR 232,243,8192\n"
"TR 234,243,10240\n"
"MA v,48,a,212,v,80\n"
"s_lshl_b32 s87, s44, 2\n"
"s_add_u32 s87, s87, L_JOIN%=-.Lfused_text0+28000\n"
"s_cmp_eq_u32 s53, 4\n"
"s_cselect_b32 s87, s87, s55\n"
"TR 236,243,12288\n"
"TR 238,243,14336\n"
"MZ v,52,a,212,v,84\n"
"s_add_u32 s11, s47, 16384\n"
"v_mul_f32 v16, v16, v236\n"
"v_mul_f32 v17, v17, v236\n"
"MA v,52,a,208,v,68\n"
"v_mul_f32 v18, v18, v236\n"
"v_mul_f32 v19, v19, v236\n"
"MZ v,56,a,208,v,72\n"
"buffer_load_dword v235, v232, s[96:99], s87 offen\n"
"MA v,56,a,212,v,88\n"
"v_cvt_pk_bf16_f32 v226, v16, v17\n"
"MZ v,60,a,212,v,92\n"
"v_cvt_pk_bf16_f32 v227, v18, v19\n"
"v_mul_f32 v20, v20, v237\n"
"MA v,60,a,208,v,76\n"
"buffer_store_dwordx4 v[224:227], v249, s[16:19], s11 offen sc1\n"
"MZ v,220,a,216,v,76\n"
"TR 192,242,16384\n"
"MA v,220,a,220,v,92\n"
"buffer_load_dwordx4 a[16:19], v252, s[16:19], s20 offen sc1\n"
"MZ v,216,a,220,v,88\n"
"TR 194,242,18432\n"
"MA v,216,a,216,v,72\n"
"TR 196,242,20480\n"
"TR 198,242,22528\n"
"MZ v,212,a,216,v,68\n"
"v_mul_f32 v21, v21, v237\n"
"v_mul_f32 v22, v22, v237\n"
"MA v,212,a,220,v,84\n"
"v_mul_f32 v23, v23, v237\n"
"v_cvt_pk_bf16_f32 v230, v20, v21\n"
"MZ v,208,a,220,v,80\n"
"v_cvt_pk_bf16_f32 v231, v22, v23\n"
"v_mul_f32 v10, v10, v238\n"
"MA v,208,a,216,v,64\n"
"s_waitcnt lgkmcnt(4)\n"
"buffer_store_dwordx4 v[228:231], v249, s[16:19], s11 offen offset:1024 sc1\n"
"MA v,48,a,224,a,96\n"
"TR 200,243,16384\n"
"MA v,48,a,228,a,112\n"
"TR 202,243,18432\n"
"TR 204,243,20480\n"
"MA v,52,a,228,a,116\n"
"buffer_load_dwordx4 a[20:23], v252, s[16:19], s20 offen offset:1024 sc1\n"
"MA v,52,a,224,a,100\n"
"TR 206,243,22528\n"
"MA v,56,a,224,a,104\n"
"v_mul_f32 v11, v11, v238\n"
"v_mul_f32 v24, v24, v238\n"
"MA v,56,a,228,a,120\n"
"v_mul_f32 v25, v25, v238\n"
"v_mul_f32 v26, v26, v238\n"
"MA v,60,a,228,a,124\n"
"v_cvt_pk_bf16_f32 v224, v8, v9\n"
"v_mul_f32 v27, v27, v238\n"
"MA v,60,a,224,a,108\n"
"v_cvt_pk_bf16_f32 v225, v10, v11\n"
"v_cvt_pk_bf16_f32 v226, v24, v25\n"
"MA v,220,a,232,a,108\n"
"TR 208,242,24576\n"
"TR 210,242,26624\n"
"MA v,220,a,236,a,124\n"
"TR 212,242,28672\n"
"TR 214,242,30720\n"
"MA v,216,a,236,a,120\n"
"v_cvt_pk_bf16_f32 v227, v26, v27\n"
"v_mul_f32 v12, v12, v239\n"
"MA v,216,a,232,a,104\n"
"buffer_store_dwordx4 v[224:227], v249, s[16:19], s11 offen offset:2048 sc1\n"
"MA v,212,a,232,a,100\n"
"v_mul_f32 v13, v13, v239\n"
"MA v,212,a,236,a,116\n"
"buffer_load_dwordx4 a[24:27], v252, s[16:19], s20 offen offset:2048 sc1\n"
"MA v,208,a,236,a,112\n"
"v_mul_f32 v14, v14, v239\n"
"MA v,208,a,232,a,96\n"
"s_waitcnt lgkmcnt(4)\n"
"v_mul_f32 v15, v15, v239\n"
"v_mul_f32 v28, v28, v239\n"
"MA v,48,a,192,a,128\n"
"TR 216,243,24576\n"
"TR 218,243,26624\n"
"MA v,48,a,196,a,144\n"
"TR 220,243,28672\n"
"TR 222,243,30720\n"
"MA v,52,a,196,a,148\n"
"v_mul_f32 v29, v29, v239\n"
"v_mul_f32 v30, v30, v239\n"
"MA v,52,a,192,a,132\n"
"v_cvt_pk_bf16_f32 v228, v12, v13\n"
"v_mul_f32 v31, v31, v239\n"
"MA v,56,a,192,a,136\n"
"v_cvt_pk_bf16_f32 v229, v14, v15\n"
"v_cvt_pk_bf16_f32 v230, v28, v29\n"
"MA v,56,a,196,a,152\n"
"v_cvt_pk_bf16_f32 v231, v30, v31\n"
"MA v,60,a,196,a,156\n"
"buffer_load_dwordx4 a[28:31], v252, s[16:19], s20 offen offset:3072 sc1\n"
"MA v,60,a,192,a,140\n"
"MA v,220,a,200,a,140\n"
"TR 224,242,32768\n"
"TR 226,242,34816\n"
"MA v,220,a,204,a,156\n"
"TR 228,242,36864\n"
"TR 230,242,38912\n"
"MA v,216,a,204,a,152\n"
"buffer_store_dwordx4 v[228:231], v249, s[16:19], s11 offen offset:3072 sc1\n"
"MA v,216,a,200,a,136\n"
"MA v,212,a,200,a,132\n"
"MA v,212,a,204,a,148\n"
"MA v,208,a,204,a,144\n"
"MA v,208,a,200,a,128\n"
"s_waitcnt lgkmcnt(4)\n"
"MA v,48,a,208,a,160\n"
"buffer_load_dwordx4 a[32:35], v249, s[16:19], s21 offen sc1\n"
"MA v,48,a,212,a,176\n"
"TR 232,243,32768\n"
"MA v,52,a,212,a,180\n"
"TR 234,243,34816\n"
"TR 236,243,36864\n"
"MA v,52,a,208,a,164\n"
"TR 238,243,38912\n"
"MA v,56,a,208,a,168\n"
"MA v,56,a,212,a,184\n"
"MA v,60,a,212,a,188\n"
"MA v,60,a,208,a,172\n"
"MA v,220,a,216,a,172\n"
"TR 192,242,40960\n"
"TR 194,242,43008\n"
"MA v,220,a,220,a,188\n"
"TR 196,242,45056\n"
"TR 198,242,47104\n"
"MA v,216,a,220,a,184\n"
"buffer_load_dwordx4 a[36:39], v249, s[16:19], s21 offen offset:1024 sc1\n"
"MA v,216,a,216,a,168\n"
"MA v,212,a,216,a,164\n"
"MA v,212,a,220,a,180\n"
"MA v,208,a,220,a,176\n"
"MA v,208,a,216,a,160\n"
"s_waitcnt lgkmcnt(4)\n"
"MA v,48,a,224,v,96\n"
"TR 200,243,40960\n"
"TR 202,243,43008\n"
"MA v,48,a,228,v,112\n"
"TR 204,243,45056\n"
"TR 206,243,47104\n"
"MA v,52,a,228,v,116\n"
"MA v,52,a,224,v,100\n"
"MA v,56,a,224,v,104\n"
"buffer_load_dwordx4 a[40:43], v249, s[16:19], s21 offen offset:2048 sc1\n"
"MA v,56,a,228,v,120\n"
"MA v,60,a,228,v,124\n"
"MA v,60,a,224,v,108\n"
"MA v,220,a,232,v,108\n"
"TR 208,242,49152\n"
"TR 210,242,51200\n"
"MA v,220,a,236,v,124\n"
"TR 212,242,53248\n"
"TR 214,242,55296\n"
"MA v,216,a,236,v,120\n"
"MA v,216,a,232,v,104\n"
"MA v,212,a,232,v,100\n"
"MA v,212,a,236,v,116\n"
"MA v,208,a,236,v,112\n"
"buffer_load_dwordx4 a[44:47], v249, s[16:19], s21 offen offset:3072 sc1\n"
"MA v,208,a,232,v,96\n"
"s_waitcnt lgkmcnt(4)\n"
"MA v,48,a,192,v,128\n"
"TR 216,243,49152\n"
"TR 218,243,51200\n"
"MA v,48,a,196,v,144\n"
"TR 220,243,53248\n"
"TR 222,243,55296\n"
"MA v,52,a,196,v,148\n"
"MA v,52,a,192,v,132\n"
"MA v,56,a,192,v,136\n"
"s_add_u32 s0, s0, 0x20000000\n"
"s_cselect_b32 s2, s84, s77\n"
"s_add_u32 s46, s46, 0x20000000\n"
"s_cselect_b32 s49, s48, 0\n"
"MA v,56,a,196,v,152\n"
"s_add_u32 s2, s2, s49\n"
"MA v,60,a,196,v,156\n"
"s_add_u32 s20, s22, s2\n"
"s_mul_i32 s2, s2, 10\n"
"MA v,60,a,192,v,140\n"
"s_add_u32 s23, s5, s2\n"
"MA v,220,a,200,v,140\n"
"buffer_load_dwordx4 a[48:51], v252, s[16:19], s21 offen sc1\n"
"MA v,220,a,204,v,156\n"
"s_add_u32 s12, s56, 3\n"
"v_mov_b32 v255, s57\n"
"MA v,216,a,204,v,152\n"
"TR 224,242,57344\n"
"v_mov_b32 v254, s12\n"
"MA v,216,a,200,v,136\n"
"s_add_u32 s12, s56, 1\n"
"TR 226,242,59392\n"
"TR 228,242,61440\n"
"MA v,212,a,200,v,132\n"
"TR 230,242,63488\n"
"MA v,212,a,204,v,148\n"
"MA v,208,a,204,v,144\n"
"MA v,208,a,200,v,128\n"
"s_waitcnt lgkmcnt(4)\n"
"MA v,48,a,208,v,160\n"
"s_waitcnt vmcnt(26)\n"
"buffer_store_dwordx2 v[254:255], v234, s[16:19], s60 offen sc1\n"
"MA v,48,a,212,v,176\n"
"TR 232,243,57344\n"
"MA v,52,a,212,v,180\n"
"buffer_load_dwordx4 a[52:55], v252, s[16:19], s21 offen offset:1024 sc1\n"
"MA v,52,a,208,v,164\n"
"TR 234,243,59392\n"
"MA v,56,a,208,v,168\n"
"s_xor_b32 s1, s1, 49152\n"
"TR 236,243,61440\n"
"TR 238,243,63488\n"
"MA v,56,a,212,v,184\n"
"MA v,60,a,212,v,188\n"
"MA v,60,a,208,v,172\n"
"s_add_u32 m0, s1, 0\n"
"MA v,220,a,216,v,172\n"
"s_barrier\n"
"TR 192,244,0\n"
"TR 194,244,2048\n"
"MA v,220,a,220,v,188\n"
"TR 196,244,4096\n"
"TR 198,244,6144\n"
"MA v,216,a,220,v,184\n"
"buffer_load_dwordx4 v246, s[72:75], s23 offen lds\n"
"MA v,216,a,216,v,168\n"
"MA v,212,a,216,v,164\n"
"buffer_load_dwordx4 a[56:59], v252, s[16:19], s21 offen offset:2048 sc1\n"
"MA v,212,a,220,v,180\n"
"MA v,208,a,220,v,176\n"
"MA v,208,a,216,v,160\n"
"s_waitcnt lgkmcnt(4)\n"
"buffer_load_dwordx4 v247, s[72:75], s23 offen offset:1024 lds\n"
"MA v,48,a,224,v,192\n"
"TR 200,244,8\n"
"MA v,48,a,228,v,32\n"
"s_add_u32 m0, s1, 8192\n"
"TR 202,244,2056\n"
"TR 204,244,4104\n"
"MA v,52,a,228,v,36\n"
"TR 206,244,6152\n"
"MA v,52,a,224,v,196\n"
"MA v,56,a,224,v,200\n"
"buffer_load_dwordx4 v250, s[72:75], s23 offen lds\n"
"MA v,56,a,228,v,40\n"
"MA v,60,a,228,v,44\n"
"buffer_load_dwordx4 a[60:63], v252, s[16:19], s21 offen offset:3072 sc1\n"
"MA v,60,a,224,v,204\n"
"MA v,220,a,232,v,204\n"
"TR 208,245,0\n"
"MA v,220,a,236,v,44\n"
"buffer_load_dwordx4 v251, s[72:75], s23 offen offset:1024 lds\n"
"MA v,216,a,236,v,40\n"
"MA v,216,a,232,v,200\n"
"TR 210,245,2048\n"
"MA v,212,a,232,v,196\n"
"TR 212,245,4096\n"
"TR 214,245,6144\n"
"v_mul_f32 v48, v48, v236\n"
"MA v,212,a,236,v,36\n"
"v_mul_f32 v49, v49, v236\n"
"v_mul_f32 v50, v50, v236\n"
"MA v,208,a,236,v,32\n"
"v_mul_f32 v51, v51, v236\n"
"v_cvt_pk_bf16_f32 v224, v48, v49\n"
"MA v,208,a,232,v,192\n"
"v_cvt_pk_bf16_f32 v225, v50, v51\n"
"v_mul_f32 v52, v52, v237\n"
"s_bitcmp1_b32 s54, 17\n"
"s_cbranch_scc1 L_IDLE%=\n"
"s_waitcnt vmcnt(25) lgkmcnt(4)\n"
"MZ v,64,a,192,a,0\n"
"s_waitcnt vmcnt(19)\n"
"buffer_load_dwordx4 a[64:67], v249, s[16:19], s22 offen sc1\n"
"MA v,64,a,196,a,16\n"
"s_waitcnt vmcnt(18)\n"
"MZ v,72,a,196,a,20\n"
"TR 216,245,8\n"
"MA v,72,a,192,a,4\n"
"TR 218,245,2056\n"
"TR 220,245,4104\n"
"TR 222,245,6152\n"
"MZ v,80,a,192,a,8\n"
"s_waitcnt vmcnt(16)\n"
"buffer_load_dwordx4 a[68:71], v249, s[16:19], s22 offen offset:1024 sc1\n"
"MA v,80,a,196,a,24\n"
"s_waitcnt vmcnt(16)\n"
"MZ v,88,a,196,a,28\n"
"v_mul_f32 v208, v208, v236\n"
"MA v,88,a,192,a,12\n"
"v_mul_f32 v209, v209, v236\n"
"v_mul_f32 v210, v210, v236\n"
"v_mul_f32 v211, v211, v236\n"
"MZ v,92,a,200,a,12\n"
"buffer_load_dwordx4 a[72:75], v249, s[16:19], s22 offen offset:2048 sc1\n"
"MA v,92,a,204,a,28\n"
"s_add_u32 s13, s47, 20480\n"
"MZ v,84,a,204,a,24\n"
"v_cvt_pk_bf16_f32 v226, v208, v209\n"
"MA v,84,a,200,a,8\n"
"TR 224,244,8192\n"
"TR 226,244,10240\n"
"v_cvt_pk_bf16_f32 v227, v210, v211\n"
"MZ v,76,a,200,a,4\n"
"buffer_load_dwordx4 a[76:79], v249, s[16:19], s22 offen offset:3072 sc1\n"
"MA v,76,a,204,a,20\n"
"MZ v,68,a,204,a,16\n"
"TR 228,244,12288\n"
"MA v,68,a,200,a,0\n"
"s_waitcnt lgkmcnt(3)\n"
"buffer_store_dwordx4 v[224:227], v249, s[16:19], s13 offen sc1\n"
"MZ v,96,a,208,a,0\n"
"MA v,96,a,212,a,16\n"
"buffer_load_dwordx4 a[80:83], v252, s[16:19], s22 offen sc1\n"
"MZ v,104,a,212,a,20\n"
"MA v,104,a,208,a,4\n"
"TR 230,244,14336\n"
"TR 232,244,8200\n"
"MZ v,112,a,208,a,8\n"
"s_add_u32 s0, s0, 0x20000000\n"
"s_cselect_b32 s2, s84, s77\n"
"buffer_load_dwordx4 a[84:87], v252, s[16:19], s22 offen offset:1024 sc1\n"
"MA v,112,a,212,a,24\n"
"s_add_u32 s46, s46, 0x20000000\n"
"s_cselect_b32 s49, s48, 0\n"
"MZ v,120,a,212,a,28\n"
"s_add_u32 s2, s2, s49\n"
"TR 234,244,10248\n"
"MA v,120,a,208,a,12\n"
"TR 236,244,12296\n"
"TR 238,244,14344\n"
"v_mul_f32 v53, v53, v237\n"
"MZ v,124,a,216,a,12\n"
"buffer_load_dwordx4 a[88:91], v252, s[16:19], s22 offen offset:2048 sc1\n"
"MA v,124,a,220,a,28\n"
"s_add_u32 s21, s20, s2\n"
"MZ v,116,a,220,a,24\n"
"s_mul_i32 s2, s2, 10\n"
"v_mul_f32 v54, v54, v237\n"
"v_mul_f32 v55, v55, v237\n"
"MA v,116,a,216,a,8\n"
"s_add_u32 s4, s23, s2\n"
"v_mul_f32 v212, v212, v237\n"
"v_mul_f32 v213, v213, v237\n"
"MZ v,108,a,216,a,4\n"
"buffer_load_dwordx4 a[92:95], v252, s[16:19], s22 offen offset:3072 sc1\n"
"MA v,108,a,220,a,20\n"
"MZ v,100,a,220,a,16\n"
"v_mul_f32 v214, v214, v237\n"
"v_mul_f32 v215, v215, v237\n"
"MA v,100,a,216,a,0\n"
"s_waitcnt lgkmcnt(0)\n"
"v_cvt_pk_bf16_f32 v228, v52, v53\n"
"v_cvt_pk_bf16_f32 v229, v54, v55\n"
"MZ v,128,a,224,a,0\n"
"v_cvt_pk_bf16_f32 v230, v212, v213\n"
"v_cvt_pk_bf16_f32 v231, v214, v215\n"
"MA v,128,a,228,a,16\n"
"buffer_store_dwordx4 v[228:231], v249, s[16:19], s13 offen offset:1024 sc1\n"
"MZ v,136,a,228,a,20\n"
"MA v,136,a,224,a,4\n"
"TR 192,245,8192\n"
"TR 194,245,10240\n"
"MZ v,144,a,224,a,8\n"
"TR 196,245,12288\n"
"TR 198,245,14336\n"
"MA v,144,a,228,a,24\n"
"TR 200,245,8200\n"
"TR 202,245,10248\n"
"MZ v,152,a,228,a,28\n"
"v_mul_f32 v56, v56, v238\n"
"v_mul_f32 v57, v57, v238\n"
"v_mul_f32 v58, v58, v238\n"
"MA v,152,a,224,a,12\n"
"s_add_u32 m0, s1, 16384\n"
"v_mul_f32 v59, v59, v238\n"
"MZ v,156,a,232,a,12\n"
"s_barrier\n"
"v_mul_f32 v216, v216, v238\n"
"MA v,156,a,236,a,28\n"
"v_mul_f32 v217, v217, v238\n"
"v_mul_f32 v218, v218, v238\n"
"v_mul_f32 v219, v219, v238\n"
"MZ v,148,a,236,a,24\n"
"buffer_load_dwordx4 v246, s[72:75], s4 offen lds\n"
"MA v,148,a,232,a,8\n"
"MZ v,140,a,232,a,4\n"
"TR 204,245,12296\n"
"TR 206,245,14344\n"
"MA v,140,a,236,a,20\n"
"v_cvt_pk_bf16_f32 v224, v56, v57\n"
"v_cvt_pk_bf16_f32 v225, v58, v59\n"
"MZ v,132,a,236,a,16\n"
"v_cvt_pk_bf16_f32 v226, v216, v217\n"
"v_cvt_pk_bf16_f32 v227, v218, v219\n"
"MA v,132,a,232,a,0\n"
"s_waitcnt lgkmcnt(2)\n"
"buffer_load_dwordx4 v247, s[72:75], s4 offen offset:1024 lds\n"
"MZ v,160,a,192,a,0\n"
"MA v,160,a,196,a,16\n"
"s_add_u32 m0, s1, 24576\n"
"buffer_store_dwordx4 v[224:227], v249, s[16:19], s13 offen offset:2048 sc1\n"
"MZ v,168,a,196,a,20\n"
"MA v,168,a,192,a,4\n"
"TR 208,244,16384\n"
"TR 210,244,18432\n"
"MZ v,176,a,192,a,8\n"
"buffer_load_dwordx4 v250, s[72:75], s4 offen lds\n"
"MA v,176,a,196,a,24\n"
"MZ v,184,a,196,a,28\n"
"TR 212,244,20480\n"
"TR 214,244,22528\n"
"MA v,184,a,192,a,12\n"
"v_mul_f32 v60, v60, v239\n"
"v_mul_f32 v61, v61, v239\n"
"MZ v,188,a,200,a,12\n"
"s_waitcnt lgkmcnt(4)\n"
"v_mul_f32 v62, v62, v239\n"
"v_mul_f32 v63, v63, v239\n"
"v_mul_f32 v220, v220, v239\n"
"MA v,188,a,204,a,28\n"
"buffer_load_dwordx4 v251, s[72:75], s4 offen offset:1024 lds\n"
"MZ v,180,a,204,a,24\n"
"MA v,180,a,200,a,8\n"
"v_mul_f32 v221, v221, v239\n"
"MZ v,172,a,200,a,4\n"
"v_cvt_pk_bf16_f32 v228, v60, v61\n"
"v_mul_f32 v222, v222, v239\n"
"v_mul_f32 v223, v223, v239\n"
"MA v,172,a,204,a,20\n"
"v_cvt_pk_bf16_f32 v229, v62, v63\n"
"v_cvt_pk_bf16_f32 v230, v220, v221\n"
"MZ v,164,a,204,a,16\n"
"TR 216,244,16392\n"
"TR 218,244,18440\n"
"v_cvt_pk_bf16_f32 v231, v222, v223\n"
"MA v,164,a,200,a,0\n"
"s_waitcnt vmcnt(27) lgkmcnt(2)\n"
"buffer_store_dwordx4 v[228:231], v249, s[16:19], s13 offen offset:3072 sc1\n"
"MA v,64,a,208,a,32\n"
"s_waitcnt vmcnt(24)\n"
"TR 220,244,20488\n"
"MA v,64,a,212,a,48\n"
"s_waitcnt vmcnt(22)\n"
"TR 222,244,22536\n"
"TR 224,245,16384\n"
"MA v,72,a,212,a,52\n"
"buffer_load_dwordx4 a[0:3], v249, s[16:19], s20 offen sc1\n"
"MA v,72,a,208,a,36\n"
"TR 226,245,18432\n"
"MA v,80,a,208,a,40\n"
"s_waitcnt vmcnt(21)\n"
"buffer_load_dwordx4 a[4:7], v249, s[16:19], s20 offen offset:1024 sc1\n"
"MA v,80,a,212,a,56\n"
"s_waitcnt vmcnt(19)\n"
"TR 228,245,20480\n"
"MA v,88,a,212,a,60\n"
"TR 230,245,22528\n"
"TR 232,245,16392\n"
"MA v,88,a,208,a,44\n"
"s_waitcnt lgkmcnt(7)\n"
"TR 234,245,18440\n"
"TR 236,245,20488\n"
"MA v,92,a,216,a,44\n"
"s_waitcnt lgkmcnt(7)\n"
"buffer_load_dwordx4 a[8:11], v249, s[16:19], s20 offen offset:2048 sc1\n"
"MA v,92,a,220,a,60\n"
"TR 238,245,22536\n"
"MA v,84,a,220,a,56\n"
"TR 192,244,24576\n"
"TR 194,244,26624\n"
"MA v,84,a,216,a,40\n"
"TR 196,244,28672\n"
"TR 198,244,30720\n"
"MA v,76,a,216,a,36\n"
"buffer_load_dwordx4 a[12:15], v249, s[16:19], s20 offen offset:3072 sc1\n"
"MA v,76,a,220,a,52\n"
"MA v,68,a,220,a,48\n"
"MA v,68,a,216,a,32\n"
"s_waitcnt lgkmcnt(5)\n"
"MA v,96,a,224,a,32\n"
"buffer_load_dwordx4 a[16:19], v252, s[16:19], s20 offen sc1\n"
"MA v,96,a,228,a,48\n"
"TR 200,244,24584\n"
"MA v,104,a,228,a,52\n"
"TR 202,244,26632\n"
"TR 204,244,28680\n"
"MA v,104,a,224,a,36\n"
"TR 206,244,30728\n"
"MA v,112,a,224,a,40\n"
"s_add_u32 s0, s0, 0x20000000\n"
"s_cselect_b32 s2, s84, s77\n"
"buffer_load_dwordx4 a[20:23], v252, s[16:19], s20 offen offset:1024 sc1\n"
"MA v,112,a,228,a,56\n"
"s_add_u32 s46, s46, 0x20000000\n"
"s_cselect_b32 s49, s48, 0\n"
"MA v,120,a,228,a,60\n"
"s_add_u32 s2, s2, s49\n"
"MA v,120,a,224,a,44\n"
"s_add_u32 s22, s21, s2\n"
"MA v,124,a,232,a,44\n"
"s_waitcnt lgkmcnt(4)\n"
"buffer_load_dwordx4 a[24:27], v252, s[16:19], s20 offen offset:2048 sc1\n"
"MA v,124,a,236,a,60\n"
"s_mul_i32 s2, s2, 10\n"
"TR 208,245,24576\n"
"MA v,116,a,236,a,56\n"
"s_add_u32 s5, s4, s2\n"
"TR 210,245,26624\n"
"TR 212,245,28672\n"
"MA v,116,a,232,a,40\n"
"TR 214,245,30720\n"
"MA v,108,a,232,a,36\n"
"buffer_load_dwordx4 a[28:31], v252, s[16:19], s20 offen offset:3072 sc1\n"
"MA v,108,a,236,a,52\n"
"MA v,100,a,236,a,48\n"
"MA v,100,a,232,a,32\n"
"MA v,128,a,192,a,32\n"
"TR 216,245,24584\n"
"TR 218,245,26632\n"
"MA v,128,a,196,a,48\n"
"TR 220,245,28680\n"
"TR 222,245,30728\n"
"MA v,136,a,196,a,52\n"
"MA v,136,a,192,a,36\n"
"MA v,144,a,192,a,40\n"
"MA v,144,a,196,a,56\n"
"MA v,152,a,196,a,60\n"
"MA v,152,a,192,a,44\n"
"s_waitcnt lgkmcnt(4)\n"
"s_add_u32 m0, s1, 32768\n"
"MA v,156,a,200,a,44\n"
"s_barrier\n"
"TR 224,244,32768\n"
"MA v,156,a,204,a,60\n"
"TR 226,244,34816\n"
"TR 228,244,36864\n"
"MA v,148,a,204,a,56\n"
"buffer_load_dwordx4 v246, s[72:75], s5 offen lds\n"
"MA v,148,a,200,a,40\n"
"TR 230,244,38912\n"
"MA v,140,a,200,a,36\n"
"MA v,140,a,204,a,52\n"
"MA v,132,a,204,a,48\n"
"MA v,132,a,200,a,32\n"
"buffer_load_dwordx4 v247, s[72:75], s5 offen offset:1024 lds\n"
"MA v,160,a,208,a,32\n"
"TR 232,244,32776\n"
"MA v,160,a,212,a,48\n"
"s_add_u32 m0, s1, 40960\n"
"TR 234,244,34824\n"
"TR 236,244,36872\n"
"MA v,168,a,212,a,52\n"
"TR 238,244,38920\n"
"MA v,168,a,208,a,36\n"
"MA v,176,a,208,a,40\n"
"buffer_load_dwordx4 v250, s[72:75], s5 offen lds\n"
"MA v,176,a,212,a,56\n"
"MA v,184,a,212,a,60\n"
"MA v,184,a,208,a,44\n"
"s_waitcnt lgkmcnt(4)\n"
"MA v,188,a,216,a,44\n"
"TR 192,245,32768\n"
"TR 194,245,34816\n"
"MA v,188,a,220,a,60\n"
"buffer_load_dwordx4 v251, s[72:75], s5 offen offset:1024 lds\n"
"MA v,180,a,220,a,56\n"
"TR 196,245,36864\n"
"MA v,180,a,216,a,40\n"
"TR 198,245,38912\n"
"MA v,172,a,216,a,36\n"
"MA v,172,a,220,a,52\n"
"MA v,164,a,220,a,48\n"
"MA v,164,a,216,a,32\n"
"s_waitcnt vmcnt(27)\n"
"MA v,64,a,224,a,64\n"
"s_waitcnt vmcnt(22)\n"
"buffer_load_dwordx4 a[32:35], v249, s[16:19], s21 offen sc1\n"
"MA v,64,a,228,a,80\n"
"s_waitcnt vmcnt(22)\n"
"TR 200,245,32776\n"
"MA v,72,a,228,a,84\n"
"TR 202,245,34824\n"
"TR 204,245,36872\n"
"MA v,72,a,224,a,68\n"
"TR 206,245,38920\n"
"MA v,80,a,224,a,72\n"
"s_waitcnt vmcnt(21)\n"
"buffer_load_dwordx4 a[36:39], v249, s[16:19], s21 offen offset:1024 sc1\n"
"MA v,80,a,228,a,88\n"
"s_waitcnt vmcnt(21)\n"
"MA v,88,a,228,a,92\n"
"MA v,88,a,224,a,76\n"
"s_waitcnt lgkmcnt(4)\n"
"MA v,92,a,232,a,76\n"
"buffer_load_dwordx4 a[40:43], v249, s[16:19], s21 offen offset:2048 sc1\n"
"MA v,92,a,236,a,92\n"
"TR 208,244,40960\n"
"MA v,84,a,236,a,88\n"
"TR 210,244,43008\n"
"TR 212,244,45056\n"
"MA v,84,a,232,a,72\n"
"TR 214,244,47104\n"
"MA v,76,a,232,a,68\n"
"buffer_load_dwordx4 a[44:47], v249, s[16:19], s21 offen offset:3072 sc1\n"
"MA v,76,a,236,a,84\n"
"MA v,68,a,236,a,80\n"
"MA v,68,a,232,a,64\n"
"MA v,96,a,192,a,64\n"
"buffer_load_dwordx4 a[48:51], v252, s[16:19], s21 offen sc1\n"
"MA v,96,a,196,a,80\n"
"TR 216,244,40968\n"
"v_bfe_u32 v16, v249, 6, 2\n"
"v_lshrrev_b32 v17, 8, v249\n"
"v_cmp_eq_u32 vcc, v16, v17\n"
"v_and_b32 v16, 16, v249\n"
"v_mov_b32 v17, 0x3f80\n"
"v_lshlrev_b32 v17, v16, v17\n"
"v_and_b32 v16, 32, v249\n"
"v_cndmask_b32 v19, 0, v17, vcc\n"
"v_cmp_eq_u32 vcc, 0, v16\n"
"s_nop 1\n"
"v_cndmask_b32 v18, 0, v19, vcc\n"
"s_mov_b32 s7, 6\n"
"L_LOOPB_%=:\n"
"MA v,104,a,196,a,84\n"
"TR 218,244,43016\n"
"TR 220,244,45064\n"
"MA v,104,a,192,a,68\n"
"TR 222,244,47112\n"
"v_xor_b32 v244, 0xc000, v244\n"
"MA v,112,a,192,a,72\n"
"s_add_u32 s0, s0, 0x20000000\n"
"s_cselect_b32 s2, s84, s77\n"
"buffer_load_dwordx4 a[52:55], v252, s[16:19], s21 offen offset:1024 sc1\n"
"MA v,112,a,196,a,88\n"
"s_add_u32 s46, s46, 0x20000000\n"
"s_cselect_b32 s49, s48, 0\n"
"MA v,120,a,196,a,92\n"
"s_add_u32 s2, s2, s49\n"
"MA v,120,a,192,a,76\n"
"s_waitcnt lgkmcnt(4)\n"
"s_add_u32 s20, s22, s2\n"
"MA v,124,a,200,a,76\n"
"buffer_load_dwordx4 a[56:59], v252, s[16:19], s21 offen offset:2048 sc1\n"
"MA v,124,a,204,a,92\n"
"s_mul_i32 s2, s2, 10\n"
"TR 224,245,40960\n"
"MA v,116,a,204,a,88\n"
"s_add_u32 s23, s5, s2\n"
"TR 226,245,43008\n"
"TR 228,245,45056\n"
"MA v,116,a,200,a,72\n"
"TR 230,245,47104\n"
"MA v,108,a,200,a,68\n"
"buffer_load_dwordx4 a[60:63], v252, s[16:19], s21 offen offset:3072 sc1\n"
"MA v,108,a,204,a,84\n"
"MA v,100,a,204,a,80\n"
"MA v,100,a,200,a,64\n"
"MA v,128,a,208,a,64\n"
"TR 232,245,40968\n"
"TR 234,245,43016\n"
"MA v,128,a,212,a,80\n"
"TR 236,245,45064\n"
"TR 238,245,47112\n"
"MA v,136,a,212,a,84\n"
"v_xor_b32 v245, 0xc000, v245\n"
"MA v,136,a,208,a,68\n"
"MA v,144,a,208,a,72\n"
"s_xor_b32 s1, s1, 49152\n"
"MA v,144,a,212,a,88\n"
"MA v,152,a,212,a,92\n"
"MA v,152,a,208,a,76\n"
"s_waitcnt lgkmcnt(4)\n"
"s_add_u32 m0, s1, 0\n"
"MA v,156,a,216,a,76\n"
"s_barrier\n"
"TR 192,244,0\n"
"MA v,156,a,220,a,92\n"
"TR 194,244,2048\n"
"TR 196,244,4096\n"
"MA v,148,a,220,a,88\n"
"buffer_load_dwordx4 v246, s[72:75], s23 offen lds\n"
"MA v,148,a,216,a,72\n"
"TR 198,244,6144\n"
"MA v,140,a,216,a,68\n"
"s_cmp_eq_u32 s7, 6\n"
"s_cselect_b32 s8, s60, s55\n"
"MA v,140,a,220,a,84\n"
"MA v,132,a,220,a,80\n"
"MA v,132,a,216,a,64\n"
"buffer_load_dwordx4 v247, s[72:75], s23 offen offset:1024 lds\n"
"MA v,160,a,224,a,64\n"
"TR 200,244,8\n"
"MA v,160,a,228,a,80\n"
"s_add_u32 m0, s1, 8192\n"
"TR 202,244,2056\n"
"TR 204,244,4104\n"
"MA v,168,a,228,a,84\n"
"TR 206,244,6152\n"
"MA v,168,a,224,a,68\n"
"MA v,176,a,224,a,72\n"
"buffer_load_dwordx4 v250, s[72:75], s23 offen lds\n"
"MA v,176,a,228,a,88\n"
"MA v,184,a,228,a,92\n"
"MA v,184,a,224,a,76\n"
"s_waitcnt lgkmcnt(4)\n"
"MA v,188,a,232,a,76\n"
"TR 208,245,0\n"
"TR 210,245,2048\n"
"MA v,188,a,236,a,92\n"
"buffer_load_dwordx4 v251, s[72:75], s23 offen offset:1024 lds\n"
"MA v,180,a,236,a,88\n"
"TR 212,245,4096\n"
"MA v,180,a,232,a,72\n"
"TR 214,245,6144\n"
"MA v,172,a,232,a,68\n"
"MA v,172,a,236,a,84\n"
"MA v,164,a,236,a,80\n"
"s_add_u32 s9, s56, 4\n"
"v_mov_b32 v23, s57\n"
"MA v,164,a,232,a,64\n"
"s_waitcnt vmcnt(23)\n"
"v_mov_b32 v22, s9\n"
"MA v,64,a,192,a,0\n"
"s_waitcnt vmcnt(19)\n"
"buffer_load_dwordx4 a[64:67], v249, s[16:19], s22 offen sc1\n"
"MA v,64,a,196,a,16\n"
"s_waitcnt vmcnt(19)\n"
"TR 216,245,8\n"
"MA v,72,a,196,a,20\n"
"TR 218,245,2056\n"
"TR 220,245,4104\n"
"MA v,72,a,192,a,4\n"
"TR 222,245,6152\n"
"MA v,80,a,192,a,8\n"
"s_waitcnt vmcnt(18)\n"
"buffer_load_dwordx4 a[68:71], v249, s[16:19], s22 offen offset:1024 sc1\n"
"MA v,80,a,196,a,24\n"
"s_waitcnt vmcnt(18)\n"
"s_bitcmp0_b32 s7, 0\n"
"s_cselect_b32 s10, s61, s55\n"
"s_cselect_b32 s6, s64, 0\n"
"MA v,88,a,196,a,28\n"
"buffer_store_dwordx2 v[22:23], v234, s[16:19], s8 offen sc1\n"
"MA v,88,a,192,a,12\n"
"s_waitcnt lgkmcnt(4)\n"
"MA v,92,a,200,a,12\n"
"buffer_load_dwordx4 a[72:75], v249, s[16:19], s22 offen offset:2048 sc1\n"
"MA v,92,a,204,a,28\n"
"TR 224,244,8192\n"
"MA v,84,a,204,a,24\n"
"TR 226,244,10240\n"
"TR 228,244,12288\n"
"MA v,84,a,200,a,8\n"
"TR 230,244,14336\n"
"MA v,76,a,200,a,4\n"
"buffer_load_dwordx4 a[76:79], v249, s[16:19], s22 offen offset:3072 sc1\n"
"MA v,76,a,204,a,20\n"
"MA v,68,a,204,a,16\n"
"MA v,68,a,200,a,0\n"
"MA v,96,a,208,a,0\n"
"buffer_load_dwordx4 a[80:83], v252, s[16:19], s22 offen sc1\n"
"MA v,96,a,212,a,16\n"
"TR 232,244,8200\n"
"MA v,104,a,212,a,20\n"
"TR 234,244,10248\n"
"TR 236,244,12296\n"
"MA v,104,a,208,a,4\n"
"TR 238,244,14344\n"
"MA v,112,a,208,a,8\n"
"s_add_u32 s0, s0, 0x20000000\n"
"s_cselect_b32 s2, s84, s77\n"
"buffer_load_dwordx4 a[84:87], v252, s[16:19], s22 offen offset:1024 sc1\n"
"MA v,112,a,212,a,24\n"
"s_add_u32 s46, s46, 0x20000000\n"
"s_cselect_b32 s49, s48, 0\n"
"MA v,120,a,212,a,28\n"
"s_add_u32 s2, s2, s49\n"
"buffer_load_dwordx2 v[254:255], v233, s[16:19], s10 offen sc1\n"
"MA v,120,a,208,a,12\n"
"s_waitcnt lgkmcnt(4)\n"
"s_add_u32 s21, s20, s2\n"
"MA v,124,a,216,a,12\n"
"buffer_load_dwordx4 a[88:91], v252, s[16:19], s22 offen offset:2048 sc1\n"
"MA v,124,a,220,a,28\n"
"s_mul_i32 s2, s2, 10\n"
"TR 192,245,8192\n"
"MA v,116,a,220,a,24\n"
"s_add_u32 s4, s23, s2\n"
"TR 194,245,10240\n"
"TR 196,245,12288\n"
"MA v,116,a,216,a,8\n"
"TR 198,245,14336\n"
"MA v,108,a,216,a,4\n"
"buffer_load_dwordx4 a[92:95], v252, s[16:19], s22 offen offset:3072 sc1\n"
"MA v,108,a,220,a,20\n"
"MA v,100,a,220,a,16\n"
"MA v,100,a,216,a,0\n"
"MA v,128,a,224,a,0\n"
"TR 200,245,8200\n"
"TR 202,245,10248\n"
"MA v,128,a,228,a,16\n"
"TR 204,245,12296\n"
"TR 206,245,14344\n"
"MA v,136,a,228,a,20\n"
"MA v,136,a,224,a,4\n"
"MA v,144,a,224,a,8\n"
"MA v,144,a,228,a,24\n"
"MA v,152,a,228,a,28\n"
"MA v,152,a,224,a,12\n"
"s_waitcnt lgkmcnt(4)\n"
"s_add_u32 m0, s1, 16384\n"
"MA v,156,a,232,a,12\n"
"s_barrier\n"
"TR 208,244,16384\n"
"MA v,156,a,236,a,28\n"
"TR 210,244,18432\n"
"TR 212,244,20480\n"
"MA v,148,a,236,a,24\n"
"buffer_load_dwordx4 v246, s[72:75], s4 offen lds\n"
"MA v,148,a,232,a,8\n"
"TR 214,244,22528\n"
"MA v,140,a,232,a,4\n"
"MA v,140,a,236,a,20\n"
"MA v,132,a,236,a,16\n"
"MA v,132,a,232,a,0\n"
"buffer_load_dwordx4 v247, s[72:75], s4 offen offset:1024 lds\n"
"MA v,160,a,192,a,0\n"
"TR 216,244,16392\n"
"MA v,160,a,196,a,16\n"
"s_add_u32 m0, s1, 24576\n"
"TR 218,244,18440\n"
"TR 220,244,20488\n"
"MA v,168,a,196,a,20\n"
"TR 222,244,22536\n"
"MA v,168,a,192,a,4\n"
"MA v,176,a,192,a,8\n"
"buffer_load_dwordx4 v250, s[72:75], s4 offen lds\n"
"MA v,176,a,196,a,24\n"
"MA v,184,a,196,a,28\n"
"MA v,184,a,192,a,12\n"
"s_waitcnt lgkmcnt(4)\n"
"MA v,188,a,200,a,12\n"
"TR 224,245,16384\n"
"TR 226,245,18432\n"
"MA v,188,a,204,a,28\n"
"buffer_load_dwordx4 v251, s[72:75], s4 offen offset:1024 lds\n"
"MA v,180,a,204,a,24\n"
"TR 228,245,20480\n"
"MA v,180,a,200,a,8\n"
"TR 230,245,22528\n"
"MA v,172,a,200,a,4\n"
"MA v,172,a,204,a,20\n"
"MA v,164,a,204,a,16\n"
"MA v,164,a,200,a,0\n"
"s_waitcnt vmcnt(25)\n"
"MA v,64,a,208,a,32\n"
"s_waitcnt vmcnt(21)\n"
"buffer_load_dwordx4 a[0:3], v249, s[16:19], s20 offen sc1\n"
"MA v,64,a,212,a,48\n"
"s_waitcnt vmcnt(21)\n"
"TR 232,245,16392\n"
"MA v,72,a,212,a,52\n"
"TR 234,245,18440\n"
"TR 236,245,20488\n"
"MA v,72,a,208,a,36\n"
"TR 238,245,22536\n"
"MA v,80,a,208,a,40\n"
"s_waitcnt vmcnt(20)\n"
"buffer_load_dwordx4 a[4:7], v249, s[16:19], s20 offen offset:1024 sc1\n"
"MA v,80,a,212,a,56\n"
"s_waitcnt vmcnt(20)\n"
"MA v,88,a,212,a,60\n"
"MA v,88,a,208,a,44\n"
"s_waitcnt lgkmcnt(4)\n"
"MA v,92,a,216,a,44\n"
"buffer_load_dwordx4 a[8:11], v249, s[16:19], s20 offen offset:2048 sc1\n"
"MA v,92,a,220,a,60\n"
"TR 192,244,24576\n"
"MA v,84,a,220,a,56\n"
"TR 194,244,26624\n"
"TR 196,244,28672\n"
"MA v,84,a,216,a,40\n"
"TR 198,244,30720\n"
"MA v,76,a,216,a,36\n"
"buffer_load_dwordx4 a[12:15], v249, s[16:19], s20 offen offset:3072 sc1\n"
"MA v,76,a,220,a,52\n"
"MA v,68,a,220,a,48\n"
"MA v,68,a,216,a,32\n"
"MA v,96,a,224,a,32\n"
"buffer_load_dwordx4 a[16:19], v252, s[16:19], s20 offen sc1\n"
"MA v,96,a,228,a,48\n"
"TR 200,244,24584\n"
"MA v,104,a,228,a,52\n"
"TR 202,244,26632\n"
"TR 204,244,28680\n"
"MA v,104,a,224,a,36\n"
"TR 206,244,30728\n"
"MA v,112,a,224,a,40\n"
"s_add_u32 s0, s0, 0x20000000\n"
"s_cselect_b32 s2, s84, s77\n"
"buffer_load_dwordx4 a[20:23], v252, s[16:19], s20 offen offset:1024 sc1\n"
"MA v,112,a,228,a,56\n"
"s_add_u32 s46, s46, 0x20000000\n"
"s_cselect_b32 s49, s48, 0\n"
"MA v,120,a,228,a,60\n"
"s_add_u32 s2, s2, s49\n"
"MA v,120,a,224,a,44\n"
"s_waitcnt lgkmcnt(4)\n"
"s_add_u32 s22, s21, s2\n"
"MA v,124,a,232,a,44\n"
"buffer_load_dwordx4 a[24:27], v252, s[16:19], s20 offen offset:2048 sc1\n"
"MA v,124,a,236,a,60\n"
"s_mul_i32 s2, s2, 10\n"
"TR 208,245,24576\n"
"MA v,116,a,236,a,56\n"
"s_add_u32 s5, s4, s2\n"
"TR 210,245,26624\n"
"TR 212,245,28672\n"
"MA v,116,a,232,a,40\n"
"TR 214,245,30720\n"
"MA v,108,a,232,a,36\n"
"buffer_load_dwordx4 a[28:31], v252, s[16:19], s20 offen offset:3072 sc1\n"
"MA v,108,a,236,a,52\n"
"MA v,100,a,236,a,48\n"
"MA v,100,a,232,a,32\n"
"MA v,128,a,192,a,32\n"
"TR 216,245,24584\n"
"TR 218,245,26632\n"
"MA v,128,a,196,a,48\n"
"TR 220,245,28680\n"
"TR 222,245,30728\n"
"MA v,136,a,196,a,52\n"
"MA v,136,a,192,a,36\n"
"MA v,144,a,192,a,40\n"
"MA v,144,a,196,a,56\n"
"MA v,152,a,196,a,60\n"
"MA v,152,a,192,a,44\n"
"s_waitcnt lgkmcnt(4)\n"
"s_add_u32 m0, s1, 32768\n"
"MA v,156,a,200,a,44\n"
"s_barrier\n"
"TR 224,244,32768\n"
"MA v,156,a,204,a,60\n"
"TR 226,244,34816\n"
"TR 228,244,36864\n"
"MA v,148,a,204,a,56\n"
"buffer_load_dwordx4 v246, s[72:75], s5 offen lds\n"
"MA v,148,a,200,a,40\n"
"TR 230,244,38912\n"
"MA v,140,a,200,a,36\n"
"MA v,140,a,204,a,52\n"
"MA v,132,a,204,a,48\n"
"MA v,132,a,200,a,32\n"
"buffer_load_dwordx4 v247, s[72:75], s5 offen offset:1024 lds\n"
"MA v,160,a,208,a,32\n"
"TR 232,244,32776\n"
"MA v,160,a,212,a,48\n"
"s_add_u32 m0, s1, 40960\n"
"TR 234,244,34824\n"
"TR 236,244,36872\n"
"MA v,168,a,212,a,52\n"
"TR 238,244,38920\n"
"MA v,168,a,208,a,36\n"
"MA v,176,a,208,a,40\n"
"buffer_load_dwordx4 v250, s[72:75], s5 offen lds\n"
"MA v,176,a,212,a,56\n"
"MA v,184,a,212,a,60\n"
"MA v,184,a,208,a,44\n"
"s_waitcnt lgkmcnt(4)\n"
"MA v,188,a,216,a,44\n"
"TR 192,245,32768\n"
"TR 194,245,34816\n"
"MA v,188,a,220,a,60\n"
"buffer_load_dwordx4 v251, s[72:75], s5 offen offset:1024 lds\n"
"MA v,180,a,220,a,56\n"
"s_cmp_eq_u32 s7, 6\n"
"s_cselect_b32 s11, 2, 3\n"
"s_mul_i32 s11, s11, s3\n"
"s_add_u32 s11, s11, s56\n"
"s_add_u32 s11, s11, 1\n"
"TR 196,245,36864\n"
"MA v,180,a,216,a,40\n"
"TR 198,245,38912\n"
"MA v,172,a,216,a,36\n"
"MA v,172,a,220,a,52\n"
"MA v,164,a,220,a,48\n"
"MA v,164,a,216,a,32\n"
"s_waitcnt vmcnt(25)\n"
"MA v,64,a,224,a,64\n"
"s_waitcnt vmcnt(20)\n"
"buffer_load_dwordx4 a[32:35], v249, s[16:19], s21 offen sc1\n"
"MA v,64,a,228,a,80\n"
"s_waitcnt vmcnt(19)\n"
"v_subrev_u32 v254, s11, v254\n"
"MA v,72,a,228,a,84\n"
"v_cmp_gt_u32 vcc, 8, v254\n"
"v_cmp_eq_u32 s[94:95], s57, v255\n"
"MA v,72,a,224,a,68\n"
"TR 200,245,32776\n"
"TR 202,245,34824\n"
"MA v,80,a,224,a,72\n"
"s_waitcnt vmcnt(18)\n"
"buffer_load_dwordx4 a[36:39], v249, s[16:19], s21 offen offset:1024 sc1\n"
"MA v,80,a,228,a,88\n"
"s_and_b32 vcc_lo, vcc_lo, s94\n"
"s_andn2_b32 vcc_lo, s6, vcc_lo\n"
"s_cbranch_scc1 L_SLOWB_%=\n"
"L_BACKB_%=:\n"
"s_waitcnt vmcnt(18)\n"
"MA v,88,a,228,a,92\n"
"TR 204,245,36872\n"
"TR 206,245,38920\n"
"MA v,88,a,224,a,76\n"
"s_waitcnt lgkmcnt(4)\n"
"MA v,92,a,232,a,76\n"
"buffer_load_dwordx4 a[40:43], v249, s[16:19], s21 offen offset:2048 sc1\n"
"MA v,92,a,236,a,92\n"
"TR 208,244,40960\n"
"MA v,84,a,236,a,88\n"
"TR 210,244,43008\n"
"TR 212,244,45056\n"
"MA v,84,a,232,a,72\n"
"TR 214,244,47104\n"
"MA v,76,a,232,a,68\n"
"buffer_load_dwordx4 a[44:47], v249, s[16:19], s21 offen offset:3072 sc1\n"
"MA v,76,a,236,a,84\n"
"MA v,68,a,236,a,80\n"
"MA v,68,a,232,a,64\n"
"MA v,96,a,192,a,64\n"
"buffer_load_dwordx4 a[48:51], v252, s[16:19], s21 offen sc1\n"
"MA v,96,a,196,a,80\n"
"TR 216,244,40968\n"
"s_sub_u32 s7, s7, 1\n"
"s_cmp_lg_u32 s7, 0\n"
"s_cbranch_scc1 L_LOOPB_%=\n"
"v_mov_b32 v255, s57\n"
"v_mov_b32 v254, s12\n"
"MA v,104,a,196,a,84\n"
"TR 218,244,43016\n"
"TR 220,244,45064\n"
"MA v,104,a,192,a,68\n"
"TR 222,244,47112\n"
"v_xor_b32 v244, 0xc000, v244\n"
"MA v,112,a,192,a,72\n"
"buffer_load_dwordx4 a[52:55], v252, s[16:19], s21 offen offset:1024 sc1\n"
"MA v,112,a,196,a,88\n"
"MA v,120,a,196,a,92\n"
"MA v,120,a,192,a,76\n"
"s_waitcnt lgkmcnt(4)\n"
"MA v,124,a,200,a,76\n"
"buffer_load_dwordx4 a[56:59], v252, s[16:19], s21 offen offset:2048 sc1\n"
"MA v,124,a,204,a,92\n"
"s_add_u32 s8, s78, 0\n"
"TR 224,245,40960\n"
"MA v,116,a,204,a,88\n"
"s_add_u32 s9, s78, 4096\n"
"TR 226,245,43008\n"
"TR 228,245,45056\n"
"MA v,116,a,200,a,72\n"
"s_add_u32 s10, s78, 8192\n"
"s_add_u32 s11, s78, 12288\n"
"TR 230,245,47104\n"
"MA v,108,a,200,a,68\n"
"buffer_load_dwordx4 a[60:63], v252, s[16:19], s21 offen offset:3072 sc1\n"
"MA v,108,a,204,a,84\n"
"MA v,100,a,204,a,80\n"
"MA v,100,a,200,a,64\n"
"MA v,128,a,208,a,64\n"
"TR 232,245,40968\n"
"TR 234,245,43016\n"
"MA v,128,a,212,a,80\n"
"TR 236,245,45064\n"
"TR 238,245,47112\n"
"MA v,136,a,212,a,84\n"
"buffer_load_dwordx4 a[96:99], v249, s[16:19], s22 offen sc1\n"
"MA v,136,a,208,a,68\n"
"v_add_u32 v48, 2621440, v249\n"
"MA v,144,a,208,a,72\n"
"v_add_u32 v49, 5242880, v249\n"
"v_add_u32 v50, 7864320, v249\n"
"MA v,144,a,212,a,88\n"
"buffer_load_dwordx4 a[100:103], v249, s[16:19], s22 offen offset:1024 sc1\n"
"MA v,152,a,212,a,92\n"
"v_add_u32 v51, 10485760, v249\n"
"MA v,152,a,208,a,76\n"
"s_barrier\n"
"v_xor_b32 v245, 0xc000, v245\n"
"s_waitcnt lgkmcnt(4)\n"
"MA v,156,a,216,a,76\n"
"buffer_load_dwordx4 a[104:107], v249, s[16:19], s22 offen offset:2048 sc1\n"
"MA v,156,a,220,a,92\n"
"TR 192,244,0\n"
"MA v,148,a,220,a,88\n"
"TR 194,244,2048\n"
"TR 196,244,4096\n"
"MA v,148,a,216,a,72\n"
"buffer_load_dwordx4 a[108:111], v249, s[16:19], s22 offen offset:3072 sc1\n"
"MA v,140,a,216,a,68\n"
"TR 198,244,6144\n"
"MA v,140,a,220,a,84\n"
"MA v,132,a,220,a,80\n"
"buffer_load_dwordx4 a[112:115], v252, s[16:19], s22 offen sc1\n"
"MA v,132,a,216,a,64\n"
"MA v,160,a,224,a,64\n"
"TR 200,244,8\n"
"TR 202,244,2056\n"
"MA v,160,a,228,a,80\n"
"buffer_load_dwordx4 a[116:119], v252, s[16:19], s22 offen offset:1024 sc1\n"
"MA v,168,a,228,a,84\n"
"TR 204,244,4104\n"
"MA v,168,a,224,a,68\n"
"TR 206,244,6152\n"
"MA v,176,a,224,a,72\n"
"buffer_load_dwordx4 a[120:123], v252, s[16:19], s22 offen offset:2048 sc1\n"
"MA v,176,a,228,a,88\n"
"MA v,184,a,228,a,92\n"
"MA v,184,a,224,a,76\n"
"s_waitcnt lgkmcnt(4)\n"
"buffer_load_dwordx4 a[124:127], v252, s[16:19], s22 offen offset:3072 sc1\n"
"MA v,188,a,232,a,76\n"
"s_waitcnt vmcnt(28)\n"
"s_barrier\n"
"MA v,188,a,236,a,92\n"
"TR 208,244,16384\n"
"TR 210,244,18432\n"
"MA v,180,a,236,a,88\n"
"TR 212,244,20480\n"
"TR 214,244,22528\n"
"MA v,180,a,232,a,72\n"
"MA v,172,a,232,a,68\n"
"MA v,172,a,236,a,84\n"
"MA v,164,a,236,a,80\n"
"MA v,164,a,232,a,64\n"
"s_waitcnt vmcnt(27)\n"
"MA v,64,a,192,a,0\n"
"s_waitcnt vmcnt(23)\n"
"TR 216,244,16392\n"
"TR 218,244,18440\n"
"MA v,64,a,196,a,16\n"
"s_waitcnt vmcnt(22)\n"
"TR 220,244,20488\n"
"TR 222,244,22536\n"
"MA v,72,a,196,a,20\n"
"MA v,72,a,192,a,4\n"
"MA v,80,a,192,a,8\n"
"s_waitcnt vmcnt(21)\n"
"MA v,80,a,196,a,24\n"
"s_waitcnt vmcnt(20)\n"
"MA v,88,a,196,a,28\n"
"s_mov_b32 s20, 0\n"
"s_lshr_b32 s21, s45, 8\n"
"s_not_b32 s21, s21\n"
"s_add_u32 s46, s56, 1\n"
"MA v,88,a,192,a,12\n"
"s_waitcnt vmcnt(16)\n"
"s_barrier\n"
"s_waitcnt lgkmcnt(4)\n"
"MA v,92,a,200,a,12\n"
"TR 224,244,32768\n"
"TR 226,244,34816\n"
"MA v,92,a,204,a,28\n"
"TR 228,244,36864\n"
"TR 230,244,38912\n"
"MA v,84,a,204,a,24\n"
"MA v,84,a,200,a,8\n"
"MA v,76,a,200,a,4\n"
"MA v,76,a,204,a,20\n"
"MA v,68,a,204,a,16\n"
"MA v,68,a,200,a,0\n"
"s_waitcnt vmcnt(15)\n"
"MA v,64,a,208,a,32\n"
"s_waitcnt vmcnt(11)\n"
"TR 232,244,32776\n"
"TR 234,244,34824\n"
"MA v,64,a,212,a,48\n"
"s_waitcnt vmcnt(10)\n"
"TR 236,244,36872\n"
"TR 238,244,38920\n"
"MA v,72,a,212,a,52\n"
"MA v,72,a,208,a,36\n"
"MA v,80,a,208,a,40\n"
"s_waitcnt vmcnt(9)\n"
"v_sub_u32 v19, v19, v18\n"
"v_mov_b32 v16, 0\n"
"MA v,80,a,212,a,56\n"
"s_waitcnt vmcnt(8)\n"
"v_mov_b32 v17, 0\n"
"v_mov_b32 v20, 0\n"
"MA v,88,a,212,a,60\n"
"v_mov_b32 v21, 0\n"
"MA v,88,a,208,a,44\n"
"s_waitcnt lgkmcnt(4)\n"
"MA v,92,a,216,a,44\n"
"TR 192,245,0\n"
"TR 194,245,2048\n"
"MA v,92,a,220,a,60\n"
"TR 196,245,4096\n"
"TR 198,245,6144\n"
"MA v,84,a,220,a,56\n"
"MA v,84,a,216,a,40\n"
"MA v,76,a,216,a,36\n"
"MA v,76,a,220,a,52\n"
"MA v,68,a,220,a,48\n"
"buffer_load_dwordx4 v[192:195], v253, s[28:31], s80 offen\n"
"MA v,68,a,216,a,32\n"
"s_waitcnt vmcnt(8)\n"
"MA v,64,a,224,a,96\n"
"s_waitcnt vmcnt(4)\n"
"TR 200,245,8\n"
"TR 202,245,2056\n"
"MA v,64,a,228,a,112\n"
"s_waitcnt vmcnt(3)\n"
"TR 204,245,4104\n"
"TR 206,245,6152\n"
"MA v,72,a,228,a,116\n"
"MA v,72,a,224,a,100\n"
"MA v,80,a,224,a,104\n"
"s_waitcnt vmcnt(2)\n"
"s_nop 0\n"
"v_cvt_pk_bf16_f32 v0, v64, v65\n"
"v_cvt_pk_bf16_f32 v1, v66, v67\n"
"MA v,80,a,228,a,120\n"
"s_waitcnt vmcnt(1)\n"
"MA v,88,a,228,a,124\n"
"v_cvt_pk_bf16_f32 v4, v72, v73\n"
"v_cvt_pk_bf16_f32 v5, v74, v75\n"
"MA v,88,a,224,a,108\n"
"s_waitcnt lgkmcnt(4)\n"
"MA v,92,a,232,a,108\n"
"TR 208,245,16384\n"
"TR 210,245,18432\n"
"MA v,92,a,236,a,124\n"
"TR 212,245,20480\n"
"TR 214,245,22528\n"
"MA v,84,a,236,a,120\n"
"buffer_load_dwordx4 v[196:199], v253, s[28:31], s81 offen\n"
"MA v,84,a,232,a,104\n"
"v_cvt_pk_bf16_f32 v8, v80, v81\n"
"MA v,76,a,232,a,100\n"
"v_cvt_pk_bf16_f32 v9, v82, v83\n"
"v_cvt_pk_bf16_f32 v12, v88, v89\n"
"MA v,76,a,236,a,116\n"
"v_cvt_pk_bf16_f32 v13, v90, v91\n"
"v_cvt_pk_bf16_f32 v14, v92, v93\n"
"MA v,68,a,236,a,112\n"
"v_cvt_pk_bf16_f32 v10, v84, v85\n"
"v_cvt_pk_bf16_f32 v11, v86, v87\n"
"MA v,68,a,232,a,96\n"
"buffer_store_dwordx4 v[8:11], v249, s[16:19], s10 offen sc1\n"
"MA v,96,a,192,a,0\n"
"v_cvt_pk_bf16_f32 v6, v76, v77\n"
"MA v,96,a,196,a,16\n"
"TR 216,245,16392\n"
"v_cvt_pk_bf16_f32 v7, v78, v79\n"
"MA v,104,a,196,a,20\n"
"s_nop 0\n"
"v_cvt_pk_bf16_f32 v2, v68, v69\n"
"v_cvt_pk_bf16_f32 v3, v70, v71\n"
"MA v,104,a,192,a,4\n"
"buffer_store_dwordx4 v[0:3], v249, s[16:19], s8 offen sc1\n"
"MA v,112,a,192,a,8\n"
"TR 218,245,18440\n"
"MA v,112,a,196,a,24\n"
"buffer_store_dwordx4 v[4:7], v249, s[16:19], s9 offen sc1\n"
"MA v,120,a,196,a,28\n"
"TR 220,245,20488\n"
"MA v,120,a,192,a,12\n"
"s_waitcnt lgkmcnt(3)\n"
"buffer_load_dwordx4 v[200:203], v253, s[28:31], s82 offen\n"
"MA v,124,a,200,a,12\n"
"TR 222,245,22536\n"
"MA v,124,a,204,a,28\n"
"TR 224,245,32768\n"
"v_cvt_pk_bf16_f32 v15, v94, v95\n"
"MA v,116,a,204,a,24\n"
"buffer_store_dwordx4 v[12:15], v249, s[16:19], s11 offen sc1\n"
"MA v,116,a,200,a,8\n"
"TR 226,245,34816\n"
"MA v,108,a,200,a,4\n"
"TR 228,245,36864\n"
"TR 230,245,38912\n"
"MA v,108,a,204,a,20\n"
"MA v,100,a,204,a,16\n"
"MA v,100,a,200,a,0\n"
"MA v,96,a,208,a,32\n"
"TR 232,245,32776\n"
"TR 234,245,34824\n"
"MA v,96,a,212,a,48\n"
"TR 236,245,36872\n"
"TR 238,245,38920\n"
"MA v,104,a,212,a,52\n"
"MA v,104,a,208,a,36\n"
"MA v,112,a,208,a,40\n"
"MA v,112,a,212,a,56\n"
"MA v,120,a,212,a,60\n"
"MA v,120,a,208,a,44\n"
"s_waitcnt lgkmcnt(4)\n"
"MA v,124,a,216,a,44\n"
"TR 192,244,8192\n"
"TR 194,244,10240\n"
"MA v,124,a,220,a,60\n"
"TR 196,244,12288\n"
"TR 198,244,14336\n"
"MA v,116,a,220,a,56\n"
"MA v,116,a,216,a,40\n"
"MA v,108,a,216,a,36\n"
"MA v,108,a,220,a,52\n"
"MA v,100,a,220,a,48\n"
"MA v,100,a,216,a,32\n"
"MA v,96,a,224,a,96\n"
"TR 200,244,8200\n"
"TR 202,244,10248\n"
"MA v,96,a,228,a,112\n"
"TR 204,244,12296\n"
"TR 206,244,14344\n"
"MA v,104,a,228,a,116\n"
"MA v,104,a,224,a,100\n"
"MA v,112,a,224,a,104\n"
"s_nop 2\n"
"v_cvt_pk_bf16_f32 v0, v96, v97\n"
"v_cvt_pk_bf16_f32 v1, v98, v99\n"
"MA v,112,a,228,a,120\n"
"MA v,120,a,228,a,124\n"
"v_cvt_pk_bf16_f32 v4, v104, v105\n"
"v_cvt_pk_bf16_f32 v5, v106, v107\n"
"MA v,120,a,224,a,108\n"
"s_waitcnt lgkmcnt(4)\n"
"MA v,124,a,232,a,108\n"
"TR 208,244,24576\n"
"TR 210,244,26624\n"
"MA v,124,a,236,a,124\n"
"TR 212,244,28672\n"
"TR 214,244,30720\n"
"MA v,116,a,236,a,120\n"
"v_cvt_pk_bf16_f32 v8, v112, v113\n"
"v_cvt_pk_bf16_f32 v9, v114, v115\n"
"MA v,116,a,232,a,104\n"
"v_cvt_pk_bf16_f32 v12, v120, v121\n"
"v_cvt_pk_bf16_f32 v13, v122, v123\n"
"MA v,108,a,232,a,100\n"
"v_cvt_pk_bf16_f32 v14, v124, v125\n"
"v_cvt_pk_bf16_f32 v15, v126, v127\n"
"MA v,108,a,236,a,116\n"
"buffer_store_dwordx4 v[12:15], v249, s[16:19], s11 offen offset:1024 sc1\n"
"MA v,100,a,236,a,112\n"
"v_cvt_pk_bf16_f32 v10, v116, v117\n"
"MA v,100,a,232,a,96\n"
"v_cvt_pk_bf16_f32 v11, v118, v119\n"
"MA v,128,a,192,a,0\n"
"s_nop 1\n"
"v_cvt_pk_bf16_f32 v6, v108, v109\n"
"v_cvt_pk_bf16_f32 v7, v110, v111\n"
"MA v,128,a,196,a,16\n"
"buffer_store_dwordx4 v[4:7], v249, s[16:19], s9 offen offset:1024 sc1\n"
"MA v,136,a,196,a,20\n"
"v_cvt_pk_bf16_f32 v2, v100, v101\n"
"MA v,136,a,192,a,4\n"
"TR 216,244,24584\n"
"v_cvt_pk_bf16_f32 v3, v102, v103\n"
"MA v,144,a,192,a,8\n"
"buffer_store_dwordx4 v[0:3], v249, s[16:19], s8 offen offset:1024 sc1\n"
"MA v,144,a,196,a,24\n"
"TR 218,244,26632\n"
"MA v,152,a,196,a,28\n"
"buffer_store_dwordx4 v[8:11], v249, s[16:19], s10 offen offset:1024 sc1\n"
"MA v,152,a,192,a,12\n"
"s_waitcnt lgkmcnt(2)\n"
"TR 220,244,28680\n"
"MA v,156,a,200,a,12\n"
"TR 222,244,30728\n"
"TR 224,244,40960\n"
"MA v,156,a,204,a,28\n"
"TR 226,244,43008\n"
"TR 228,244,45056\n"
"MA v,148,a,204,a,24\n"
"TR 230,244,47104\n"
"MA v,148,a,200,a,8\n"
"MA v,140,a,200,a,4\n"
"MA v,140,a,204,a,20\n"
"MA v,132,a,204,a,16\n"
"MA v,132,a,200,a,0\n"
"MA v,128,a,208,a,32\n"
"TR 232,244,40968\n"
"TR 234,244,43016\n"
"MA v,128,a,212,a,48\n"
"TR 236,244,45064\n"
"TR 238,244,47112\n"
"MA v,136,a,212,a,52\n"
"MA v,136,a,208,a,36\n"
"MA v,144,a,208,a,40\n"
"MA v,144,a,212,a,56\n"
"MA v,152,a,212,a,60\n"
"MA v,152,a,208,a,44\n"
"s_waitcnt lgkmcnt(4)\n"
"MA v,156,a,216,a,44\n"
"TR 192,245,8192\n"
"TR 194,245,10240\n"
"MA v,156,a,220,a,60\n"
"TR 196,245,12288\n"
"TR 198,245,14336\n"
"MA v,148,a,220,a,56\n"
"MA v,148,a,216,a,40\n"
"MA v,140,a,216,a,36\n"
"MA v,140,a,220,a,52\n"
"MA v,132,a,220,a,48\n"
"MA v,132,a,216,a,32\n"
"MA v,128,a,224,a,96\n"
"TR 200,245,8200\n"
"TR 202,245,10248\n"
"MA v,128,a,228,a,112\n"
"TR 204,245,12296\n"
"TR 206,245,14344\n"
"MA v,136,a,228,a,116\n"
"MA v,136,a,224,a,100\n"
"MA v,144,a,224,a,104\n"
"s_nop 2\n"
"v_cvt_pk_bf16_f32 v0, v128, v129\n"
"v_cvt_pk_bf16_f32 v1, v130, v131\n"
"MA v,144,a,228,a,120\n"
"MA v,152,a,228,a,124\n"
"v_cvt_pk_bf16_f32 v4, v136, v137\n"
"v_cvt_pk_bf16_f32 v5, v138, v139\n"
"MA v,152,a,224,a,108\n"
"s_waitcnt lgkmcnt(4)\n"
"MA v,156,a,232,a,108\n"
"TR 208,245,24576\n"
"TR 210,245,26624\n"
"MA v,156,a,236,a,124\n"
"TR 212,245,28672\n"
"TR 214,245,30720\n"
"MA v,148,a,236,a,120\n"
"v_cvt_pk_bf16_f32 v8, v144, v145\n"
"v_cvt_pk_bf16_f32 v9, v146, v147\n"
"MA v,148,a,232,a,104\n"
"v_cvt_pk_bf16_f32 v12, v152, v153\n"
"v_cvt_pk_bf16_f32 v13, v154, v155\n"
"MA v,140,a,232,a,100\n"
"v_cvt_pk_bf16_f32 v14, v156, v157\n"
"v_cvt_pk_bf16_f32 v15, v158, v159\n"
"MA v,140,a,236,a,116\n"
"buffer_store_dwordx4 v[12:15], v249, s[16:19], s11 offen offset:2048 sc1\n"
"MA v,132,a,236,a,112\n"
"v_cvt_pk_bf16_f32 v10, v148, v149\n"
"MA v,132,a,232,a,96\n"
"v_cvt_pk_bf16_f32 v11, v150, v151\n"
"MA v,160,a,192,a,0\n"
"s_nop 1\n"
"v_cvt_pk_bf16_f32 v6, v140, v141\n"
"v_cvt_pk_bf16_f32 v7, v142, v143\n"
"MA v,160,a,196,a,16\n"
"buffer_store_dwordx4 v[4:7], v249, s[16:19], s9 offen offset:2048 sc1\n"
"MA v,168,a,196,a,20\n"
"v_cvt_pk_bf16_f32 v2, v132, v133\n"
"MA v,168,a,192,a,4\n"
"TR 216,245,24584\n"
"v_cvt_pk_bf16_f32 v3, v134, v135\n"
"MA v,176,a,192,a,8\n"
"buffer_store_dwordx4 v[0:3], v249, s[16:19], s8 offen offset:2048 sc1\n"
"MA v,176,a,196,a,24\n"
"TR 218,245,26632\n"
"MA v,184,a,196,a,28\n"
"buffer_store_dwordx4 v[8:11], v249, s[16:19], s10 offen offset:2048 sc1\n"
"MA v,184,a,192,a,12\n"
"s_waitcnt lgkmcnt(2)\n"
"TR 220,245,28680\n"
"MA v,188,a,200,a,12\n"
"TR 222,245,30728\n"
"TR 224,245,40960\n"
"MA v,188,a,204,a,28\n"
"TR 226,245,43008\n"
"TR 228,245,45056\n"
"MA v,180,a,204,a,24\n"
"TR 230,245,47104\n"
"MA v,180,a,200,a,8\n"
"MA v,172,a,200,a,4\n"
"MA v,172,a,204,a,20\n"
"MA v,164,a,204,a,16\n"
"MA v,164,a,200,a,0\n"
"MA v,160,a,208,a,32\n"
"TR 232,245,40968\n"
"TR 234,245,43016\n"
"MA v,160,a,212,a,48\n"
"TR 236,245,45064\n"
"TR 238,245,47112\n"
"MA v,168,a,212,a,52\n"
"MA v,168,a,208,a,36\n"
"MA v,176,a,208,a,40\n"
"MA v,176,a,212,a,56\n"
"MA v,184,a,212,a,60\n"
"MA v,184,a,208,a,44\n"
"s_waitcnt lgkmcnt(4)\n"
"MA v,188,a,216,a,44\n"
"MA v,188,a,220,a,60\n"
"MA v,180,a,220,a,56\n"
"MA v,180,a,216,a,40\n"
"MA v,172,a,216,a,36\n"
"MA v,172,a,220,a,52\n"
"MA v,164,a,220,a,48\n"
"MA v,164,a,216,a,32\n"
"MA v,160,a,224,a,96\n"
"MA v,160,a,228,a,112\n"
"MA v,168,a,228,a,116\n"
"MA v,168,a,224,a,100\n"
"MA v,176,a,224,a,104\n"
"s_nop 4\n"
"v_cvt_pk_bf16_f32 v0, v160, v161\n"
"v_cvt_pk_bf16_f32 v1, v162, v163\n"
"MA v,176,a,228,a,120\n"
"MA v,184,a,228,a,124\n"
"v_cvt_pk_bf16_f32 v4, v168, v169\n"
"v_cvt_pk_bf16_f32 v5, v170, v171\n"
"MA v,184,a,224,a,108\n"
"s_waitcnt lgkmcnt(0)\n"
"MA v,188,a,232,a,108\n"
"s_nop 1\n"
"v_cvt_pk_bf16_f32 v8, v176, v177\n"
"v_cvt_pk_bf16_f32 v9, v178, v179\n"
"MA v,188,a,236,a,124\n"
"MA v,180,a,236,a,120\n"
"v_cvt_pk_bf16_f32 v12, v184, v185\n"
"v_cvt_pk_bf16_f32 v13, v186, v187\n"
"MA v,180,a,232,a,104\n"
"MA v,172,a,232,a,100\n"
"s_nop 2\n"
"v_cvt_pk_bf16_f32 v14, v188, v189\n"
"v_cvt_pk_bf16_f32 v15, v190, v191\n"
"MA v,172,a,236,a,116\n"
"buffer_store_dwordx4 v[12:15], v249, s[16:19], s11 offen offset:3072 sc1\n"
"MA v,164,a,236,a,112\n"
"v_cvt_pk_bf16_f32 v10, v180, v181\n"
"v_cvt_pk_bf16_f32 v11, v182, v183\n"
"MA v,164,a,232,a,96\n"
"buffer_store_dwordx4 v[8:11], v249, s[16:19], s10 offen offset:3072 sc1\n"
"s_nop 1\n"
"v_cvt_pk_bf16_f32 v6, v172, v173\n"
"v_cvt_pk_bf16_f32 v7, v174, v175\n"
"buffer_store_dwordx4 v[4:7], v249, s[16:19], s9 offen offset:3072 sc1\n"
"s_nop 1\n"
"v_cvt_pk_bf16_f32 v2, v164, v165\n"
"v_cvt_pk_bf16_f32 v3, v166, v167\n"
"buffer_store_dwordx4 v[0:3], v249, s[16:19], s8 offen offset:3072 sc1\n"
"s_waitcnt vmcnt(0)\n"
"buffer_store_dwordx2 v[254:255], v234, s[16:19], s62 offen sc1\n"
"L_FINAL%=:\n"
"s_mov_b32 s2, 0x3c319c34\n"
"s_mov_b32 s4, 0\n"
"s_mov_b32 s5, 0\n"
"v_lshlrev_b32 v204, 16, v192\n"
"v_and_b32 v205, 0xffff0000, v192\n"
"v_lshlrev_b32 v206, 16, v193\n"
"v_and_b32 v207, 0xffff0000, v193\n"
"v_lshlrev_b32 v208, 16, v194\n"
"v_and_b32 v209, 0xffff0000, v194\n"
"v_lshlrev_b32 v210, 16, v195\n"
"v_and_b32 v211, 0xffff0000, v195\n"
"v_lshlrev_b32 v212, 16, v196\n"
"v_and_b32 v213, 0xffff0000, v196\n"
"v_lshlrev_b32 v214, 16, v197\n"
"v_and_b32 v215, 0xffff0000, v197\n"
"v_lshlrev_b32 v216, 16, v198\n"
"v_and_b32 v217, 0xffff0000, v198\n"
"v_lshlrev_b32 v218, 16, v199\n"
"v_and_b32 v219, 0xffff0000, v199\n"
"v_lshlrev_b32 v220, 16, v200\n"
"v_and_b32 v221, 0xffff0000, v200\n"
"v_lshlrev_b32 v222, 16, v201\n"
"v_and_b32 v223, 0xffff0000, v201\n"
"v_lshlrev_b32 v224, 16, v202\n"
"v_and_b32 v225, 0xffff0000, v202\n"
"v_lshlrev_b32 v226, 16, v203\n"
"v_and_b32 v227, 0xffff0000, v203\n"
"L_ROUND%=:\n"
"v_mov_b32 v52, s5\n"
"s_cmp_eq_u32 s44, 0\n"
"s_cbranch_scc0 L_NOPOLL2%=\n"
"buffer_load_dwordx2 v[254:255], v233, s[16:19], s63 offen sc1\n"
"s_waitcnt vmcnt(0)\n"
"v_subrev_u32 v254, s46, v254\n"
"v_cmp_gt_u32 vcc, 8, v254\n"
"v_cmp_eq_u32 s[94:95], s57, v255\n"
"s_nop 3\n"
"s_and_b32 vcc_lo, vcc_lo, s94\n"
"s_lshr_b32 s22, vcc_lo, 8\n"
"s_or_b32 s22, s22, s21\n"
"s_and_b32 s22, s22, vcc_lo\n"
"s_and_b32 s22, s22, 31\n"
"s_sub_u32 s65, s65, 1\n"
"s_cselect_b32 s22, 31, s22\n"
"v_mov_b32 v53, s22\n"
"ds_write_b32 v52, v53\n"
"s_waitcnt lgkmcnt(0)\n"
"L_NOPOLL2%=:\n"
"s_barrier\n"
"ds_read_b32 v53, v52\n"
"s_xor_b32 s5, s5, 4\n"
"s_lshr_b32 s0, s20, 4\n"
"s_andn2_b32 s0, s0, s4\n"
"s_cbranch_scc0 L_NOW%=\n"
"s_waitcnt vmcnt(0)\n"
"L_NOW%=:\n"
"s_waitcnt lgkmcnt(0)\n"
"v_readfirstlane_b32 s22, v53\n"
"s_nop 3\n"
"s_andn2_b32 s23, s22, s20\n"
"s_bitcmp1_b32 s23, 4\n"
"s_cbranch_scc0 L_SKR4_%=\n"
"buffer_load_dwordx4 a[144:147], v51, s[16:19], s79 offen sc1\n"
"buffer_load_dwordx4 a[164:167], v51, s[16:19], s83 offen sc1\n"
"buffer_load_dwordx4 a[184:187], v51, s[16:19], s85 offen sc1\n"
"L_SKR4_%=:\n"
"s_bitcmp1_b32 s23, 0\n"
"s_cbranch_scc0 L_SKR0_%=\n"
"buffer_load_dwordx4 a[128:131], v249, s[16:19], s79 offen sc1\n"
"buffer_load_dwordx4 a[148:151], v249, s[16:19], s83 offen sc1\n"
"buffer_load_dwordx4 a[168:171], v249, s[16:19], s85 offen sc1\n"
"L_SKR0_%=:\n"
"s_bitcmp1_b32 s23, 1\n"
"s_cbranch_scc0 L_SKR1_%=\n"
"buffer_load_dwordx4 a[132:135], v48, s[16:19], s79 offen sc1\n"
"buffer_load_dwordx4 a[152:155], v48, s[16:19], s83 offen sc1\n"
"buffer_load_dwordx4 a[172:175], v48, s[16:19], s85 offen sc1\n"
"L_SKR1_%=:\n"
"s_bitcmp1_b32 s23, 2\n"
"s_cbranch_scc0 L_SKR2_%=\n"
"buffer_load_dwordx4 a[136:139], v49, s[16:19], s79 offen sc1\n"
"buffer_load_dwordx4 a[156:159], v49, s[16:19], s83 offen sc1\n"
"buffer_load_dwordx4 a[176:179], v49, s[16:19], s85 offen sc1\n"
"L_SKR2_%=:\n"
"s_bitcmp1_b32 s23, 3\n"
"s_cbranch_scc0 L_SKR3_%=\n"
"buffer_load_dwordx4 a[140:143], v50, s[16:19], s79 offen sc1\n"
"buffer_load_dwordx4 a[160:163], v50, s[16:19], s83 offen sc1\n"
"buffer_load_dwordx4 a[180:183], v50, s[16:19], s85 offen sc1\n"
"L_SKR3_%=:\n"
"s_or_b32 s20, s20, s22\n"
"s_cmp_eq_u32 s0, 0\n"
"s_cbranch_scc1 L_NOGATE%=\n"
"MZ v,24,v,18,a,144\n"
"MZ v,28,v,16,a,144\n"
"MZ v,32,v,18,a,164\n"
"MZ v,36,v,16,a,164\n"
"s_nop 4\n"
"v_mul_f32 v0, 0xc3051591, v24\n"
"v_mul_f32 v1, 0xc3051591, v25\n"
"v_mul_f32 v2, 0xc3051591, v26\n"
"v_mul_f32 v3, 0xc3051591, v27\n"
"MZ v,40,v,18,a,184\n"
"v_exp_f32 v0, v0\n"
"v_exp_f32 v1, v1\n"
"v_exp_f32 v2, v2\n"
"v_exp_f32 v3, v3\n"
"v_mul_f32 v4, 0xc3051591, v28\n"
"v_mul_f32 v5, 0xc3051591, v29\n"
"v_mul_f32 v6, 0xc3051591, v30\n"
"v_mul_f32 v7, 0xc3051591, v31\n"
"MZ v,44,v,16,a,184\n"
"v_fma_f32 v0, v0, s2, s2\n"
"v_fma_f32 v1, v1, s2, s2\n"
"v_fma_f32 v2, v2, s2, s2\n"
"v_fma_f32 v3, v3, s2, s2\n"
"v_exp_f32 v4, v4\n"
"v_exp_f32 v5, v5\n"
"v_exp_f32 v6, v6\n"
"v_exp_f32 v7, v7\n"
"v_mul_f32 v8, 0xc3051591, v32\n"
"v_mul_f32 v9, 0xc3051591, v33\n"
"v_mul_f32 v10, 0xc3051591, v34\n"
"v_mul_f32 v11, 0xc3051591, v35\n"
"v_rcp_f32 v0, v0\n"
"v_rcp_f32 v1, v1\n"
"v_rcp_f32 v2, v2\n"
"v_rcp_f32 v3, v3\n"
"v_fma_f32 v4, v4, s2, s2\n"
"v_fma_f32 v5, v5, s2, s2\n"
"v_fma_f32 v6, v6, s2, s2\n"
"v_fma_f32 v7, v7, s2, s2\n"
"v_exp_f32 v8, v8\n"
"v_exp_f32 v9, v9\n"
"v_exp_f32 v10, v10\n"
"v_exp_f32 v11, v11\n"
"v_mul_f32 v12, 0xc3051591, v36\n"
"v_mul_f32 v13, 0xc3051591, v37\n"
"v_mul_f32 v14, 0xc3051591, v38\n"
"v_mul_f32 v15, 0xc3051591, v39\n"
"v_mul_f32 v24, v24, v0\n"
"v_mul_f32 v25, v25, v1\n"
"v_mul_f32 v26, v26, v2\n"
"v_mul_f32 v27, v27, v3\n"
"v_rcp_f32 v4, v4\n"
"v_rcp_f32 v5, v5\n"
"v_rcp_f32 v6, v6\n"
"v_rcp_f32 v7, v7\n"
"v_fma_f32 v8, v8, s2, s2\n"
"v_fma_f32 v9, v9, s2, s2\n"
"v_fma_f32 v10, v10, s2, s2\n"
"v_fma_f32 v11, v11, s2, s2\n"
"v_exp_f32 v12, v12\n"
"v_exp_f32 v13, v13\n"
"v_exp_f32 v14, v14\n"
"v_exp_f32 v15, v15\n"
"v_mul_f32 v0, 0xc3051591, v40\n"
"v_mul_f32 v1, 0xc3051591, v41\n"
"v_mul_f32 v2, 0xc3051591, v42\n"
"v_mul_f32 v3, 0xc3051591, v43\n"
"v_mul_f32 v28, v28, v4\n"
"v_mul_f32 v29, v29, v5\n"
"v_mul_f32 v30, v30, v6\n"
"v_mul_f32 v31, v31, v7\n"
"v_rcp_f32 v8, v8\n"
"v_rcp_f32 v9, v9\n"
"v_rcp_f32 v10, v10\n"
"v_rcp_f32 v11, v11\n"
"v_fma_f32 v12, v12, s2, s2\n"
"v_fma_f32 v13, v13, s2, s2\n"
"v_fma_f32 v14, v14, s2, s2\n"
"v_fma_f32 v15, v15, s2, s2\n"
"v_exp_f32 v0, v0\n"
"v_exp_f32 v1, v1\n"
"v_exp_f32 v2, v2\n"
"v_exp_f32 v3, v3\n"
"v_mul_f32 v4, 0xc3051591, v44\n"
"v_mul_f32 v5, 0xc3051591, v45\n"
"v_mul_f32 v6, 0xc3051591, v46\n"
"v_mul_f32 v7, 0xc3051591, v47\n"
"v_mul_f32 v32, v32, v8\n"
"v_mul_f32 v33, v33, v9\n"
"v_mul_f32 v34, v34, v10\n"
"v_mul_f32 v35, v35, v11\n"
"v_rcp_f32 v12, v12\n"
"v_rcp_f32 v13, v13\n"
"v_rcp_f32 v14, v14\n"
"v_rcp_f32 v15, v15\n"
"v_fma_f32 v0, v0, s2, s2\n"
"v_fma_f32 v1, v1, s2, s2\n"
"v_fma_f32 v2, v2, s2, s2\n"
"v_fma_f32 v3, v3, s2, s2\n"
"v_exp_f32 v4, v4\n"
"v_exp_f32 v5, v5\n"
"v_exp_f32 v6, v6\n"
"v_exp_f32 v7, v7\n"
"v_mul_f32 v36, v36, v12\n"
"v_mul_f32 v37, v37, v13\n"
"v_mul_f32 v38, v38, v14\n"
"v_mul_f32 v39, v39, v15\n"
"v_rcp_f32 v0, v0\n"
"v_rcp_f32 v1, v1\n"
"v_rcp_f32 v2, v2\n"
"v_rcp_f32 v3, v3\n"
"v_fma_f32 v4, v4, s2, s2\n"
"v_fma_f32 v5, v5, s2, s2\n"
"v_fma_f32 v6, v6, s2, s2\n"
"v_fma_f32 v7, v7, s2, s2\n"
"v_mul_f32 v40, v40, v0\n"
"v_mul_f32 v41, v41, v1\n"
"v_mul_f32 v42, v42, v2\n"
"v_mul_f32 v43, v43, v3\n"
"v_rcp_f32 v4, v4\n"
"v_rcp_f32 v5, v5\n"
"v_rcp_f32 v6, v6\n"
"v_rcp_f32 v7, v7\n"
"v_mul_f32 v44, v44, v4\n"
"v_mul_f32 v45, v45, v5\n"
"v_mul_f32 v46, v46, v6\n"
"v_mul_f32 v47, v47, v7\n"
"s_mov_b32 s4, 1\n"
"L_NOGATE%=:\n"
"s_cmp_eq_u32 s22, 31\n"
"s_cbranch_scc1 L_ALL%=\n"
"s_sleep 0\n"
"s_branch L_ROUND%=\n"
"L_ALL%=:\n"
"s_waitcnt vmcnt(0)\n"
"s_cmp_eq_u32 s4, 0\n"
"s_cbranch_scc1 L_FINA%=\n"
"MZ v,124,v,18,a,128\n"
"MA v,124,v,18,a,132\n"
"MA v,124,v,18,a,136\n"
"MA v,124,v,18,a,140\n"
"MZ v,128,v,16,a,128\n"
"MA v,128,v,16,a,132\n"
"MA v,128,v,16,a,136\n"
"s_nop 4\n"
"v_fma_f32 v124, v124, v24, v204\n"
"v_fma_f32 v125, v125, v25, v205\n"
"v_fma_f32 v126, v126, v26, v206\n"
"v_fma_f32 v127, v127, v27, v207\n"
"MA v,128,v,16,a,140\n"
"v_cvt_pk_bf16_f32 v192, v124, v125\n"
"v_cvt_pk_bf16_f32 v193, v126, v127\n"
"MZ v,132,v,18,a,148\n"
"MA v,132,v,18,a,152\n"
"MA v,132,v,18,a,156\n"
"s_nop 2\n"
"v_fma_f32 v128, v128, v28, v208\n"
"v_fma_f32 v129, v129, v29, v209\n"
"v_fma_f32 v130, v130, v30, v210\n"
"v_fma_f32 v131, v131, v31, v211\n"
"MA v,132,v,18,a,160\n"
"v_cvt_pk_bf16_f32 v194, v128, v129\n"
"v_cvt_pk_bf16_f32 v195, v130, v131\n"
"MZ v,136,v,16,a,148\n"
"buffer_store_dwordx4 v[192:195], v253, s[32:35], s80 offen sc1\n"
"MA v,136,v,16,a,152\n"
"MA v,136,v,16,a,156\n"
"s_nop 1\n"
"v_fma_f32 v132, v132, v32, v212\n"
"v_fma_f32 v133, v133, v33, v213\n"
"v_fma_f32 v134, v134, v34, v214\n"
"v_fma_f32 v135, v135, v35, v215\n"
"MA v,136,v,16,a,160\n"
"v_cvt_pk_bf16_f32 v196, v132, v133\n"
"v_cvt_pk_bf16_f32 v197, v134, v135\n"
"MZ v,140,v,18,a,168\n"
"MA v,140,v,18,a,172\n"
"MA v,140,v,18,a,176\n"
"s_nop 2\n"
"v_fma_f32 v136, v136, v36, v216\n"
"v_fma_f32 v137, v137, v37, v217\n"
"v_fma_f32 v138, v138, v38, v218\n"
"v_fma_f32 v139, v139, v39, v219\n"
"MA v,140,v,18,a,180\n"
"v_cvt_pk_bf16_f32 v198, v136, v137\n"
"v_cvt_pk_bf16_f32 v199, v138, v139\n"
"MZ v,144,v,16,a,168\n"
"buffer_store_dwordx4 v[196:199], v253, s[32:35], s81 offen sc1\n"
"MA v,144,v,16,a,172\n"
"MA v,144,v,16,a,176\n"
"s_nop 1\n"
"v_fma_f32 v140, v140, v40, v220\n"
"v_fma_f32 v141, v141, v41, v221\n"
"v_fma_f32 v142, v142, v42, v222\n"
"v_fma_f32 v143, v143, v43, v223\n"
"MA v,144,v,16,a,180\n"
"v_cvt_pk_bf16_f32 v200, v140, v141\n"
"v_cvt_pk_bf16_f32 v201, v142, v143\n"
"s_nop 5\n"
"v_fma_f32 v144, v144, v44, v224\n"
"v_fma_f32 v145, v145, v45, v225\n"
"v_fma_f32 v146, v146, v46, v226\n"
"v_fma_f32 v147, v147, v47, v227\n"
"v_cvt_pk_bf16_f32 v202, v144, v145\n"
"v_cvt_pk_bf16_f32 v203, v146, v147\n"
"buffer_store_dwordx4 v[200:203], v253, s[32:35], s82 offen sc1\n"
"s_branch L_FINB%=\n"
"L_FINA%=:\n"
"MZ v,24,v,18,a,144\n"
"MZ v,28,v,16,a,144\n"
"MZ v,32,v,18,a,164\n"
"MZ v,36,v,16,a,164\n"
"s_nop 4\n"
"v_mul_f32 v0, 0xc3051591, v24\n"
"v_mul_f32 v1, 0xc3051591, v25\n"
"v_mul_f32 v2, 0xc3051591, v26\n"
"v_mul_f32 v3, 0xc3051591, v27\n"
"MZ v,40,v,18,a,184\n"
"v_exp_f32 v0, v0\n"
"v_exp_f32 v1, v1\n"
"v_mul_f32 v4, 0xc3051591, v28\n"
"v_mul_f32 v5, 0xc3051591, v29\n"
"v_mul_f32 v6, 0xc3051591, v30\n"
"v_mul_f32 v7, 0xc3051591, v31\n"
"MZ v,44,v,16,a,184\n"
"v_exp_f32 v2, v2\n"
"v_exp_f32 v3, v3\n"
"v_exp_f32 v4, v4\n"
"v_exp_f32 v5, v5\n"
"MZ v,124,v,18,a,128\n"
"v_fma_f32 v0, v0, s2, s2\n"
"v_fma_f32 v1, v1, s2, s2\n"
"v_fma_f32 v2, v2, s2, s2\n"
"v_fma_f32 v3, v3, s2, s2\n"
"v_exp_f32 v6, v6\n"
"v_exp_f32 v7, v7\n"
"MA v,124,v,18,a,132\n"
"v_rcp_f32 v0, v0\n"
"v_rcp_f32 v1, v1\n"
"v_fma_f32 v4, v4, s2, s2\n"
"v_fma_f32 v5, v5, s2, s2\n"
"v_fma_f32 v6, v6, s2, s2\n"
"v_fma_f32 v7, v7, s2, s2\n"
"MA v,124,v,18,a,136\n"
"v_rcp_f32 v2, v2\n"
"v_rcp_f32 v3, v3\n"
"v_rcp_f32 v4, v4\n"
"v_rcp_f32 v5, v5\n"
"MA v,124,v,18,a,140\n"
"v_mul_f32 v24, v24, v0\n"
"v_mul_f32 v25, v25, v1\n"
"v_mul_f32 v26, v26, v2\n"
"v_mul_f32 v27, v27, v3\n"
"v_rcp_f32 v6, v6\n"
"v_rcp_f32 v7, v7\n"
"MZ v,128,v,16,a,128\n"
"v_mul_f32 v28, v28, v4\n"
"v_mul_f32 v29, v29, v5\n"
"v_mul_f32 v30, v30, v6\n"
"v_mul_f32 v31, v31, v7\n"
"v_mul_f32 v8, 0xc3051591, v32\n"
"v_mul_f32 v9, 0xc3051591, v33\n"
"v_mul_f32 v10, 0xc3051591, v34\n"
"v_mul_f32 v11, 0xc3051591, v35\n"
"MA v,128,v,16,a,132\n"
"v_exp_f32 v8, v8\n"
"v_exp_f32 v9, v9\n"
"v_mul_f32 v12, 0xc3051591, v36\n"
"v_mul_f32 v13, 0xc3051591, v37\n"
"v_mul_f32 v14, 0xc3051591, v38\n"
"v_mul_f32 v15, 0xc3051591, v39\n"
"MA v,128,v,16,a,136\n"
"v_fma_f32 v124, v124, v24, v204\n"
"v_fma_f32 v125, v125, v25, v205\n"
"v_fma_f32 v126, v126, v26, v206\n"
"v_fma_f32 v127, v127, v27, v207\n"
"v_exp_f32 v10, v10\n"
"v_exp_f32 v11, v11\n"
"MA v,128,v,16,a,140\n"
"v_cvt_pk_bf16_f32 v192, v124, v125\n"
"v_cvt_pk_bf16_f32 v193, v126, v127\n"
"v_exp_f32 v12, v12\n"
"v_exp_f32 v13, v13\n"
"v_exp_f32 v14, v14\n"
"MZ v,132,v,18,a,148\n"
"v_fma_f32 v8, v8, s2, s2\n"
"v_fma_f32 v9, v9, s2, s2\n"
"v_fma_f32 v10, v10, s2, s2\n"
"v_fma_f32 v11, v11, s2, s2\n"
"v_fma_f32 v12, v12, s2, s2\n"
"v_fma_f32 v13, v13, s2, s2\n"
"v_exp_f32 v15, v15\n"
"MA v,132,v,18,a,152\n"
"v_rcp_f32 v8, v8\n"
"v_rcp_f32 v9, v9\n"
"v_rcp_f32 v10, v10\n"
"v_fma_f32 v14, v14, s2, s2\n"
"v_fma_f32 v15, v15, s2, s2\n"
"MA v,132,v,18,a,156\n"
"v_fma_f32 v128, v128, v28, v208\n"
"v_fma_f32 v129, v129, v29, v209\n"
"v_fma_f32 v130, v130, v30, v210\n"
"v_fma_f32 v131, v131, v31, v211\n"
"v_mul_f32 v32, v32, v8\n"
"v_rcp_f32 v11, v11\n"
"MA v,132,v,18,a,160\n"
"v_cvt_pk_bf16_f32 v194, v128, v129\n"
"v_cvt_pk_bf16_f32 v195, v130, v131\n"
"v_rcp_f32 v12, v12\n"
"v_rcp_f32 v13, v13\n"
"v_rcp_f32 v14, v14\n"
"MZ v,136,v,16,a,148\n"
"buffer_store_dwordx4 v[192:195], v253, s[32:35], s80 offen sc1\n"
"MA v,136,v,16,a,152\n"
"v_mul_f32 v33, v33, v9\n"
"v_mul_f32 v34, v34, v10\n"
"v_mul_f32 v35, v35, v11\n"
"v_mul_f32 v36, v36, v12\n"
"v_mul_f32 v37, v37, v13\n"
"v_mul_f32 v38, v38, v14\n"
"v_rcp_f32 v15, v15\n"
"v_mul_f32 v0, 0xc3051591, v40\n"
"MA v,136,v,16,a,156\n"
"v_mul_f32 v39, v39, v15\n"
"v_fma_f32 v132, v132, v32, v212\n"
"v_fma_f32 v133, v133, v33, v213\n"
"v_fma_f32 v134, v134, v34, v214\n"
"v_fma_f32 v135, v135, v35, v215\n"
"v_mul_f32 v1, 0xc3051591, v41\n"
"v_mul_f32 v2, 0xc3051591, v42\n"
"v_mul_f32 v3, 0xc3051591, v43\n"
"MA v,136,v,16,a,160\n"
"v_cvt_pk_bf16_f32 v196, v132, v133\n"
"v_cvt_pk_bf16_f32 v197, v134, v135\n"
"v_exp_f32 v0, v0\n"
"v_mul_f32 v4, 0xc3051591, v44\n"
"v_mul_f32 v5, 0xc3051591, v45\n"
"v_mul_f32 v6, 0xc3051591, v46\n"
"v_mul_f32 v7, 0xc3051591, v47\n"
"MZ v,140,v,18,a,168\n"
"v_exp_f32 v1, v1\n"
"v_exp_f32 v2, v2\n"
"v_exp_f32 v3, v3\n"
"v_exp_f32 v4, v4\n"
"MA v,140,v,18,a,172\n"
"v_fma_f32 v0, v0, s2, s2\n"
"v_fma_f32 v1, v1, s2, s2\n"
"v_exp_f32 v5, v5\n"
"v_exp_f32 v6, v6\n"
"v_exp_f32 v7, v7\n"
"MA v,140,v,18,a,176\n"
"v_fma_f32 v136, v136, v36, v216\n"
"v_fma_f32 v137, v137, v37, v217\n"
"v_fma_f32 v138, v138, v38, v218\n"
"v_fma_f32 v139, v139, v39, v219\n"
"v_fma_f32 v2, v2, s2, s2\n"
"v_fma_f32 v3, v3, s2, s2\n"
"v_fma_f32 v4, v4, s2, s2\n"
"v_fma_f32 v5, v5, s2, s2\n"
"MA v,140,v,18,a,180\n"
"v_cvt_pk_bf16_f32 v198, v136, v137\n"
"v_cvt_pk_bf16_f32 v199, v138, v139\n"
"v_rcp_f32 v0, v0\n"
"v_fma_f32 v6, v6, s2, s2\n"
"v_fma_f32 v7, v7, s2, s2\n"
"MZ v,144,v,16,a,168\n"
"buffer_store_dwordx4 v[196:199], v253, s[32:35], s81 offen sc1\n"
"MA v,144,v,16,a,172\n"
"v_rcp_f32 v1, v1\n"
"v_rcp_f32 v2, v2\n"
"v_rcp_f32 v3, v3\n"
"v_rcp_f32 v4, v4\n"
"v_rcp_f32 v5, v5\n"
"MA v,144,v,16,a,176\n"
"v_mul_f32 v40, v40, v0\n"
"v_mul_f32 v41, v41, v1\n"
"v_mul_f32 v42, v42, v2\n"
"v_mul_f32 v43, v43, v3\n"
"v_mul_f32 v44, v44, v4\n"
"v_mul_f32 v45, v45, v5\n"
"v_rcp_f32 v6, v6\n"
"v_rcp_f32 v7, v7\n"
"MA v,144,v,16,a,180\n"
"v_mul_f32 v46, v46, v6\n"
"v_mul_f32 v47, v47, v7\n"
"v_fma_f32 v140, v140, v40, v220\n"
"v_fma_f32 v141, v141, v41, v221\n"
"v_fma_f32 v142, v142, v42, v222\n"
"v_fma_f32 v143, v143, v43, v223\n"
"v_cvt_pk_bf16_f32 v200, v140, v141\n"
"v_cvt_pk_bf16_f32 v201, v142, v143\n"
"v_fma_f32 v144, v144, v44, v224\n"
"v_fma_f32 v145, v145, v45, v225\n"
"v_fma_f32 v146, v146, v46, v226\n"
"v_fma_f32 v147, v147, v47, v227\n"
"v_cvt_pk_bf16_f32 v202, v144, v145\n"
"v_cvt_pk_bf16_f32 v203, v146, v147\n"
"buffer_store_dwordx4 v[200:203], v253, s[32:35], s82 offen sc1\n"
"L_FINB%=:\n"
"s_branch L_END%=\n"
"L_FALLBACK%=:\n"
"s_waitcnt vmcnt(0) lgkmcnt(0)\n"
"s_barrier\n"
"s_mov_b32 s51, 0\n"
"s_mul_i32 s86, s44, 3\n"
"s_sub_u32 s20, s20, s86\n"
"s_subb_u32 s21, s21, 0\n"
"s_add_u32 s22, s22, s86\n"
"v_lshrrev_b32 v224, 4, v249\n"
"v_and_b32 v224, 15, v224\n"
"v_mul_u32_u24 v224, 384, v224\n"
"v_lshrrev_b32 v225, 8, v249\n"
"v_lshl_add_u32 v224, v225, 4, v224\n"
"v_add_u32 v225, 6144, v224\n"
"v_mov_b32 v228, 0xff800000\n"
"v_mov_b32 v229, 0xff800000\n"
"v_mov_b32 v230, 0xff800000\n"
"v_mov_b32 v231, 0xff800000\n"
"s_mov_b32 s87, 0\n"
"L_FBLOOP%=:\n"
"buffer_load_dwordx4 a[192:195], v224, s[20:23], s87 offen\n"
"buffer_load_dwordx4 a[196:199], v225, s[20:23], s87 offen\n"
"buffer_load_dwordx4 a[200:203], v224, s[20:23], s87 offen offset:64\n"
"buffer_load_dwordx4 a[204:207], v225, s[20:23], s87 offen offset:64\n"
"buffer_load_dwordx4 a[208:211], v224, s[20:23], s87 offen offset:128\n"
"buffer_load_dwordx4 a[212:215], v225, s[20:23], s87 offen offset:128\n"
"buffer_load_dwordx4 a[216:219], v224, s[20:23], s87 offen offset:192\n"
"buffer_load_dwordx4 a[220:223], v225, s[20:23], s87 offen offset:192\n"
"buffer_load_dwordx4 a[224:227], v224, s[20:23], s87 offen offset:256\n"
"buffer_load_dwordx4 a[228:231], v225, s[20:23], s87 offen offset:256\n"
"buffer_load_dwordx4 a[232:235], v224, s[20:23], s87 offen offset:320\n"
"buffer_load_dwordx4 a[236:239], v225, s[20:23], s87 offen offset:320\n"
"s_waitcnt vmcnt(11)\n"
"MZ v,0,a,192,a,0\n"
"MZ v,4,a,192,a,4\n"
"MZ v,8,a,192,a,8\n"
"MZ v,12,a,192,a,12\n"
"s_waitcnt vmcnt(10)\n"
"MZ v,16,a,196,a,0\n"
"MZ v,20,a,196,a,4\n"
"MZ v,24,a,196,a,8\n"
"MZ v,28,a,196,a,12\n"
"s_waitcnt vmcnt(9)\n"
"MA v,0,a,200,a,16\n"
"MA v,4,a,200,a,20\n"
"MA v,8,a,200,a,24\n"
"MA v,12,a,200,a,28\n"
"s_waitcnt vmcnt(8)\n"
"MA v,16,a,204,a,16\n"
"MA v,20,a,204,a,20\n"
"MA v,24,a,204,a,24\n"
"MA v,28,a,204,a,28\n"
"s_waitcnt vmcnt(7)\n"
"MA v,0,a,208,a,32\n"
"MA v,4,a,208,a,36\n"
"MA v,8,a,208,a,40\n"
"MA v,12,a,208,a,44\n"
"s_waitcnt vmcnt(6)\n"
"MA v,16,a,212,a,32\n"
"MA v,20,a,212,a,36\n"
"MA v,24,a,212,a,40\n"
"MA v,28,a,212,a,44\n"
"s_waitcnt vmcnt(5)\n"
"MA v,0,a,216,a,48\n"
"MA v,4,a,216,a,52\n"
"MA v,8,a,216,a,56\n"
"MA v,12,a,216,a,60\n"
"s_waitcnt vmcnt(4)\n"
"MA v,16,a,220,a,48\n"
"MA v,20,a,220,a,52\n"
"MA v,24,a,220,a,56\n"
"MA v,28,a,220,a,60\n"
"s_waitcnt vmcnt(3)\n"
"MA v,0,a,224,a,64\n"
"MA v,4,a,224,a,68\n"
"MA v,8,a,224,a,72\n"
"MA v,12,a,224,a,76\n"
"s_waitcnt vmcnt(2)\n"
"MA v,16,a,228,a,64\n"
"MA v,20,a,228,a,68\n"
"MA v,24,a,228,a,72\n"
"MA v,28,a,228,a,76\n"
"s_waitcnt vmcnt(1)\n"
"MA v,0,a,232,a,80\n"
"MA v,4,a,232,a,84\n"
"MA v,8,a,232,a,88\n"
"MA v,12,a,232,a,92\n"
"s_waitcnt vmcnt(0)\n"
"MA v,16,a,236,a,80\n"
"MA v,20,a,236,a,84\n"
"MA v,24,a,236,a,88\n"
"MA v,28,a,236,a,92\n"
"v_max3_f32 v228, v228, v0, v1\n"
"v_max3_f32 v228, v228, v2, v3\n"
"s_nop 2\n"
"v_max3_f32 v228, v228, v16, v17\n"
"v_max3_f32 v228, v228, v18, v19\n"
"v_max3_f32 v229, v229, v4, v5\n"
"v_max3_f32 v229, v229, v6, v7\n"
"v_max3_f32 v229, v229, v20, v21\n"
"v_max3_f32 v229, v229, v22, v23\n"
"v_max3_f32 v230, v230, v8, v9\n"
"v_max3_f32 v230, v230, v10, v11\n"
"v_max3_f32 v230, v230, v24, v25\n"
"v_max3_f32 v230, v230, v26, v27\n"
"v_max3_f32 v231, v231, v12, v13\n"
"v_max3_f32 v231, v231, v14, v15\n"
"v_max3_f32 v231, v231, v28, v29\n"
"v_max3_f32 v231, v231, v30, v31\n"
"s_add_u32 s87, s87, 12288\n"
"s_cmp_lt_u32 s87, 196608\n"
"s_nop 7\n"
"s_cbranch_scc1 L_FBLOOP%=\n"
"s_add_u32 s20, s20, s86\n"
"s_addc_u32 s21, s21, 0\n"
"s_sub_u32 s22, s22, s86\n"
"s_add_u32 m0, s44, 114688\n"
"s_mov_b32 s86, 0\n"
"buffer_load_dwordx4 v248, s[20:23], s86 offen lds\n"
"s_mov_b32 s87, 2048\n"
"buffer_load_dwordx4 v248, s[20:23], s87 offen offset:1024 lds\n"
"s_add_u32 m0, s44, 122880\n"
"s_mov_b32 s88, 128\n"
"buffer_load_dwordx4 v248, s[20:23], s88 offen lds\n"
"s_mov_b32 s89, 2176\n"
"buffer_load_dwordx4 v248, s[20:23], s89 offen offset:1024 lds\n"
"s_add_u32 m0, s44, 131072\n"
"s_mov_b32 s90, 256\n"
"buffer_load_dwordx4 v248, s[20:23], s90 offen lds\n"
"s_mov_b32 s91, 2304\n"
"buffer_load_dwordx4 v248, s[20:23], s91 offen offset:1024 lds\n"
"s_add_u32 m0, s44, 139264\n"
"s_mov_b32 s86, 24576\n"
"buffer_load_dwordx4 v248, s[20:23], s86 offen lds\n"
"s_mov_b32 s87, 26624\n"
"buffer_load_dwordx4 v248, s[20:23], s87 offen offset:1024 lds\n"
"s_add_u32 m0, s44, 147456\n"
"s_mov_b32 s88, 24704\n"
"buffer_load_dwordx4 v248, s[20:23], s88 offen lds\n"
"s_mov_b32 s89, 26752\n"
"buffer_load_dwordx4 v248, s[20:23], s89 offen offset:1024 lds\n"
"s_add_u32 m0, s44, 155648\n"
"s_mov_b32 s90, 24832\n"
"buffer_load_dwordx4 v248, s[20:23], s90 offen lds\n"
"s_mov_b32 s91, 26880\n"
"buffer_load_dwordx4 v248, s[20:23], s91 offen offset:1024 lds\n"
"s_waitcnt vmcnt(0)\n"
"s_barrier\n"
"v_cmp_gt_u32 vcc, 2560, v232\n"
"v_mov_b32 v255, s55\n"
"s_nop 0\n"
"v_cndmask_b32 v255, v255, v232, vcc\n"
"v_mov_b32 v252, 0x3f803f80\n"
"v_mov_b32 v253, 0x3f803f80\n"
"s_barrier\n"
"ds_read_b128 a[192:195], v246 offset:0\n"
"ds_read_b128 a[196:199], v247 offset:0\n"
"ds_read_b128 a[200:203], v246 offset:2048\n"
"ds_read_b128 a[204:207], v247 offset:2048\n"
"ds_read_b128 a[208:211], v246 offset:8192\n"
"ds_read_b128 a[212:215], v247 offset:8192\n"
"s_waitcnt lgkmcnt(5)\n"
"MZ v,0,a,192,a,0\n"
"s_waitcnt lgkmcnt(4)\n"
"ds_read_b128 a[216:219], v246 offset:10240\n"
"MA v,0,a,196,a,16\n"
"ds_read_b128 a[220:223], v247 offset:10240\n"
"MZ v,4,a,196,a,20\n"
"MA v,4,a,192,a,4\n"
"s_add_u32 m0, s44, 65536\n"
"s_mov_b32 s86, 49152\n"
"MZ v,8,a,192,a,8\n"
"MA v,8,a,196,a,24\n"
"MZ v,12,a,196,a,28\n"
"buffer_load_dwordx4 v248, s[20:23], s86 offen lds\n"
"MA v,12,a,192,a,12\n"
"s_waitcnt lgkmcnt(5)\n"
"MZ v,28,a,200,a,12\n"
"s_waitcnt lgkmcnt(4)\n"
"ds_read_b128 a[224:227], v246 offset:16384\n"
"MA v,28,a,204,a,28\n"
"s_mov_b32 s87, 51200\n"
"ds_read_b128 a[228:231], v247 offset:16384\n"
"MZ v,24,a,204,a,24\n"
"MA v,24,a,200,a,8\n"
"MZ v,20,a,200,a,4\n"
"buffer_load_dwordx4 v248, s[20:23], s87 offen offset:1024 lds\n"
"MA v,20,a,204,a,20\n"
"MZ v,16,a,204,a,16\n"
"MA v,16,a,200,a,0\n"
"s_waitcnt lgkmcnt(5)\n"
"s_add_u32 m0, s44, 73728\n"
"MA v,0,a,208,a,32\n"
"s_waitcnt lgkmcnt(4)\n"
"s_mov_b32 s88, 49280\n"
"ds_read_b128 a[232:235], v246 offset:18432\n"
"MA v,0,a,212,a,48\n"
"ds_read_b128 a[236:239], v247 offset:18432\n"
"MA v,4,a,212,a,52\n"
"buffer_load_dwordx4 v248, s[20:23], s88 offen lds\n"
"MA v,4,a,208,a,36\n"
"MA v,8,a,208,a,40\n"
"MA v,8,a,212,a,56\n"
"s_mov_b32 s89, 51328\n"
"MA v,12,a,212,a,60\n"
"MA v,12,a,208,a,44\n"
"s_waitcnt lgkmcnt(5)\n"
"MA v,28,a,216,a,44\n"
"s_waitcnt lgkmcnt(4)\n"
"buffer_load_dwordx4 v248, s[20:23], s89 offen offset:1024 lds\n"
"MA v,28,a,220,a,60\n"
"MA v,24,a,220,a,56\n"
"MA v,24,a,216,a,40\n"
"s_add_u32 m0, s44, 81920\n"
"MA v,20,a,216,a,36\n"
"s_mov_b32 s90, 49408\n"
"MA v,20,a,220,a,52\n"
"MA v,16,a,220,a,48\n"
"MA v,16,a,216,a,32\n"
"s_waitcnt lgkmcnt(3)\n"
"MA v,0,a,224,a,64\n"
"s_waitcnt lgkmcnt(2)\n"
"MA v,0,a,228,a,80\n"
"s_mov_b32 s91, 51456\n"
"MA v,4,a,228,a,84\n"
"MA v,4,a,224,a,68\n"
"buffer_load_dwordx4 v248, s[20:23], s90 offen lds\n"
"MA v,8,a,224,a,72\n"
"MA v,8,a,228,a,88\n"
"ds_read_b128 a[192:195], v246 offset:4096\n"
"v_mov_b32 v250, 0x3f803f80\n"
"MA v,12,a,228,a,92\n"
"ds_read_b128 a[196:199], v247 offset:4096\n"
"v_mov_b32 v251, 0x3f803f80\n"
"MA v,12,a,224,a,76\n"
"s_waitcnt lgkmcnt(3)\n"
"buffer_load_dwordx4 v248, s[20:23], s91 offen offset:1024 lds\n"
"MA v,28,a,232,a,76\n"
"s_waitcnt lgkmcnt(2)\n"
"ds_read_b128 a[200:203], v246 offset:6144\n"
"MA v,28,a,236,a,92\n"
"ds_read_b128 a[204:207], v247 offset:6144\n"
"MA v,24,a,236,a,88\n"
"ds_read_b128 a[208:211], v246 offset:12288\n"
"MA v,24,a,232,a,72\n"
"ds_read_b128 a[212:215], v247 offset:12288\n"
"MA v,20,a,232,a,68\n"
"MA v,20,a,236,a,84\n"
"MA v,16,a,236,a,80\n"
"MA v,16,a,232,a,64\n"
"s_add_u32 m0, s44, 90112\n"
"s_waitcnt vmcnt(0) lgkmcnt(0)\n"
"s_nop 7\n"
"s_branch L_JOIN%=\n"
"L_IDLE%=:\n"
"s_waitcnt vmcnt(0) lgkmcnt(0)\n"
"s_nop 7\n"
"v_mul_f32 v208, v208, v236\n"
"v_mul_f32 v209, v209, v236\n"
"v_mul_f32 v210, v210, v236\n"
"v_mul_f32 v211, v211, v236\n"
"s_add_u32 s13, s47, 20480\n"
"v_cvt_pk_bf16_f32 v226, v208, v209\n"
"v_cvt_pk_bf16_f32 v227, v210, v211\n"
"buffer_store_dwordx4 v[224:227], v249, s[16:19], s13 offen sc1\n"
"v_mul_f32 v53, v53, v237\n"
"v_mul_f32 v54, v54, v237\n"
"v_mul_f32 v55, v55, v237\n"
"v_mul_f32 v212, v212, v237\n"
"v_mul_f32 v213, v213, v237\n"
"v_mul_f32 v214, v214, v237\n"
"v_mul_f32 v215, v215, v237\n"
"v_cvt_pk_bf16_f32 v228, v52, v53\n"
"v_cvt_pk_bf16_f32 v229, v54, v55\n"
"v_cvt_pk_bf16_f32 v230, v212, v213\n"
"v_cvt_pk_bf16_f32 v231, v214, v215\n"
"buffer_store_dwordx4 v[228:231], v249, s[16:19], s13 offen offset:1024 sc1\n"
"v_mul_f32 v56, v56, v238\n"
"v_mul_f32 v57, v57, v238\n"
"v_mul_f32 v58, v58, v238\n"
"v_mul_f32 v59, v59, v238\n"
"v_mul_f32 v216, v216, v238\n"
"v_mul_f32 v217, v217, v238\n"
"v_mul_f32 v218, v218, v238\n"
"v_mul_f32 v219, v219, v238\n"
"v_cvt_pk_bf16_f32 v224, v56, v57\n"
"v_cvt_pk_bf16_f32 v225, v58, v59\n"
"v_cvt_pk_bf16_f32 v226, v216, v217\n"
"v_cvt_pk_bf16_f32 v227, v218, v219\n"
"buffer_store_dwordx4 v[224:227], v249, s[16:19], s13 offen offset:2048 sc1\n"
"v_mul_f32 v60, v60, v239\n"
"v_mul_f32 v61, v61, v239\n"
"v_mul_f32 v62, v62, v239\n"
"v_mul_f32 v63, v63, v239\n"
"v_mul_f32 v220, v220, v239\n"
"v_mul_f32 v221, v221, v239\n"
"v_cvt_pk_bf16_f32 v228, v60, v61\n"
"v_mul_f32 v222, v222, v239\n"
"v_mul_f32 v223, v223, v239\n"
"v_cvt_pk_bf16_f32 v229, v62, v63\n"
"v_cvt_pk_bf16_f32 v230, v220, v221\n"
"v_cvt_pk_bf16_f32 v231, v222, v223\n"
"buffer_store_dwordx4 v[228:231], v249, s[16:19], s13 offen offset:3072 sc1\n"
"v_bfe_u32 v16, v249, 6, 2\n"
"v_lshrrev_b32 v17, 8, v249\n"
"v_cmp_eq_u32 vcc, v16, v17\n"
"v_and_b32 v16, 16, v249\n"
"v_mov_b32 v17, 0x3f80\n"
"v_lshlrev_b32 v17, v16, v17\n"
"v_and_b32 v16, 32, v249\n"
"v_cndmask_b32 v19, 0, v17, vcc\n"
"v_cmp_eq_u32 vcc, 0, v16\n"
"s_nop 1\n"
"v_cndmask_b32 v18, 0, v19, vcc\n"
"s_mov_b32 s8, s60\n"
"s_add_u32 s9, s56, 4\n"
"v_mov_b32 v23, s57\n"
"v_mov_b32 v22, s9\n"
"s_waitcnt vmcnt(0)\n"
"buffer_store_dwordx2 v[22:23], v234, s[16:19], s8 offen sc1\n"
"v_add_u32 v48, 2621440, v249\n"
"v_add_u32 v49, 5242880, v249\n"
"v_add_u32 v50, 7864320, v249\n"
"v_add_u32 v51, 10485760, v249\n"
"s_mov_b32 s20, 0\n"
"s_lshr_b32 s21, s45, 8\n"
"s_not_b32 s21, s21\n"
"s_add_u32 s46, s56, 1\n"
"v_sub_u32 v19, v19, v18\n"
"v_mov_b32 v16, 0\n"
"v_mov_b32 v17, 0\n"
"v_mov_b32 v20, 0\n"
"v_mov_b32 v21, 0\n"
"buffer_load_dwordx4 v[192:195], v253, s[28:31], s80 offen\n"
"buffer_load_dwordx4 v[196:199], v253, s[28:31], s81 offen\n"
"buffer_load_dwordx4 v[200:203], v253, s[28:31], s82 offen\n"
"s_waitcnt vmcnt(0)\n"
"s_sleep 127\n"
"s_sleep 127\n"
"s_sleep 127\n"
"s_sleep 94\n"
"s_branch L_FINAL%=\n"
"L_SLOW0_%=:\n"
"s_cmp_eq_u32 s65, 0\n"
"s_cbranch_scc1 L_BACK0_%=\n"
"s_sub_u32 s65, s65, 1\n"
"s_sleep 1\n"
"buffer_load_dwordx2 v[194:195], v45, s[16:19], s59 offen sc1\n"
"s_waitcnt vmcnt(0)\n"
"v_subrev_u32 v194, s87, v194\n"
"v_cmp_gt_u32 vcc, 8, v194\n"
"v_cmp_eq_u32 s[94:95], s57, v195\n"
"s_nop 3\n"
"s_and_b32 vcc_lo, vcc_lo, s94\n"
"s_andn2_b32 vcc_lo, -1, vcc_lo\n"
"s_cbranch_scc1 L_SLOW0_%=\n"
"s_branch L_BACK0_%=\n"
"L_SLOW1_%=:\n"
"s_cmp_eq_u32 s65, 0\n"
"s_cbranch_scc1 L_BACK1_%=\n"
"s_sub_u32 s65, s65, 1\n"
"s_sleep 4\n"
"buffer_load_dwordx2 v[254:255], v253, s[16:19], s11 offen sc1\n"
"s_waitcnt vmcnt(0)\n"
"v_subrev_u32 v254, s12, v254\n"
"v_cmp_gt_u32 vcc, 8, v254\n"
"v_cmp_eq_u32 s[94:95], s57, v255\n"
"s_nop 3\n"
"s_and_b32 vcc_lo, vcc_lo, s94\n"
"s_andn2_b32 vcc_lo, s66, vcc_lo\n"
"s_cbranch_scc1 L_SLOW1_%=\n"
"s_branch L_BACK1_%=\n"
"L_SLOW2_%=:\n"
"s_cmp_eq_u32 s65, 0\n"
"s_cbranch_scc1 L_BACK2_%=\n"
"s_sub_u32 s65, s65, 1\n"
"s_sleep 4\n"
"buffer_load_dwordx2 v[254:255], v233, s[16:19], s61 offen sc1\n"
"s_waitcnt vmcnt(0)\n"
"v_subrev_u32 v254, s10, v254\n"
"v_cmp_gt_u32 vcc, 8, v254\n"
"v_cmp_eq_u32 s[94:95], s57, v255\n"
"s_nop 3\n"
"s_and_b32 vcc_lo, vcc_lo, s94\n"
"s_andn2_b32 vcc_lo, s64, vcc_lo\n"
"s_cbranch_scc1 L_SLOW2_%=\n"
"s_branch L_BACK2_%=\n"
"L_SLOWB_%=:\n"
"s_cmp_eq_u32 s65, 0\n"
"s_cbranch_scc1 L_BACKB_%=\n"
"s_sub_u32 s65, s65, 1\n"
"s_sleep 4\n"
"buffer_load_dwordx2 v[254:255], v233, s[16:19], s10 offen sc1\n"
"s_waitcnt vmcnt(0)\n"
"v_subrev_u32 v254, s11, v254\n"
"v_cmp_gt_u32 vcc, 8, v254\n"
"v_cmp_eq_u32 s[94:95], s57, v255\n"
"s_nop 3\n"
"s_and_b32 vcc_lo, vcc_lo, s94\n"
"s_andn2_b32 vcc_lo, s6, vcc_lo\n"
"s_cbranch_scc1 L_SLOWB_%=\n"
"s_branch L_BACKB_%=\n"
"L_END%=:\n"
".Lfused_text1:\n"
".purgem MA\n"
".purgem MZ\n"
".purgem TR\n"
    :
    : "{s[96:99]}"(s_text), "{s[56:57]}"(epoch), "{s[8:11]}"(s_wq), "{s[12:15]}"(s_gu), "{s[16:19]}"(s_ws),
      "{s[20:23]}"(srd(kc + kvo + w * 6144, 196608 - w * 6144)), "{s[24:27]}"(srd(vc + kvo + w * 49152, 196608 - w * 49152)),
      "{s[28:31]}"(srd(x, 2621440)), "{s[32:35]}"(srd(out, 2621440)), "{s[36:39]}"(srd(wo, 7864320)), "{s[40:43]}"(srd(wd, 1966080)),
      "{s44}"(m0w), "{s45}"(m0g), "{s46}"(U(16384 * w)), "{s47}"(U(abg + 24576 * h)),
      "{s48}"(U(abg + (192 + 3 * (h >> 1) + 2 * (h & 1)) * 4096)), "{s49}"(U(abg + (193 + 3 * (h >> 1)) * 4096 + 8 * (h & 1))), "{s50}"(U(131072 + 256 * w + (u32)(size_t)smem)),
      "{s52}"(U(WS_XN + G * 81920)), "{s53}"(U(qb)), "{s100}"(U(qb - 2)), "{s101}"(U((qb + 3) & 7)),
      "{s59}"(U(WS_FL + qb * 256)), "{s60}"(U(WS_FA + G * 256 + 8 * h)), "{s61}"(U(WS_FA + (gb & 31) * 256)),
      "{s62}"(U(WS_FB + (tile * 4 + w) * 64 + 8 * c)), "{s63}"(U(WS_FB + u0 * 64)),
      "{s64}"(U(act ? (att ? 0xffu << (8 * c) : 0xffffffffu) : 0u)), "{s66}"(U(0u - ((b + w - 1u) >> 31))),
      "{s68}"(U((b * 4 + w) * 7680)), "{s69}"(U(w == 0 ? b * 7680 : OOB)), "{s70}"(U(qb * 6144)),
      "{s71}"(U(act ? WS_AB + gb * 983040 + 48 * c * 4096 : OOB)),
      "{s[72:75]}"(srd((att ? wo + c * 1966080 : wd) + 256 * nt, act ? 1966080 - 256 * nt : 0)),
      "{s76}"(U(w * 20480)), "{s77}"(U(att ? 24576u : 8192u)), "{s84}"(U(att ? 0u - 172032u : 0u - 57344u)),
      "{s78}"(U(WS_PART + c * ARR + (tile * 4 + w) * 16384)), "{s79}"(U(pl[0])), "{s83}"(U(pl[1])), "{s85}"(U(pl[2])),
      "{s80}"(U(xo[0])), "{s81}"(U(xo[1])), "{s82}"(U(xo[2])),
      "{s54}"(U(0x1f + (u1 - u0) * 0x1f00 + (att << 16) + ((1 - act) << 17) + (rot << 18))),
      "{v192}"((l3 * 12288 + 128 * lr) | ((47u - l) & OOB)), "{v193}"(((8 * hh + l3) * 3072 + gul) | ((23u - l) & OOB)),
      "{v232}"(128 * l), "{v233}"((l3 * 384 + 128 * lr) | ((47u - l) & OOB)), "{v234}"(l0),
      "{v236}"(wqd), "{v237}"(gud0), "{v238}"(gud1), "{v239}"(gud2),
      "{v240}"(vrd[0]), "{v241}"(vrd[1]), "{v242}"(vrd[2]), "{v243}"(vrd[3]),
      "{v244}"(kr0 + 65536), "{v245}"(kr1 + 65536), "{v246}"(kr0 + 114688), "{v247}"(kr1 + 114688),
      "{v248}"(rr * 384 + swz), "{v249}"(16 * l),
      "{v250}"(wqa0), "{v251}"(wqa1), "{v252}"(wqa0), "{v253}"(wqa1), "{v254}"(gua), "{v255}"(gua)
    : "v0", "v1", "v2", "v3", "v4", "v5", "v6", "v7", "v8", "v9", "v10", "v11", "v12", "v13", "v14", "v15", "v16", "v17", "v18", "v19", "v20", "v21", "v22", "v23", "v24", "v25", "v26", "v27", "v28", "v29", "v30", "v31", "v32", "v33", "v34", "v35", "v36", "v37", "v38", "v39", "v40", "v41", "v42", "v43", "v44", "v45", "v46", "v47", "v48", "v49", "v50", "v51", "v52", "v53", "v54", "v55", "v56", "v57", "v58", "v59", "v60", "v61", "v62", "v63", "v64", "v65", "v66", "v67", "v68", "v69", "v70", "v71", "v72", "v73", "v74", "v75", "v76", "v77", "v78", "v79", "v80", "v81", "v82", "v83", "v84", "v85", "v86", "v87", "v88", "v89", "v90", "v91", "v92", "v93", "v94", "v95", "v96", "v97", "v98", "v99", "v100", "v101", "v102", "v103", "v104", "v105", "v106", "v107", "v108", "v109", "v110", "v111", "v112", "v113", "v114", "v115", "v116", "v117", "v118", "v119", "v120", "v121", "v122", "v123", "v124", "v125", "v126", "v127", "v128", "v129", "v130", "v131", "v132", "v133", "v134", "v135", "v136", "v137", "v138", "v139", "v140", "v141", "v142", "v143", "v144", "v145", "v146", "v147", "v148", "v149", "v150", "v151", "v152", "v153", "v154", "v155", "v156", "v157", "v158", "v159", "v160", "v161", "v162", "v163", "v164", "v165", "v166", "v167", "v168", "v169", "v170", "v171", "v172", "v173", "v174", "v175", "v176", "v177", "v178", "v179", "v180", "v181", "v182", "v183", "v184", "v185", "v186", "v187", "v188", "v189", "v190", "v191", "v194", "v195", "v196", "v197", "v198", "v199", "v200", "v201", "v202", "v203", "v204", "v205", "v206", "v207", "v208", "v209", "v210", "v211", "v212", "v213", "v214", "v215", "v216", "v217", "v218", "v219", "v220", "v221", "v222", "v223", "v224", "v225", "v226", "v227", "v228", "v229", "v230", "v231", "v235", "a0", "a1", "a2", "a3", "a4", "a5", "a6", "a7", "a8", "a9", "a10", "a11", "a12", "a13", "a14", "a15", "a16", "a17", "a18", "a19", "a20", "a21", "a22", "a23", "a24", "a25", "a26", "a27", "a28", "a29", "a30", "a31", "a32", "a33", "a34", "a35", "a36", "a37", "a38", "a39", "a40", "a41", "a42", "a43", "a44", "a45", "a46", "a47", "a48", "a49", "a50", "a51", "a52", "a53", "a54", "a55", "a56", "a57", "a58", "a59", "a60", "a61", "a62", "a63", "a64", "a65", "a66", "a67", "a68", "a69", "a70", "a71", "a72", "a73", "a74", "a75", "a76", "a77", "a78", "a79", "a80", "a81", "a82", "a83", "a84", "a85", "a86", "a87", "a88", "a89", "a90", "a91", "a92", "a93", "a94", "a95", "a96", "a97", "a98", "a99", "a100", "a101", "a102", "a103", "a104", "a105", "a106", "a107", "a108", "a109", "a110", "a111", "a112", "a113", "a114", "a115", "a116", "a117", "a118", "a119", "a120", "a121", "a122", "a123", "a124", "a125", "a126", "a127", "a128", "a129", "a130", "a131", "a132", "a133", "a134", "a135", "a136", "a137", "a138", "a139", "a140", "a141", "a142", "a143", "a144", "a145", "a146", "a147", "a148", "a149", "a150", "a151", "a152", "a153", "a154", "a155", "a156", "a157", "a158", "a159", "a160", "a161", "a162", "a163", "a164", "a165", "a166", "a167", "a168", "a169", "a170", "a171", "a172", "a173", "a174", "a175", "a176", "a177", "a178", "a179", "a180", "a181", "a182", "a183", "a184", "a185", "a186", "a187", "a188", "a189", "a190", "a191", "a192", "a193", "a194", "a195", "a196", "a197", "a198", "a199", "a200", "a201", "a202", "a203", "a204", "a205", "a206", "a207", "a208", "a209", "a210", "a211", "a212", "a213", "a214", "a215", "a216", "a217", "a218", "a219", "a220", "a221", "a222", "a223", "a224", "a225", "a226", "a227", "a228", "a229", "a230", "a231", "a232", "a233", "a234", "a235", "a236", "a237", "a238", "a239", "a240", "a241", "a242", "a243", "a244", "a245", "a246", "a247", "a248", "a249", "a250", "a251", "a252", "a253", "a254", "a255", "s51", "s55", "s65", "s86", "s87", "s88", "s89", "s90", "s91", "s92", "s93", "s94", "s95", "s0", "s1", "s2", "s3", "s4", "s5", "s6", "s7", "vcc", "scc", "memory");
}

extern "C" void kernel_launch(void* const* d_in, const int* in_sizes, int n_in, void* d_out, int out_size, void* d_ws, size_t ws_size,
                              hipStream_t stream) {
  fused_block<<<256, 256, 0, stream>>>((const char*)d_in[0], (const char*)d_in[1], (const char*)d_in[2], (const char*)d_in[3],
                                       (const char*)d_in[4], (const char*)d_in[6], (const char*)d_in[7], (const char*)d_in[8], (char*)d_out,
                                       (char*)d_ws);
}
```
